```python
import torch

def _softmax(x, axis=-1):
    return torch.softmax(x, dim=axis)

def _gelu(x):
    return torch.nn.functional.gelu(x, approximate='tanh')

def _silu(x):
    return torch.nn.functional.silu(x)

def _sigmoid(x):
    return torch.sigmoid(x)

def _layernorm(x, axis=-1, eps=1e-05):
    m = x.mean(dim=axis, keepdim=True)
    v = x.var(dim=axis, keepdim=True, unbiased=False)
    return (x - m) / torch.sqrt(v + eps)

def _scatter_add(tgt, idx, src, axis=0):
    return tgt.clone().index_add_(axis, idx, src)

def _segment_sum(src, seg, n):
    out = torch.zeros((n,) + src.shape[1:], dtype=src.dtype)
    return out.index_add_(0, seg, src)

def _pad(x, pw):
    flat = tuple((v for pair in reversed(pw) for v in pair))
    return torch.nn.functional.pad(x, flat)
kernel_name = 'adaLN_cond__memory_aug_recurrent_mem_tr__mixer_gmlp__prompt'

def setup_inputs():

    _w = lambda a: (lambda t: t.to(torch.bfloat16) if t.is_floating_point() else t)(torch.as_tensor(a))

    def _A_setup():
        B, S, Dm = (1, 2048, 576)
        H, Dh = (32, 128)
        M, seg = (8, 32)
        seg = min(seg, S)
        S = S // seg * seg
        return {'x': _w((lambda *s: torch.randn(*s, dtype=torch.bfloat16) * 0.05)(B, S, Dm)), 'mem0': _w((lambda *s: torch.randn(*s, dtype=torch.bfloat16) * 0.4)(B, M, Dm)), 'Wq': _w((lambda *s: torch.randn(*s, dtype=torch.bfloat16) * 3.2)(Dm, H, Dh) * Dm ** (-0.5)), 'Wk': _w((lambda *s: torch.randn(*s, dtype=torch.bfloat16) * 3.2)(Dm, H, Dh) * Dm ** (-0.5)), 'Wv': _w((lambda *s: torch.randn(*s, dtype=torch.bfloat16) * 0.4)(Dm, H, Dh) * Dm ** (-0.5)), 'Wo': _w((lambda *s: torch.randn(*s, dtype=torch.bfloat16) * 0.4)(H, Dh, Dm) * (H * Dh) ** (-0.5)), 'seg_len': seg}
    _A_in = _A_setup()

    def _B_setup():
        B, S, D, F = (1, 2048, 576, 4608)
        Fh = F // 2
        return {'x': _w((lambda *s: torch.randn(*s, dtype=torch.bfloat16) * 0.05)(B, S, D)), 'w_in': _w((lambda *s: torch.randn(*s, dtype=torch.bfloat16) * 0.8)(D, 2 * Fh)), 'w_sp': _w(0.01 * (lambda *s: torch.randn(*s, dtype=torch.bfloat16) * 0.8)(S, S)), 'b_sp': _w(1.0 + 0.01 * (lambda *s: torch.randn(*s, dtype=torch.bfloat16) * 0.05)(S)), 'w_out': _w((lambda *s: torch.randn(*s, dtype=torch.bfloat16) * 0.05)(Fh, D))}
    _B_in = _B_setup()
    return {'A_x': _A_in['x'], 'A_mem0': _A_in['mem0'], 'A_Wq': _A_in['Wq'], 'A_Wk': _A_in['Wk'], 'A_Wv': _A_in['Wv'], 'A_Wo': _A_in['Wo'], 'A_seg_len': _A_in['seg_len'], 'B_x': _B_in['x'], 'B_w_in': _B_in['w_in'], 'B_w_sp': _B_in['w_sp'], 'B_b_sp': _B_in['b_sp'], 'B_w_out': _B_in['w_out'], 'c': _w((lambda *s: torch.randn(*s, dtype=torch.bfloat16) * 0.1)(1, 576) * 8.0), 'w_mod': _w((lambda *s: torch.randn(*s, dtype=torch.bfloat16) * 0.1)(576, 6 * 576) * 576 ** (-0.5) * 8.0)}

def reference(A_x, A_mem0, A_Wq, A_Wk, A_Wv, A_Wo, A_seg_len, B_x, B_w_in, B_w_sp, B_b_sp, B_w_out, c, w_mod):

    def _A_ref(x, mem0, Wq, Wk, Wv, Wo, seg_len):
        B, S, Dm = x.shape
        H, Dh = (Wq.shape[1], Wq.shape[2])
        M = mem0.shape[1]
        scale = Dh ** (-0.5)
        n_seg = S // seg_len
        L = M + seg_len
        idx = torch.arange(L)
        mask = (idx[:, None] < M) | (idx[:, None] >= idx[None, :])
        mem = mem0
        outs = []
        for s in range(n_seg):
            xs = x[:, s * seg_len:(s + 1) * seg_len, :].clone()
            h = torch.concatenate([mem, xs], axis=1)
            q = torch.einsum('btd,dhe->bhte', h, Wq)
            k = torch.einsum('btd,dhe->bhte', h, Wk)
            v = torch.einsum('btd,dhe->bhte', h, Wv)
            scores = torch.einsum('bhqe,bhke->bhqk', q, k) * scale
            scores = torch.where(mask[None, None], scores, -1000000000.0)
            p = _softmax(scores, axis=-1)
            o = torch.einsum('bhqk,bhke->bhqe', p, v)
            o = torch.einsum('bhte,hed->btd', o, Wo)
            mem = o[:, :M, :].clone()
            outs.append(o[:, M:, :])
        return torch.concatenate(outs, axis=1)

    def _B_ref(x, w_in, w_sp, b_sp, w_out):
        B, S, D = x.shape
        F = w_in.shape[-1]
        Fh = F // 2
        z = _gelu(torch.einsum('bsd,df->bsf', x, w_in))
        z1 = z[..., :Fh].clone()
        z2 = _layernorm(z[..., Fh:], axis=-1)
        g = torch.einsum('ts,bsf->btf', w_sp, z2) + b_sp[None, :, None]
        return torch.einsum('bsf,fd->bsd', z1 * g, w_out)
    x = A_x
    mod = (_silu(c) @ w_mod).reshape(c.shape[0], 6, 1, -1)
    sa, ca, ga, sb, cb, gb = (mod[:, 0].clone(), mod[:, 1].clone(), mod[:, 2].clone(), mod[:, 3].clone(), mod[:, 4].clone(), mod[:, 5].clone())
    h = _layernorm(x, axis=-1) * (1 + ca) + sa
    x = x + ga * _A_ref(h, A_mem0, A_Wq, A_Wk, A_Wv, A_Wo, A_seg_len)
    h = _layernorm(x, axis=-1) * (1 + cb) + sb
    x = x + gb * _B_ref(h, B_w_in, B_w_sp, B_b_sp, B_w_out)
    return x
```

```cpp
#include <hip/hip_runtime.h>
#include <hip/hip_bf16.h>
#include <stdint.h>

#define GAS __attribute__((address_space(1)))
#define LAS __attribute__((address_space(3)))
#define DI __device__ __forceinline__
typedef unsigned short u16;
typedef __bf16 bf16x2 __attribute__((ext_vector_type(2)));
typedef __bf16 bf16x4 __attribute__((ext_vector_type(4)));
typedef __bf16 bf16x8 __attribute__((ext_vector_type(8)));
typedef __bf16 bf16x16 __attribute__((ext_vector_type(16)));
typedef short s16x4 __attribute__((ext_vector_type(4)));
typedef float f32x2 __attribute__((ext_vector_type(2)));
typedef float f32x4 __attribute__((ext_vector_type(4)));
typedef float f32x16 __attribute__((ext_vector_type(16)));
typedef unsigned v2u __attribute__((ext_vector_type(2)));
typedef unsigned v4u __attribute__((ext_vector_type(4)));
typedef unsigned v8u __attribute__((ext_vector_type(8)));
typedef GAS unsigned gu32;
#define RLX_AGENT __ATOMIC_RELAXED, __HIP_MEMORY_SCOPE_AGENT

constexpr int S_ = 2048, D_ = 576, H_ = 32, E_ = 128, HE = 4096, NSEG = 64, FF = 4608, FH = 2304;
constexpr int AUX_SC1 = 16;
constexpr int RSRC3 = 0x00020000;

constexpr size_t WS_CTL = 0;
constexpr size_t WS_MEM = 65536;
constexpr size_t MEM_STRIDE = 9216;
constexpr size_t WS_O = WS_MEM + 64 * MEM_STRIDE;
constexpr size_t O_STRIDE = 65536;
constexpr size_t WS_POISON_END = WS_O + 64 * O_STRIDE;
constexpr size_t WS_MOD = WS_POISON_END;
constexpr size_t WS_H1 = WS_MOD + 16384;
constexpr size_t WS_Q = WS_H1 + (size_t)S_ * D_ * 2;
constexpr size_t WS_K = WS_Q + (size_t)S_ * HE * 2;
constexpr size_t WS_VT = WS_K + (size_t)S_ * HE * 2;
constexpr size_t WS_KVM = WS_VT + (size_t)S_ * HE * 2;
constexpr size_t WS_OX = WS_KVM + (size_t)NSEG * H_ * 4096;
constexpr size_t WS_X1 = WS_OX + (size_t)S_ * HE * 2;
constexpr size_t WS_H2 = WS_X1 + (size_t)S_ * D_ * 2;
constexpr size_t WS_Z1 = WS_H2 + (size_t)S_ * D_ * 2;
constexpr size_t WS_Z2 = WS_Z1 + (size_t)S_ * FH * 2;
constexpr size_t WS_U = WS_Z2 + (size_t)S_ * FH * 2;
constexpr size_t WS_END = WS_U + (size_t)S_ * FH * 2;

DI unsigned f2u(float x) { return __builtin_bit_cast(unsigned, x); }
DI float u2f(unsigned x) { return __builtin_bit_cast(float, x); }
DI float bf2f(u16 b) { return __builtin_bit_cast(float, (unsigned)b << 16); }
DI float bflo(unsigned w) { return __builtin_bit_cast(float, w << 16); }
DI float bfhi(unsigned w) { return __builtin_bit_cast(float, w & 0xFFFF0000u); }
DI unsigned pk2(float lo, float hi) { f32x2 v = {lo, hi}; bf16x2 b = __builtin_convertvector(v, bf16x2); return __builtin_bit_cast(unsigned, b); }
DI float rbf(float x) { return bflo(pk2(x, 0.f) & 0xFFFFu); }
DI int lane_id() { return (int)(threadIdx.x & 63); }
DI int wave_id() { return __builtin_amdgcn_readfirstlane((int)(threadIdx.x >> 6)); }

__global__ void __launch_bounds__(256) k_mod(const u16* __restrict__ c, const u16* __restrict__ wmod, float* __restrict__ mod) {
    __shared__ float red[64][33];
    const int tid = threadIdx.x, cg = tid & 3, kr = tid >> 2;
    const int col0 = blockIdx.x * 32 + cg * 8;
    float acc[8];
#pragma unroll
    for (int j = 0; j < 8; ++j) acc[j] = 0.f;
    v4u w[9];
#pragma unroll
    for (int p = 0; p < 9; ++p) w[p] = *(const v4u*)(wmod + (size_t)(kr + 64 * p) * 3456 + col0);
#pragma unroll
    for (int p = 0; p < 9; ++p) {
        const float cv = bf2f(c[kr + 64 * p]);
        const float sv = rbf(cv / (1.f + __expf(-cv)));
        acc[0] += sv * bflo(w[p].x); acc[1] += sv * bfhi(w[p].x); acc[2] += sv * bflo(w[p].y); acc[3] += sv * bfhi(w[p].y);
        acc[4] += sv * bflo(w[p].z); acc[5] += sv * bfhi(w[p].z); acc[6] += sv * bflo(w[p].w); acc[7] += sv * bfhi(w[p].w);
    }
#pragma unroll
    for (int j = 0; j < 8; ++j) red[kr][cg * 8 + j] = acc[j];
    __syncthreads();
    if (tid < 32) {
        float s = 0.f;
        for (int r = 0; r < 64; ++r) s += red[r][tid];
        mod[blockIdx.x * 32 + tid] = rbf(s);
    }
}

DI float wave_sum(float v) {
#pragma unroll
    for (int m = 1; m < 64; m <<= 1) v += __shfl_xor(v, m);
    return v;
}
__global__ void __launch_bounds__(256) k_ln(const u16* __restrict__ x, const float* __restrict__ shift, const float* __restrict__ scale, u16* __restrict__ out) {
    const int row = blockIdx.x * 4 + (threadIdx.x >> 6), lane = threadIdx.x & 63;
    const unsigned* xr = (const unsigned*)(x + (size_t)row * D_);
    unsigned w[5];
#pragma unroll
    for (int i = 0; i < 5; ++i) w[i] = (i < 4 || lane < 32) ? xr[lane + 64 * i] : 0u;
    float s = 0.f;
#pragma unroll
    for (int i = 0; i < 5; ++i) s += bflo(w[i]) + bfhi(w[i]);
    const float mean = wave_sum(s) * (1.f / D_);
    float q = 0.f;
#pragma unroll
    for (int i = 0; i < 5; ++i) if (i < 4 || lane < 32) { const float a = bflo(w[i]) - mean, b = bfhi(w[i]) - mean; q += a * a + b * b; }
    const float rstd = 1.f / sqrtf(wave_sum(q) * (1.f / D_) + 1e-5f);
    unsigned* o = (unsigned*)(out + (size_t)row * D_);
#pragma unroll
    for (int i = 0; i < 5; ++i) if (i < 4 || lane < 32) {
        const int c = 2 * (lane + 64 * i);
        const float a = rbf((bflo(w[i]) - mean) * rstd), b = rbf((bfhi(w[i]) - mean) * rstd);
        const float ha = rbf(a * rbf(1.f + scale[c])) + shift[c], hb = rbf(b * rbf(1.f + scale[c + 1])) + shift[c + 1];
        o[lane + 64 * i] = pk2(ha, hb);
    }
}
__global__ void __launch_bounds__(256) k_ln2304(u16* __restrict__ z) {
    const int row = blockIdx.x * 4 + (threadIdx.x >> 6), lane = threadIdx.x & 63;
    unsigned* zr = (unsigned*)(z + (size_t)row * FH);
    unsigned w[18];
#pragma unroll
    for (int i = 0; i < 18; ++i) w[i] = zr[lane + 64 * i];
    float s = 0.f;
#pragma unroll
    for (int i = 0; i < 18; ++i) s += bflo(w[i]) + bfhi(w[i]);
    const float mean = wave_sum(s) * (1.f / FH);
    float q = 0.f;
#pragma unroll
    for (int i = 0; i < 18; ++i) { const float a = bflo(w[i]) - mean, b = bfhi(w[i]) - mean; q += a * a + b * b; }
    const float rstd = 1.f / sqrtf(wave_sum(q) * (1.f / FH) + 1e-5f);
#pragma unroll
    for (int i = 0; i < 18; ++i) zr[lane + 64 * i] = pk2((bflo(w[i]) - mean) * rstd, (bfhi(w[i]) - mean) * rstd);
}

template <int BM, int BN> struct GemmCfg {
    static constexpr int A_BYTES = BM * 128, B_BYTES = BN * 128, STAGE = A_BYTES + B_BYTES, LDS = 2 * STAGE;
    static constexpr int TM = BM / 64, TN = BN / 64;
};
DI void glds16(const void* g, LAS unsigned char* l) { __builtin_amdgcn_global_load_lds((const GAS unsigned*)g, (LAS unsigned*)l, 16, 0, 0); }

template <int BM, int BN>
DI void gemm_stage(LAS unsigned char* buf, const u16* __restrict__ A, int lda, const u16* __restrict__ B, int ldb, int k0, int wave, int lane) {
#pragma unroll
    for (int p = 0; p < BM / 32; ++p) {
        const int piece = wave + 4 * p, row = piece * 8 + (lane >> 3), c = (lane & 7) ^ ((row >> 1) & 7);
        glds16(A + (size_t)row * lda + k0 + c * 8, buf + piece * 1024 + lane * 16);
    }
#pragma unroll
    for (int p = 0; p < BN / 32; ++p) {
        const int piece = wave + 4 * p, sub = piece >> 3, k = (piece & 7) * 8 + (lane >> 3), c = (lane & 7) ^ (((k >> 1) & 1) << 2);
        glds16(B + (size_t)(k0 + k) * ldb + sub * 64 + c * 8, buf + BM * 128 + piece * 1024 + lane * 16);
    }
}
template <int BM, int BN, bool SWAP>
DI void gemm_tile(LAS unsigned char* lds, const u16* __restrict__ A, int lda, const u16* __restrict__ B, int ldb, int K,
                  f32x16 (&acc)[BM / 64][BN / 64]) {
    using C = GemmCfg<BM, BN>;
    const int lane = lane_id(), wave = wave_id(), wm = wave >> 1, wn = wave & 1;
#pragma unroll
    for (int i = 0; i < C::TM; ++i)
#pragma unroll
        for (int j = 0; j < C::TN; ++j)
#pragma unroll
            for (int r = 0; r < 16; ++r) acc[i][j][r] = 0.f;
    const int nt = K / 64;
    gemm_stage<BM, BN>(lds, A, lda, B, ldb, 0, wave, lane);
    __syncthreads();
    const int r31 = lane & 31, h = lane >> 5;
    int aoff[C::TM];
#pragma unroll
    for (int i = 0; i < C::TM; ++i) { const int row = wm * (BM / 2) + i * 32 + r31; aoff[i] = row * 128; }
    const int asw = (r31 >> 1) & 7;
    const int blk = (lane >> 4) & 1, q = (lane & 15) >> 2, p = lane & 3;
    for (int t = 0; t < nt; ++t) {
        LAS unsigned char* cur = lds + (t & 1) * C::STAGE;
        if (t + 1 < nt) gemm_stage<BM, BN>(lds + ((t + 1) & 1) * C::STAGE, A, lda, B, ldb, (t + 1) * 64, wave, lane);
#pragma unroll
        for (int ks = 0; ks < 4; ++ks) {
            bf16x8 af[C::TM], bfr[C::TN];
#pragma unroll
            for (int i = 0; i < C::TM; ++i) af[i] = *(LAS bf16x8*)(cur + aoff[i] + (((2 * ks + h) ^ asw) << 4));
#pragma unroll
            for (int j = 0; j < C::TN; ++j) {
                const int n = wn * (BN / 2) + j * 32 + 16 * blk + 4 * p;
                const int sub = n >> 6, cn = n & 63;
                s16x4 lo, hi;
                {
                    const int k = 16 * ks + 8 * h + q;
                    const int ch = (cn >> 3) ^ (((k >> 1) & 1) << 2);
                    lo = __builtin_amdgcn_ds_read_tr16_b64_v4i16((LAS s16x4*)(cur + BM * 128 + sub * 8192 + k * 128 + ch * 16 + (cn & 7) * 2));
                }
                {
                    const int k = 16 * ks + 8 * h + 4 + q;
                    const int ch = (cn >> 3) ^ (((k >> 1) & 1) << 2);
                    hi = __builtin_amdgcn_ds_read_tr16_b64_v4i16((LAS s16x4*)(cur + BM * 128 + sub * 8192 + k * 128 + ch * 16 + (cn & 7) * 2));
                }
                typedef short s16x8 __attribute__((ext_vector_type(8)));
                s16x8 v = {lo[0], lo[1], lo[2], lo[3], hi[0], hi[1], hi[2], hi[3]};
                bfr[j] = __builtin_bit_cast(bf16x8, v);
            }
#pragma unroll
            for (int i = 0; i < C::TM; ++i)
#pragma unroll
                for (int j = 0; j < C::TN; ++j)
                    acc[i][j] = SWAP ? __builtin_amdgcn_mfma_f32_32x32x16_bf16(bfr[j], af[i], acc[i][j], 0, 0, 0)
                                     : __builtin_amdgcn_mfma_f32_32x32x16_bf16(af[i], bfr[j], acc[i][j], 0, 0, 0);
        }
        __syncthreads();
    }
}

DI float gelu_tanh(float x) {
    const float u = 0.7978845608028654f * (x + 0.044715f * x * x * x);
    const float e = __expf(2.f * u);
    const float t = 1.f - 2.f / (e + 1.f);
    return 0.5f * x * (1.f + t);
}

__global__ void __launch_bounds__(256, 1) k_qkv(const u16* __restrict__ h1, const u16* __restrict__ Wq, const u16* __restrict__ Wk, const u16* __restrict__ Wv,
                                               u16* __restrict__ Q, u16* __restrict__ Kk, u16* __restrict__ VT) {
    extern __shared__ __attribute__((aligned(16))) unsigned char smem[];
    LAS unsigned char* lds = (LAS unsigned char*)smem;
    const int mt = blockIdx.x & 15, nt = blockIdx.x >> 4;
    const int mat = nt >> 5, head = nt & 31;
    const u16* W = mat == 0 ? Wq : (mat == 1 ? Wk : Wv);
    const int lane = lane_id(), wave = wave_id(), wm = wave >> 1, wn = wave & 1;
    f32x16 acc[2][2];
    if (mat < 2) {
        gemm_tile<128, 128, true>(lds, h1 + (size_t)mt * 128 * D_, D_, W + head * 128, HE, D_, acc);
        u16* O = (mat == 0 ? Q : Kk) + (size_t)head * S_ * E_;
#pragma unroll
        for (int i = 0; i < 2; ++i)
#pragma unroll
            for (int j = 0; j < 2; ++j) {
                const int m = mt * 128 + wm * 64 + i * 32 + (lane & 31);
#pragma unroll
                for (int rg = 0; rg < 4; ++rg) {
                    const int n = wn * 64 + j * 32 + 8 * rg + 4 * (lane >> 5);
                    v2u o = {pk2(acc[i][j][4 * rg], acc[i][j][4 * rg + 1]), pk2(acc[i][j][4 * rg + 2], acc[i][j][4 * rg + 3])};
                    *(v2u*)(O + (size_t)m * E_ + n) = o;
                }
            }
    } else {
        gemm_tile<128, 128, false>(lds, h1 + (size_t)mt * 128 * D_, D_, W + head * 128, HE, D_, acc);
        u16* O = VT + (size_t)head * E_ * S_;
#pragma unroll
        for (int i = 0; i < 2; ++i)
#pragma unroll
            for (int j = 0; j < 2; ++j) {
                const int e = wn * 64 + j * 32 + (lane & 31);
                const int seg0 = mt * 128 + wm * 64 + i * 32;
#pragma unroll
                for (int rg = 0; rg < 4; ++rg) {
                    const int tk = 8 * rg + 4 * (lane >> 5);
                    const int pos = (tk < 16) ? 2 * tk : 2 * (tk - 16) + 4;
                    v2u o = {pk2(acc[i][j][4 * rg], acc[i][j][4 * rg + 1]), pk2(acc[i][j][4 * rg + 2], acc[i][j][4 * rg + 3])};
                    *(v2u*)(O + (size_t)e * S_ + seg0 + pos) = o;
                }
            }
    }
}

__global__ void __launch_bounds__(256, 1) k_wo(const u16* __restrict__ ox, const u16* __restrict__ Wo, const u16* __restrict__ x, const float* __restrict__ ga, u16* __restrict__ x1) {
    extern __shared__ __attribute__((aligned(16))) unsigned char smem[];
    LAS unsigned char* lds = (LAS unsigned char*)smem;
    const int mt = blockIdx.x / 9, nt = blockIdx.x % 9;
    const int lane = lane_id(), wave = wave_id(), wm = wave >> 1, wn = wave & 1;
    f32x16 acc[1][1];
    gemm_tile<64, 64, true>(lds, ox + (size_t)mt * 64 * HE, HE, Wo + nt * 64, D_, HE, acc);
    const int m = mt * 64 + wm * 32 + (lane & 31);
#pragma unroll
    for (int rg = 0; rg < 4; ++rg) {
        const int n = nt * 64 + wn * 32 + 8 * rg + 4 * (lane >> 5);
        const v2u xv = *(const v2u*)(x + (size_t)m * D_ + n);
        float r[4];
        const float xs[4] = {bflo(xv.x), bfhi(xv.x), bflo(xv.y), bfhi(xv.y)};
#pragma unroll
        for (int e = 0; e < 4; ++e) r[e] = xs[e] + rbf(ga[n + e] * rbf(acc[0][0][4 * rg + e]));
        v2u o = {pk2(r[0], r[1]), pk2(r[2], r[3])};
        *(v2u*)(x1 + (size_t)m * D_ + n) = o;
    }
}

__global__ void __launch_bounds__(256, 1) k_win(const u16* __restrict__ h2, const u16* __restrict__ Win, u16* __restrict__ z1, u16* __restrict__ z2) {
    extern __shared__ __attribute__((aligned(16))) unsigned char smem[];
    LAS unsigned char* lds = (LAS unsigned char*)smem;
    const int mt = blockIdx.x & 15, nt = blockIdx.x >> 4;
    const int lane = lane_id(), wave = wave_id(), wm = wave >> 1, wn = wave & 1;
    f32x16 acc[2][2];
    gemm_tile<128, 128, true>(lds, h2 + (size_t)mt * 128 * D_, D_, Win + nt * 128, FF, D_, acc);
    u16* O = nt < 18 ? z1 + nt * 128 : z2 + (nt - 18) * 128;
#pragma unroll
    for (int i = 0; i < 2; ++i)
#pragma unroll
        for (int j = 0; j < 2; ++j) {
            const int m = mt * 128 + wm * 64 + i * 32 + (lane & 31);
#pragma unroll
            for (int rg = 0; rg < 4; ++rg) {
                const int n = wn * 64 + j * 32 + 8 * rg + 4 * (lane >> 5);
                v2u o = {pk2(gelu_tanh(rbf(acc[i][j][4 * rg])), gelu_tanh(rbf(acc[i][j][4 * rg + 1]))), pk2(gelu_tanh(rbf(acc[i][j][4 * rg + 2])), gelu_tanh(rbf(acc[i][j][4 * rg + 3])))};
                *(v2u*)(O + (size_t)m * FH + n) = o;
            }
        }
}

__global__ void __launch_bounds__(256, 1) k_sp(const u16* __restrict__ wsp, const u16* __restrict__ bsp, const u16* __restrict__ z2n, const u16* __restrict__ z1, u16* __restrict__ u) {
    extern __shared__ __attribute__((aligned(16))) unsigned char smem[];
    LAS unsigned char* lds = (LAS unsigned char*)smem;
    const int mt = blockIdx.x & 15, nt = blockIdx.x >> 4;
    const int lane = lane_id(), wave = wave_id(), wm = wave >> 1, wn = wave & 1;
    f32x16 acc[2][2];
    gemm_tile<128, 128, true>(lds, wsp + (size_t)mt * 128 * S_, S_, z2n + nt * 128, FH, S_, acc);
#pragma unroll
    for (int i = 0; i < 2; ++i)
#pragma unroll
        for (int j = 0; j < 2; ++j) {
            const int m = mt * 128 + wm * 64 + i * 32 + (lane & 31);
            const float b = bf2f(bsp[m]);
#pragma unroll
            for (int rg = 0; rg < 4; ++rg) {
                const int n = nt * 128 + wn * 64 + j * 32 + 8 * rg + 4 * (lane >> 5);
                const v2u zv = *(const v2u*)(z1 + (size_t)m * FH + n);
                const float zs[4] = {bflo(zv.x), bfhi(zv.x), bflo(zv.y), bfhi(zv.y)};
                float r[4];
#pragma unroll
                for (int e = 0; e < 4; ++e) r[e] = zs[e] * rbf(rbf(acc[i][j][4 * rg + e]) + b);
                v2u o = {pk2(r[0], r[1]), pk2(r[2], r[3])};
                *(v2u*)(u + (size_t)m * FH + n) = o;
            }
        }
}

__global__ void __launch_bounds__(256, 1) k_out(const u16* __restrict__ u, const u16* __restrict__ Wout, const u16* __restrict__ x1, const float* __restrict__ gb, u16* __restrict__ out) {
    extern __shared__ __attribute__((aligned(16))) unsigned char smem[];
    LAS unsigned char* lds = (LAS unsigned char*)smem;
    const int mt = blockIdx.x / 9, nt = blockIdx.x % 9;
    const int lane = lane_id(), wave = wave_id(), wm = wave >> 1, wn = wave & 1;
    f32x16 acc[1][1];
    gemm_tile<64, 64, true>(lds, u + (size_t)mt * 64 * FH, FH, Wout + nt * 64, D_, FH, acc);
    const int m = mt * 64 + wm * 32 + (lane & 31);
#pragma unroll
    for (int rg = 0; rg < 4; ++rg) {
        const int n = nt * 64 + wn * 32 + 8 * rg + 4 * (lane >> 5);
        const v2u xv = *(const v2u*)(x1 + (size_t)m * D_ + n);
        float r[4];
        const float xs[4] = {bflo(xv.x), bfhi(xv.x), bflo(xv.y), bfhi(xv.y)};
#pragma unroll
        for (int e = 0; e < 4; ++e) r[e] = xs[e] + rbf(gb[n + e] * rbf(acc[0][0][4 * rg + e]));
        v2u o = {pk2(r[0], r[1]), pk2(r[2], r[3])};
        *(v2u*)(out + (size_t)m * D_ + n) = o;
    }
}

DI f32x4 mfma16(bf16x8 a, bf16x8 b, f32x4 c) { return __builtin_amdgcn_mfma_f32_16x16x32_bf16(a, b, c, 0, 0, 0); }
typedef short s16x4v __attribute__((ext_vector_type(4)));
DI f32x4 mfma16k16(v2u a, v2u b, f32x4 c) { return __builtin_amdgcn_mfma_f32_16x16x16bf16_1k(__builtin_bit_cast(s16x4v, a), __builtin_bit_cast(s16x4v, b), c, 0, 0, 0); }
DI bf16x8 tr2(LAS unsigned char* a0, LAS unsigned char* a1) {
    typedef short s16x8 __attribute__((ext_vector_type(8)));
    const s16x4 lo = __builtin_amdgcn_ds_read_tr16_b64_v4i16((LAS s16x4*)a0), hi = __builtin_amdgcn_ds_read_tr16_b64_v4i16((LAS s16x4*)a1);
    s16x8 v = {lo[0], lo[1], lo[2], lo[3], hi[0], hi[1], hi[2], hi[3]};
    return __builtin_bit_cast(bf16x8, v);
}
DI float xmax16_32(float m) {
    unsigned u = f2u(m);
    auto a = __builtin_amdgcn_permlane16_swap(u, u, false, false);
    m = fmaxf(u2f(a[0]), u2f(a[1]));
    u = f2u(m);
    auto b = __builtin_amdgcn_permlane32_swap(u, u, false, false);
    return fmaxf(u2f(b[0]), u2f(b[1]));
}
DI float xsum16_32(float m) {
    unsigned u = f2u(m);
    auto a = __builtin_amdgcn_permlane16_swap(u, u, false, false);
    m = u2f(a[0]) + u2f(a[1]);
    u = f2u(m);
    auto b = __builtin_amdgcn_permlane32_swap(u, u, false, false);
    return u2f(b[0]) + u2f(b[1]);
}
constexpr float SCL = 0.08838834764831845f * 1.4426950408889634f;

DI void xattn_unit(int seg, int head, const u16* __restrict__ Q, const u16* __restrict__ Kk, const u16* __restrict__ VT, const unsigned char* __restrict__ kvm,
                   u16* __restrict__ ox, LAS unsigned char* wl  ) {
    const int lane = lane_id(), c = lane & 15, g = lane >> 4;
    const size_t tok0 = (size_t)head * S_ + seg * 32;
    const unsigned char* km = kvm + ((size_t)seg * H_ + head) * 4096;
    {
        const v4u a = *(const v4u*)(km + lane * 16), b = *(const v4u*)(km + 1024 + lane * 16);
        *(LAS v4u*)(wl + lane * 16) = a; *(LAS v4u*)(wl + 1024 + lane * 16) = b;
    }
    bf16x8 qf[2][4], kf[2][4];
#pragma unroll
    for (int t = 0; t < 2; ++t)
#pragma unroll
        for (int ks = 0; ks < 4; ++ks) {
            qf[t][ks] = *(const bf16x8*)(Q + (tok0 + 16 * t + c) * E_ + 32 * ks + 8 * g);
            kf[t][ks] = *(const bf16x8*)(Kk + (tok0 + 16 * t + c) * E_ + 32 * ks + 8 * g);
        }
    __builtin_amdgcn_s_waitcnt(0xC07F);
    __builtin_amdgcn_wave_barrier();
    const int q = c >> 2, p = c & 3;
    f32x4 st[2][3];
#pragma unroll
    for (int t = 0; t < 2; ++t)
#pragma unroll
        for (int kt = 0; kt < 3; ++kt) st[t][kt] = f32x4{0, 0, 0, 0};
#pragma unroll
    for (int ks = 0; ks < 4; ++ks) {
        const int e0 = 32 * ks + 8 * g + q;
        const bf16x8 km8 = tr2(wl + e0 * 16 + (p & 1) * 8, wl + (e0 + 4) * 16 + (p & 1) * 8);
#pragma unroll
        for (int t = 0; t < 2; ++t) {
            st[t][0] = mfma16(km8, qf[t][ks], st[t][0]);
            st[t][1] = mfma16(kf[0][ks], qf[t][ks], st[t][1]);
            st[t][2] = mfma16(kf[1][ks], qf[t][ks], st[t][2]);
        }
    }
    bf16x8 pf[2]; v2u pm[2]; float rz[2];
#pragma unroll
    for (int t = 0; t < 2; ++t) {
        const int qi = 16 * t + c;
        float s[12];
#pragma unroll
        for (int r = 0; r < 4; ++r) {
            s[r] = g < 2 ? st[t][0][r] * SCL : -INFINITY;
            s[4 + r] = (4 * g + r) <= qi ? st[t][1][r] * SCL : -INFINITY;
            s[8 + r] = (16 + 4 * g + r) <= qi ? st[t][2][r] * SCL : -INFINITY;
        }
        float m = s[0];
#pragma unroll
        for (int r = 1; r < 12; ++r) m = fmaxf(m, s[r]);
        m = xmax16_32(m);
        float z = 0.f;
#pragma unroll
        for (int r = 0; r < 12; ++r) { s[r] = __builtin_amdgcn_exp2f(s[r] - m); z += s[r]; }
        z = xsum16_32(z);
        rz[t] = 1.f / z;
        pm[t] = v2u{pk2(s[0], s[1]), pk2(s[2], s[3])};
        v4u pp = {pk2(s[4], s[5]), pk2(s[6], s[7]), pk2(s[8], s[9]), pk2(s[10], s[11])};
        pf[t] = __builtin_bit_cast(bf16x8, pp);
    }
    const unsigned char* vm = km + 2048;
#pragma unroll
    for (int ep = 0; ep < 4; ++ep) {
        f32x4 o[2][2];
#pragma unroll
        for (int et = 0; et < 2; ++et) {
            const int e = 32 * ep + 16 * et + c;
            const bf16x8 vf = *(const bf16x8*)(VT + ((size_t)head * E_ + e) * S_ + seg * 32 + 8 * g);
            const v2u vmf = *(const v2u*)(vm + e * 16 + (g & 1) * 8);
#pragma unroll
            for (int t = 0; t < 2; ++t) {
                o[et][t] = mfma16(vf, pf[t], f32x4{0, 0, 0, 0});
                o[et][t] = mfma16k16(vmf, pm[t], o[et][t]);
            }
        }
#pragma unroll
        for (int t = 0; t < 2; ++t) {
            unsigned d0 = pk2(o[0][t][0] * rz[t], o[0][t][1] * rz[t]), d1 = pk2(o[0][t][2] * rz[t], o[0][t][3] * rz[t]);
            unsigned e0 = pk2(o[1][t][0] * rz[t], o[1][t][1] * rz[t]), e1 = pk2(o[1][t][2] * rz[t], o[1][t][3] * rz[t]);
            auto s0 = __builtin_amdgcn_permlane16_swap(d0, e0, false, false);
            auto s1 = __builtin_amdgcn_permlane16_swap(d1, e1, false, false);
            v4u val = {s0[0], s1[0], s0[1], s1[1]};
            const int eo = 32 * ep + ((g & 1) << 4) + ((g >> 1) << 3);
            *(v4u*)(ox + (size_t)(seg * 32 + 16 * t + c) * HE + head * E_ + eo) = val;
        }
    }
}
__global__ void __launch_bounds__(256) k_xattn(const u16* __restrict__ Q, const u16* __restrict__ Kk, const u16* __restrict__ VT, const unsigned char* __restrict__ kvm, u16* __restrict__ ox) {
    __shared__ __attribute__((aligned(16))) unsigned char sm[4 * 2048];
    const int wave = wave_id();
    const int unit = blockIdx.x * 4 + wave;
    xattn_unit(unit >> 5, unit & 31, Q, Kk, VT, kvm, ox, (LAS unsigned char*)sm + wave * 2048);
}

constexpr unsigned SPIN_CAP = 1u << 20;
DI f32x4 smf(bf16x8 a, bf16x16 b, f32x4 c, int idx) { return __builtin_amdgcn_smfmac_f32_16x16x64_bf16(a, b, c, idx, 0, 0); }
DI unsigned nosent(unsigned x) { return x < 0xFFFFFFFEu ? x : 0xFFFFFFFEu; }
DI bool all_valid(v4u v) { return (int)(v.x != ~0u) & (int)(v.y != ~0u) & (int)(v.z != ~0u) & (int)(v.w != ~0u); }
#define CB() asm volatile("" ::: "memory")

struct ChainArgs {
    const u16* mem0; const u16* Wq; const u16* Wk; const u16* Wv; const u16* Wo;
    unsigned char* ws;
};

constexpr int HL_MEM = 0;
constexpr int HL_RHO = 4640;
constexpr int HL_QK = 9280;
constexpr int HL_S = HL_QK + 4096;
constexpr int HL_WV = HL_S + 6144;
constexpr int HL_STAGE = HL_WV + 4 * 9 * 2048;
constexpr int HL_END = HL_STAGE + 4 * 16384;

DI int wsw(int k) { return ((k & 1) | (((k >> 2) & 1) << 1) | (((k >> 3) & 1) << 2)) << 1; }
DI void stage_w(LAS unsigned char* dst, const u16* __restrict__ src, int ld, int wave, int lane) {
#pragma unroll
    for (int p = 0; p < 4; ++p) {
        const int piece = wave + 4 * p, k = piece * 4 + (lane >> 4), ch = (lane & 15) ^ wsw(k);
        glds16(src + (size_t)k * ld + ch * 8, dst + piece * 1024 + lane * 16);
    }
}
DI bf16x16 read_wfrag(LAS unsigned char* st, int col, int lane) {
    const int gb = lane >> 4, q = (lane & 15) >> 2, p = lane & 3;
    s16x4 r[4];
#pragma unroll
    for (int m = 0; m < 4; ++m) {
        const int d0 = 32 * (m >> 1) + 16 * (gb >> 1) + 4 * (gb & 1) + 2 * (m & 1);
        const int k = d0 + (q & 1) + 8 * (q >> 1);
        const int cb = (col + 4 * p) * 2;
        const int ch = (cb >> 4) ^ wsw(k);
        r[m] = __builtin_amdgcn_ds_read_tr16_b64_v4i16((LAS s16x4*)(st + k * 256 + ch * 16 + (cb & 15)));
    }
    typedef short s16x16 __attribute__((ext_vector_type(16)));
    s16x16 v = {r[0][0], r[0][1], r[0][2], r[0][3], r[1][0], r[1][1], r[1][2], r[1][3], r[2][0], r[2][1], r[2][2], r[2][3], r[3][0], r[3][1], r[3][2], r[3][3]};
    return __builtin_bit_cast(bf16x16, v);
}

DI void head_role(const ChainArgs& a, int h, LAS unsigned char* lds) {
    const int lane = lane_id(), w = wave_id(), c = lane & 15, g = lane >> 4;
    unsigned char* ws = a.ws;
    gu32* ctl = (gu32*)(ws + WS_CTL);
    const __amdgpu_buffer_rsrc_t rX = __builtin_amdgcn_make_buffer_rsrc((void*)(ws + WS_MEM), 0, (int)(WS_POISON_END - WS_MEM), RSRC3);
    const __amdgpu_buffer_rsrc_t rKVM = __builtin_amdgcn_make_buffer_rsrc((void*)(ws + WS_KVM), 0, NSEG * H_ * 4096, RSRC3);
    bf16x16 W[5][9];
    LAS unsigned char* wv1 = lds + HL_WV + w * 9 * 2048 + lane * 16;
    {
        LAS unsigned char* st = lds + HL_STAGE;
        const u16* src[3] = {a.Wq + h * 128, a.Wk + h * 128, a.Wv + h * 128};
#pragma unroll
        for (int i = 0; i < 3; ++i) stage_w(st + i * 16384, src[0] + (size_t)i * 64 * HE, HE, w, lane);
#pragma unroll
        for (int n = 0; n < 27; ++n) {
            if (n + 3 < 27) stage_w(st + ((n + 3) & 3) * 16384, src[(n + 3) / 9] + (size_t)((n + 3) % 9) * 64 * HE, HE, w, lane);
            if (n + 3 < 27) asm volatile("s_waitcnt vmcnt(12)" ::: "memory");
            else if (n + 2 < 27) asm volatile("s_waitcnt vmcnt(8)" ::: "memory");
            else if (n + 1 < 27) asm volatile("s_waitcnt vmcnt(4)" ::: "memory");
            else asm volatile("s_waitcnt vmcnt(0)" ::: "memory");
            __builtin_amdgcn_s_barrier();
            W[2 * (n / 9)][n % 9] = read_wfrag(st + (n & 3) * 16384, 32 * w, lane);
            if (n < 18) W[2 * (n / 9) + 1][n % 9] = read_wfrag(st + (n & 3) * 16384, 32 * w + 16, lane);
            else {
                const v8u t = __builtin_bit_cast(v8u, read_wfrag(st + (n & 3) * 16384, 32 * w + 16, lane));
                *(LAS v4u*)(wv1 + (n - 18) * 2048) = v4u{t[0], t[1], t[2], t[3]};
                *(LAS v4u*)(wv1 + (n - 18) * 2048 + 1024) = v4u{t[4], t[5], t[6], t[7]};
            }
            asm volatile("s_waitcnt lgkmcnt(0)" ::: "memory");
            __builtin_amdgcn_s_barrier();
        }
    }
    const int idx = (lane & 8) ? 0xEEEE : 0x4444;
    const u16* Kx = (const u16*)(ws + WS_K) + (size_t)h * S_ * E_;
    const u16* VTx = (const u16*)(ws + WS_VT) + (size_t)h * E_ * S_;
    const int q4 = c >> 2, p4 = c & 3;
    const int amem = HL_MEM + (p4 & 1) * HL_RHO + (16 * g + 8 * (p4 >> 1) + q4) * 8;
    LAS unsigned char* wq = lds + HL_QK + w * 1024;
    bool fail = false;
    for (int s = 0; s < NSEG; ++s) {
        bf16x8 kx[2], vx[2];
#pragma unroll
        for (int t = 0; t < 2; ++t) {
            kx[t] = *(const bf16x8*)(Kx + (size_t)(s * 32 + 16 * t + c) * E_ + 32 * w + 8 * g);
            vx[t] = *(const bf16x8*)(VTx + (size_t)(32 * w + 16 * t + c) * S_ + s * 32 + 8 * g);
        }
        if (s == 0) {
            for (int i = threadIdx.x; i < 8 * D_; i += 256) { const int r = i / D_, d = i % D_; *(LAS u16*)(lds + HL_MEM + (r >> 2) * HL_RHO + d * 8 + (r & 3) * 2) = a.mem0[i]; }
        } else {
            const int base = s * (int)MEM_STRIDE + lane * 16;
            const int o0 = base + w * 1024, o1 = base + (w + 4) * 1024, o2 = w == 0 ? base + 8192 : 0x7FFFFF00;
            v4u v0, v1, v2; unsigned spins = 0;
            while (true) {
                CB();
                v0 = __builtin_amdgcn_raw_buffer_load_b128(rX, o0, 0, AUX_SC1);
                v1 = __builtin_amdgcn_raw_buffer_load_b128(rX, o1, 0, AUX_SC1);
                v2 = __builtin_amdgcn_raw_buffer_load_b128(rX, o2, 0, AUX_SC1);
                if (__all((int)all_valid(v0) & (int)all_valid(v1) & (int)all_valid(v2))) break;
                __builtin_amdgcn_s_sleep(1);
                if (++spins > SPIN_CAP) { fail = true; break; }
            }
            const int b0 = w * 1024 + lane * 16, b1 = b0 + 4096, b2 = 8192 + lane * 16;
            *(LAS v4u*)(lds + HL_MEM + b0 + (b0 >= 4608 ? 32 : 0)) = v0;
            *(LAS v4u*)(lds + HL_MEM + b1 + (b1 >= 4608 ? 32 : 0)) = v1;
            if (w == 0) *(LAS v4u*)(lds + HL_MEM + b2 + 32) = v2;
        }
        __syncthreads();
        f32x4 acc[6];
#pragma unroll
        for (int T = 0; T < 6; ++T) acc[T] = f32x4{0, 0, 0, 0};
#pragma unroll
        for (int f = 0; f < 9; ++f) {
            const bf16x8 af = tr2(lds + amem + f * 512, lds + amem + f * 512 + 32);
#pragma unroll
            for (int T = 0; T < 5; ++T) acc[T] = smf(af, W[T][f], acc[T], idx);
            {
                const v4u lo = *(LAS v4u*)(wv1 + f * 2048), hi = *(LAS v4u*)(wv1 + f * 2048 + 1024);
                const v8u t = {lo[0], lo[1], lo[2], lo[3], hi[0], hi[1], hi[2], hi[3]};
                acc[5] = smf(af, __builtin_bit_cast(bf16x16, t), acc[5], idx);
            }
        }
        unsigned pq[2], pk[2], pv[2];
        {
            float x[3][4];
#pragma unroll
            for (int m = 0; m < 3; ++m)
#pragma unroll
                for (int r = 0; r < 4; ++r) {
                    auto sw = __builtin_amdgcn_permlane32_swap(f2u(acc[2 * m][r]), f2u(acc[2 * m + 1][r]), false, false);
                    x[m][r] = u2f(sw[0]) + u2f(sw[1]);
                }
            pq[0] = pk2(x[0][0], x[0][1]); pq[1] = pk2(x[0][2], x[0][3]);
            pk[0] = pk2(x[1][0], x[1][1]); pk[1] = pk2(x[1][2], x[1][3]);
            pv[0] = pk2(x[2][0], x[2][1]); pv[1] = pk2(x[2][2], x[2][3]);
        }
        {
            auto s0 = __builtin_amdgcn_permlane16_swap(pk[0], pv[0], false, false);
            auto s1 = __builtin_amdgcn_permlane16_swap(pk[1], pv[1], false, false);
            v4u val = {s0[0], s1[0], s0[1], s1[1]};
            const int off = (s * H_ + h) * 4096 + (g & 1) * 2048 + (32 * w + 16 * (g >> 1) + c) * 16;
            __builtin_amdgcn_raw_buffer_store_b128(val, rKVM, off, 0, AUX_SC1);
        }
        if (s == NSEG - 1) break;
        {
            const int el = 16 * (lane >> 5) + c;
            *(LAS v2u*)(wq + el * 16 + (g & 1) * 8) = v2u{pq[0], pq[1]};
            *(LAS v2u*)(wq + 512 + el * 16 + (g & 1) * 8) = v2u{pk[0], pk[1]};
        }
        __builtin_amdgcn_s_waitcnt(0xC07F);
        __builtin_amdgcn_wave_barrier();
        const int e0 = 8 * g + q4;
        const bf16x8 qf = tr2(wq + e0 * 16 + (p4 & 1) * 8, wq + (e0 + 4) * 16 + (p4 & 1) * 8);
        const bf16x8 kmf = tr2(wq + 512 + e0 * 16 + (p4 & 1) * 8, wq + 512 + (e0 + 4) * 16 + (p4 & 1) * 8);
        const f32x4 z4 = {0, 0, 0, 0};
        f32x4 sp[3];
        sp[0] = mfma16(kmf, qf, z4); sp[1] = mfma16(kx[0], qf, z4); sp[2] = mfma16(kx[1], qf, z4);
        if (c < 8) {
#pragma unroll
            for (int t = 0; t < 3; ++t) *(LAS f32x4*)(lds + HL_S + ((w * 3 + t) * 32 + g * 8 + c) * 16) = sp[t];
        }
        __syncthreads();
        float sc[12];
        {
            f32x4 tot[3];
#pragma unroll
            for (int t = 0; t < 3; ++t) {
                tot[t] = *(LAS f32x4*)(lds + HL_S + ((0 * 3 + t) * 32 + g * 8 + (c & 7)) * 16);
#pragma unroll
                for (int ww = 1; ww < 4; ++ww) tot[t] += *(LAS f32x4*)(lds + HL_S + ((ww * 3 + t) * 32 + g * 8 + (c & 7)) * 16);
            }
#pragma unroll
            for (int r = 0; r < 4; ++r) { sc[r] = g < 2 ? tot[0][r] * SCL : -INFINITY; sc[4 + r] = tot[1][r] * SCL; sc[8 + r] = tot[2][r] * SCL; }
        }
        float m = sc[0];
#pragma unroll
        for (int r = 1; r < 12; ++r) m = fmaxf(m, sc[r]);
        m = xmax16_32(m);
        float z = 0.f;
#pragma unroll
        for (int r = 0; r < 12; ++r) { sc[r] = __builtin_amdgcn_exp2f(sc[r] - m); z += sc[r]; }
        z = xsum16_32(z);
        const float rz = 1.f / z;
        const v2u pm = {pk2(sc[0], sc[1]), pk2(sc[2], sc[3])};
        const v4u pp = {pk2(sc[4], sc[5]), pk2(sc[6], sc[7]), pk2(sc[8], sc[9]), pk2(sc[10], sc[11])};
        const bf16x8 pf = __builtin_bit_cast(bf16x8, pp);
        v2u vm0 = {pv[0], pv[1]}, vm1;
        {
            auto u0 = __builtin_amdgcn_permlane32_swap(pv[0], pv[0], false, false);
            auto u1 = __builtin_amdgcn_permlane32_swap(pv[1], pv[1], false, false);
            vm1 = v2u{u0[1], u1[1]};
        }
        f32x4 o0 = mfma16(vx[0], pf, z4), o1 = mfma16(vx[1], pf, z4);
        o0 = mfma16k16(vm0, pm, o0); o1 = mfma16k16(vm1, pm, o1);
        {
            unsigned d0 = pk2(o0[0] * rz, o0[1] * rz), d1 = pk2(o0[2] * rz, o0[3] * rz);
            unsigned e0_ = pk2(o1[0] * rz, o1[1] * rz), e1 = pk2(o1[2] * rz, o1[3] * rz);
            auto s0 = __builtin_amdgcn_permlane16_swap(d0, e0_, false, false);
            auto s1 = __builtin_amdgcn_permlane16_swap(d1, e1, false, false);
            v4u val = {nosent(s0[0]), nosent(s1[0]), nosent(s0[1]), nosent(s1[1])};
            const int el = 32 * w + ((g & 1) << 4) + ((g >> 1) << 3);
            const int win = el >> 6, gg = (el >> 4) & 3, hf = (el >> 3) & 1;
            const int r = 8 * win + 4 * hf + (c & 3);
            const int off = (int)(WS_O - WS_MEM) + s * (int)O_STRIDE + (h * 2 + ((c >> 2) & 1)) * 1024 + (16 * gg + r) * 16;
            if (c < 8) __builtin_amdgcn_raw_buffer_store_b128(val, rX, off, 0, AUX_SC1);
        }
    }
    if (fail && threadIdx.x == 0) __hip_atomic_store(ctl, 1u, RLX_AGENT);
}

constexpr int YL_RED = 0;
constexpr int YL_STAGE = 4096;
DI void y_role(const ChainArgs& a, int tp, int rho, LAS unsigned char* lds) {
    const int lane = lane_id(), w = wave_id(), c = lane & 15, g = lane >> 4;
    unsigned char* ws = a.ws;
    gu32* ctl = (gu32*)(ws + WS_CTL);
    const __amdgpu_buffer_rsrc_t rX = __builtin_amdgcn_make_buffer_rsrc((void*)(ws + WS_MEM), 0, (int)(WS_POISON_END - WS_MEM), RSRC3);
    bf16x16 W[16][2];
    {
        LAS unsigned char* st = lds + YL_STAGE + w * 16384;
        const u16* src = a.Wo + (size_t)(1024 * w) * D_ + 32 * tp;
        auto stage = [&](int win, int slot) {
#pragma unroll
            for (int p = 0; p < 4; ++p) {
                const int k = 16 * p + (lane >> 2);
                const int ch = (lane & 3) ^ 0;
                glds16(src + (size_t)(64 * win + k) * D_ + ch * 8, st + slot * 4096 + p * 1024 + lane * 16);
            }
        };
#pragma unroll
        for (int i = 0; i < 3; ++i) stage(i, i);
        const int gb = lane >> 4, q = (lane & 15) >> 2, p = lane & 3;
#pragma unroll
        for (int n = 0; n < 16; ++n) {
            if (n + 3 < 16) stage(n + 3, (n + 3) & 3);
            if (n + 3 < 16) asm volatile("s_waitcnt vmcnt(12)" ::: "memory");
            else if (n + 2 < 16) asm volatile("s_waitcnt vmcnt(8)" ::: "memory");
            else if (n + 1 < 16) asm volatile("s_waitcnt vmcnt(4)" ::: "memory");
            else asm volatile("s_waitcnt vmcnt(0)" ::: "memory");
#pragma unroll
            for (int T = 0; T < 2; ++T) {
                s16x4 r[4];
#pragma unroll
                for (int m = 0; m < 4; ++m) {
                    const int d0 = 32 * (m >> 1) + 16 * (gb >> 1) + 4 * (gb & 1) + 2 * (m & 1);
                    const int k = d0 + (q & 1) + 8 * (q >> 1);
                    r[m] = __builtin_amdgcn_ds_read_tr16_b64_v4i16((LAS s16x4*)(st + (n & 3) * 4096 + k * 64 + (16 * T + 4 * p) * 2));
                }
                typedef short s16x16 __attribute__((ext_vector_type(16)));
                s16x16 v = {r[0][0], r[0][1], r[0][2], r[0][3], r[1][0], r[1][1], r[1][2], r[1][3], r[2][0], r[2][1], r[2][2], r[2][3], r[3][0], r[3][1], r[3][2], r[3][3]};
                W[n][T] = __builtin_bit_cast(bf16x16, v);
            }
            asm volatile("s_waitcnt lgkmcnt(0)" ::: "memory");
        }
    }
    const int idx = (lane & 4) ? 0xEEEE : 0x4444;
    bool fail = false;
    for (int s = 0; s < NSEG - 1; ++s) {
        const int base = (int)(WS_O - WS_MEM) + s * (int)O_STRIDE + (16 * w + rho) * 1024 + lane * 16;
        v4u v[8]; unsigned spins = 0;
        while (true) {
            CB();
#pragma unroll
            for (int i = 0; i < 8; ++i) v[i] = __builtin_amdgcn_raw_buffer_load_b128(rX, base + i * 2048, 0, AUX_SC1);
            int ok = 1;
#pragma unroll
            for (int i = 0; i < 8; ++i) ok &= (int)all_valid(v[i]);
            if (__all(ok)) break;
            __builtin_amdgcn_s_sleep(1);
            if (++spins > SPIN_CAP) { fail = true; break; }
        }
        f32x4 D1[2] = {f32x4{0, 0, 0, 0}, f32x4{0, 0, 0, 0}}, D2[2] = {f32x4{0, 0, 0, 0}, f32x4{0, 0, 0, 0}};
#pragma unroll
        for (int i = 0; i < 8; ++i) {
            const bf16x8 af = __builtin_bit_cast(bf16x8, v[i]);
#pragma unroll
            for (int T = 0; T < 2; ++T) { D1[T] = smf(af, W[2 * i][T], D1[T], idx); D2[T] = smf(af, W[2 * i + 1][T], D2[T], idx); }
        }
        float y[4];
#pragma unroll
        for (int r = 0; r < 4; ++r) {
            const float x0 = g < 2 ? D1[0][r] : D2[0][r], x1 = g < 2 ? D1[1][r] : D2[1][r];
            auto sw = __builtin_amdgcn_permlane32_swap(f2u(x0), f2u(x1), false, false);
            y[r] = u2f(sw[0]) + u2f(sw[1]);
        }
        float za, zb;
        {
            auto s0 = __builtin_amdgcn_permlane16_swap(f2u(y[0]), f2u(y[1]), false, false);
            za = u2f(s0[0]) + u2f(s0[1]);
            auto s1 = __builtin_amdgcn_permlane16_swap(f2u(y[2]), f2u(y[3]), false, false);
            zb = u2f(s1[0]) + u2f(s1[1]);
        }
        *(LAS f32x2*)(lds + YL_RED + (w * 64 + lane) * 8) = f32x2{za, zb};
        __syncthreads();
        if (w == 0) {
            f32x2 t = *(LAS f32x2*)(lds + YL_RED + lane * 8) + *(LAS f32x2*)(lds + YL_RED + (64 + lane) * 8) + *(LAS f32x2*)(lds + YL_RED + (128 + lane) * 8) + *(LAS f32x2*)(lds + YL_RED + (192 + lane) * 8);
            const unsigned ua = f2u(t.x), ub = f2u(t.y);
            auto sa = __builtin_amdgcn_permlane16_swap(ua, ua, false, false);
            auto sb = __builtin_amdgcn_permlane16_swap(ub, ub, false, false);
            const unsigned d0 = nosent(pk2(u2f(sa[0]), u2f(sa[1])));
            const unsigned d1 = nosent(pk2(u2f(sb[0]), u2f(sb[1])));
            const unsigned n0 = __builtin_amdgcn_update_dpp(0u, d0, 0x101, 0xF, 0xF, true), n1 = __builtin_amdgcn_update_dpp(0u, d1, 0x101, 0xF, 0xF, true);
            v4u val = {d0, d1, n0, n1};
            const int col = 32 * tp + 16 * (g >> 1) + c;
            if (((c & 1) == 0) && ((g & 1) == 0)) __builtin_amdgcn_raw_buffer_store_b128(val, rX, (s + 1) * (int)MEM_STRIDE + rho * 4608 + col * 8, 0, AUX_SC1);
        }
        __syncthreads();
    }
    if (fail && threadIdx.x == 0) __hip_atomic_store(ctl, 1u, RLX_AGENT);
}

constexpr int CHAIN_LDS = 160 * 1024;
static_assert(HL_END <= CHAIN_LDS, "lds");
__global__ void __launch_bounds__(256, 1) k_chain(ChainArgs a) {
    extern __shared__ __attribute__((aligned(16))) unsigned char smem[];
    LAS unsigned char* lds = (LAS unsigned char*)smem;
    const int b = blockIdx.x;
    if (b < 32) head_role(a, b, lds);
    else if (b < 68) y_role(a, (b - 32) >> 1, (b - 32) & 1, lds);
}

extern "C" void kernel_launch(void* const* d_in, const int* in_sizes, int n_in, void* d_out, int out_size, void* d_ws, size_t ws_size, hipStream_t stream) {
    const u16* x = (const u16*)d_in[0];
    const u16* mem0 = (const u16*)d_in[1];
    const u16* Wq = (const u16*)d_in[2];
    const u16* Wk = (const u16*)d_in[3];
    const u16* Wv = (const u16*)d_in[4];
    const u16* Wo = (const u16*)d_in[5];
    const u16* Win = (const u16*)d_in[8];
    const u16* Wsp = (const u16*)d_in[9];
    const u16* bsp = (const u16*)d_in[10];
    const u16* Wout = (const u16*)d_in[11];
    const u16* c = (const u16*)d_in[12];
    const u16* wmod = (const u16*)d_in[13];
    unsigned char* ws = (unsigned char*)d_ws;
    float* mod = (float*)(ws + WS_MOD);
    (void)hipMemsetAsync(ws + WS_CTL, 0, 65536, stream);
    (void)hipMemsetAsync(ws + WS_MEM, 0xFF, WS_POISON_END - WS_MEM, stream);
    k_mod<<<108, 256, 0, stream>>>(c, wmod, mod);
    k_ln<<<512, 256, 0, stream>>>(x, mod + 0 * D_, mod + 1 * D_, (u16*)(ws + WS_H1));
    constexpr int L128 = GemmCfg<128, 128>::LDS, L64 = GemmCfg<64, 64>::LDS;
    (void)hipFuncSetAttribute((const void*)k_qkv, hipFuncAttributeMaxDynamicSharedMemorySize, L128);
    (void)hipFuncSetAttribute((const void*)k_win, hipFuncAttributeMaxDynamicSharedMemorySize, L128);
    (void)hipFuncSetAttribute((const void*)k_sp, hipFuncAttributeMaxDynamicSharedMemorySize, L128);
    (void)hipFuncSetAttribute((const void*)k_chain, hipFuncAttributeMaxDynamicSharedMemorySize, CHAIN_LDS);
    k_qkv<<<16 * 96, 256, L128, stream>>>((const u16*)(ws + WS_H1), Wq, Wk, Wv, (u16*)(ws + WS_Q), (u16*)(ws + WS_K), (u16*)(ws + WS_VT));
    ChainArgs ca{mem0, Wq, Wk, Wv, Wo, ws};
    k_chain<<<68, 256, CHAIN_LDS, stream>>>(ca);
    k_xattn<<<512, 256, 0, stream>>>((const u16*)(ws + WS_Q), (const u16*)(ws + WS_K), (const u16*)(ws + WS_VT), ws + WS_KVM, (u16*)(ws + WS_OX));
    k_wo<<<32 * 9, 256, L64, stream>>>((const u16*)(ws + WS_OX), Wo, x, mod + 2 * D_, (u16*)(ws + WS_X1));
    k_ln<<<512, 256, 0, stream>>>((const u16*)(ws + WS_X1), mod + 3 * D_, mod + 4 * D_, (u16*)(ws + WS_H2));
    k_win<<<16 * 36, 256, L128, stream>>>((const u16*)(ws + WS_H2), Win, (u16*)(ws + WS_Z1), (u16*)(ws + WS_Z2));
    k_ln2304<<<512, 256, 0, stream>>>((u16*)(ws + WS_Z2));
    k_sp<<<16 * 18, 256, L128, stream>>>(Wsp, bsp, (const u16*)(ws + WS_Z2), (const u16*)(ws + WS_Z1), (u16*)(ws + WS_U));
    k_out<<<32 * 9, 256, L64, stream>>>((const u16*)(ws + WS_U), Wout, (const u16*)(ws + WS_X1), mod + 5 * D_, (u16*)d_out);
}
```

```cpp
#include <hip/hip_runtime.h>
#include <hip/hip_bf16.h>
#include <stdint.h>

#define GAS __attribute__((address_space(1)))
#define LAS __attribute__((address_space(3)))
#define DI __device__ __forceinline__
typedef unsigned short u16;
typedef __bf16 bf16x2 __attribute__((ext_vector_type(2)));
typedef __bf16 bf16x8 __attribute__((ext_vector_type(8)));
typedef __bf16 bf16x16 __attribute__((ext_vector_type(16)));
typedef short s16x4 __attribute__((ext_vector_type(4)));
typedef float f32x2 __attribute__((ext_vector_type(2)));
typedef float f32x4 __attribute__((ext_vector_type(4)));
typedef float f32x16 __attribute__((ext_vector_type(16)));
typedef unsigned v2u __attribute__((ext_vector_type(2)));
typedef unsigned v4u __attribute__((ext_vector_type(4)));
typedef unsigned v8u __attribute__((ext_vector_type(8)));
typedef GAS unsigned gu32;
typedef __amdgpu_buffer_rsrc_t rsrc_t;
#define RLX_AGENT __ATOMIC_RELAXED, __HIP_MEMORY_SCOPE_AGENT

constexpr int S_ = 2048, D_ = 576, H_ = 32, E_ = 128, HE = 4096, NSEG = 64, FF = 4608, FH = 2304, NRB = 16;
constexpr int AUX_SC1 = 16;
constexpr int RSRC3 = 0x00020000;
constexpr int OOB = 0x7FFFFF00;

constexpr size_t WS_CTL = 0, CTL_BYTES = 131072;
constexpr size_t WS_MOD = CTL_BYTES;
constexpr size_t WS_H1 = WS_MOD + 16384;
constexpr size_t WS_Q = WS_H1 + (size_t)S_ * D_ * 2;
constexpr size_t WS_K = WS_Q + (size_t)S_ * HE * 2;
constexpr size_t WS_VT = WS_K + (size_t)S_ * HE * 2;
constexpr size_t WS_KVM = WS_VT + (size_t)S_ * HE * 2;
constexpr size_t WS_OX = WS_KVM + (size_t)NSEG * H_ * 4096;
constexpr size_t WS_PART = WS_OX + (size_t)S_ * HE * 2;
constexpr size_t WS_X1 = WS_PART + (size_t)4 * S_ * D_ * 4;
constexpr size_t WS_H2 = WS_X1 + (size_t)S_ * D_ * 2;
constexpr size_t WS_Z1 = WS_H2 + (size_t)S_ * D_ * 2;
constexpr size_t WS_Z2 = WS_Z1 + (size_t)S_ * FH * 2;
constexpr size_t WS_Z2N = WS_Z2 + (size_t)S_ * FH * 2;
constexpr size_t WS_U = WS_Z2N + (size_t)S_ * FH * 2;
constexpr size_t WS_END = WS_U + (size_t)S_ * FH * 2;
constexpr int X_MEM = 0, X_MEM_SLOT = 32768;
constexpr int X_O = 3 * X_MEM_SLOT, X_O_SLOT = 131072;
constexpr int X_HINT = X_O + 2 * X_O_SLOT;
constexpr int X_BYTES = X_HINT + 16384;
constexpr int C_TMO = 0, C_MODA = 32, C_MODB = 64, C_H1 = 96, C_OX = 128, C_PART = C_OX + 32 * NRB, C_H2 = C_PART + 32 * NRB, C_Z = C_H2 + 32 * NRB,
              C_KV = 4096, C_Q = C_KV + 16 * NRB * H_, C_END = C_Q + 16 * NRB * H_;
static_assert(C_Z + 32 * NRB <= C_KV && C_END * 4 <= (int)CTL_BYTES, "control block");

DI unsigned f2u(float x) { return __builtin_bit_cast(unsigned, x); }
DI float u2f(unsigned x) { return __builtin_bit_cast(float, x); }
DI float bf2f(u16 b) { return __builtin_bit_cast(float, (unsigned)b << 16); }
DI float bflo(unsigned w) { return __builtin_bit_cast(float, w << 16); }
DI float bfhi(unsigned w) { return __builtin_bit_cast(float, w & 0xFFFF0000u); }
DI unsigned pk2(float lo, float hi) { f32x2 v = {lo, hi}; bf16x2 b = __builtin_convertvector(v, bf16x2); return __builtin_bit_cast(unsigned, b); }
DI float rbf(float x) { return bflo(pk2(x, 0.f) & 0xFFFFu); }
DI float fadd1(float a, float b) { float r; asm("v_add_f32_e32 %0, %1, %2" : "=v"(r) : "v"(a), "v"(b)); return r; }
DI int lane_id() { return (int)(threadIdx.x & 63); }
DI int wave_id() { return __builtin_amdgcn_readfirstlane((int)(threadIdx.x >> 6)); }

DI f32x4 mfma16(bf16x8 a, bf16x8 b, f32x4 c) { return __builtin_amdgcn_mfma_f32_16x16x32_bf16(a, b, c, 0, 0, 0); }
typedef short s16x4v __attribute__((ext_vector_type(4)));
DI f32x4 mfma16k16(v2u a, v2u b, f32x4 c) { return __builtin_amdgcn_mfma_f32_16x16x16bf16_1k(__builtin_bit_cast(s16x4v, a), __builtin_bit_cast(s16x4v, b), c, 0, 0, 0); }
DI bf16x8 tr2(LAS unsigned char* a0, LAS unsigned char* a1) {
    typedef short s16x8 __attribute__((ext_vector_type(8)));
    const s16x4 lo = __builtin_amdgcn_ds_read_tr16_b64_v4i16((LAS s16x4*)a0), hi = __builtin_amdgcn_ds_read_tr16_b64_v4i16((LAS s16x4*)a1);
    s16x8 v = {lo[0], lo[1], lo[2], lo[3], hi[0], hi[1], hi[2], hi[3]};
    return __builtin_bit_cast(bf16x8, v);
}
DI float xmax16_32(float m) {
    unsigned u = f2u(m);
    auto a = __builtin_amdgcn_permlane16_swap(u, u, false, false);
    m = fmaxf(u2f(a[0]), u2f(a[1]));
    u = f2u(m);
    auto b = __builtin_amdgcn_permlane32_swap(u, u, false, false);
    return fmaxf(u2f(b[0]), u2f(b[1]));
}
DI float xsum16_32(float m) {
    unsigned u = f2u(m);
    auto a = __builtin_amdgcn_permlane16_swap(u, u, false, false);
    m = u2f(a[0]) + u2f(a[1]);
    u = f2u(m);
    auto b = __builtin_amdgcn_permlane32_swap(u, u, false, false);
    return u2f(b[0]) + u2f(b[1]);
}
constexpr float SCL = 0.08838834764831845f * 1.4426950408889634f;

DI f32x4 smf(bf16x8 a, bf16x16 b, f32x4 c, int idx) { return __builtin_amdgcn_smfmac_f32_16x16x64_bf16(a, b, c, idx, 0, 0); }
DI unsigned nosent(unsigned x) { return x < 0xFFFFFFFEu ? x : 0xFFFFFFFEu; }
DI unsigned umax3(unsigned a, unsigned b, unsigned c) { unsigned r; asm("v_max3_u32 %0, %1, %2, %3" : "=v"(r) : "v"(a), "v"(b), "v"(c)); return r; }
DI unsigned vmax(unsigned acc, v4u v) { return umax3(umax3(acc, v.x, v.y), v.z, v.w); }
DI bool all_valid(v4u v) { return (int)(v.x != ~0u) & (int)(v.y != ~0u) & (int)(v.z != ~0u) & (int)(v.w != ~0u); }
#define CB() asm volatile("" ::: "memory")
#define SMF_A(acc, a, wt, idx) asm volatile("v_smfmac_f32_16x16x64_bf16 %0, %1, %2, %3" : "+v"(acc) : "v"(a), "a"(wt), "v"(idx))
#define SMF_V(acc, a, wt, idx) asm volatile("v_smfmac_f32_16x16x64_bf16 %0, %1, %2, %3" : "+v"(acc) : "v"(a), "v"(wt), "v"(idx))
#define MFMA32_0(d, a, b) asm volatile("v_mfma_f32_16x16x32_bf16 %0, %1, %2, 0" : "=&v"(d) : "v"(a), "v"(b))
#define MFMA32_C(d, a, b, cin) asm volatile("v_mfma_f32_16x16x32_bf16 %0, %1, %2, %3" : "=&v"(d) : "v"(a), "v"(b), "v"(cin))
#define MFMA16_0(d, a, b) asm volatile("v_mfma_f32_16x16x16_bf16 %0, %1, %2, 0" : "=&v"(d) : "v"(a), "v"(b))
#define VMAX3(d, a, b, c) asm volatile("v_max3_f32 %0, %1, %2, %3" : "=v"(d) : "v"(a), "v"(b), "v"(c))
#define VMAX(d, a, b) asm volatile("v_max_f32_e32 %0, %1, %2" : "=v"(d) : "v"(a), "v"(b))
#define VMOV(d, a) asm volatile("v_mov_b32_e32 %0, %1" : "=v"(d) : "v"(a))
#define VADD(d, a, b) asm volatile("v_add_f32_e32 %0, %1, %2" : "=v"(d) : "v"(a), "v"(b))
#define VCVT(d, a, b) asm volatile("v_cvt_pk_bf16_f32 %0, %1, %2" : "=v"(d) : "v"(a), "v"(b))
#define VEXP(x) asm volatile("v_exp_f32_e32 %0, %0" : "+v"(x))
#define SWAP16(a, b) asm volatile("v_permlane16_swap_b32_e32 %0, %1" : "+v"(a), "+v"(b))
#define SWAP32(a, b) asm volatile("v_permlane32_swap_b32_e32 %0, %1" : "+v"(a), "+v"(b))
#define PKFMA(d, a, sc, ms) asm volatile("v_pk_fma_f32 %0, %1, %2, %3 op_sel_hi:[1,0,0] neg_lo:[0,0,1] neg_hi:[0,0,1]" : "=v"(d) : "v"(a), "s"(sc), "v"(ms))
#define PKMUL(d, a, r) asm volatile("v_pk_mul_f32 %0, %1, %2 op_sel_hi:[1,0]" : "=v"(d) : "v"(a), "v"(r))
DI f32x4 zero4() { f32x2 a, b; asm volatile("v_mov_b64 %0, 0" : "=v"(a)); asm volatile("v_mov_b64 %0, 0" : "=v"(b)); return f32x4{a[0], a[1], b[0], b[1]}; }
DI v4u poison4() { v2u a, b; asm volatile("v_mov_b64 %0, -1" : "=v"(a)); asm volatile("v_mov_b64 %0, -1" : "=v"(b)); return v4u{a[0], a[1], b[0], b[1]}; }
#define NOP(n) asm volatile("s_nop " #n)
#define MFMA32_ACC(d, a, b) asm volatile("v_mfma_f32_16x16x32_bf16 %0, %1, %2, %0" : "+v"(d) : "v"(a), "v"(b))
#define MFMA16_ACC(d, a, b) asm volatile("v_mfma_f32_16x16x16_bf16 %0, %1, %2, %0" : "+v"(d) : "v"(a), "v"(b))

struct Params {
    const u16* x; const u16* mem0; const u16* Wq; const u16* Wk; const u16* Wv; const u16* Wo;
    const u16* Win; const u16* c; const u16* wmod;
    unsigned char* ws;
    unsigned char* xr;
    int lead_h, lead_y, par, pad;
};
DI rsrc_t mk_rsrc(const void* p, unsigned bytes) { return __builtin_amdgcn_make_buffer_rsrc((void*)p, 0, (int)bytes, RSRC3); }
DI void glds16(const void* g, LAS unsigned char* l) { __builtin_amdgcn_global_load_lds((const GAS unsigned*)g, (LAS unsigned*)l, 16, 0, 0); }
DI unsigned long long cyc() { unsigned long long t; asm volatile("s_memtime %0\n\ts_waitcnt lgkmcnt(0)" : "=s"(t) :: "memory"); return t; }
#define TM(slot)

constexpr unsigned WAIT_CAP = 1u << 16;
DI gu32* ctl_word(const Params& a, int idx) { return (gu32*)(a.ws + WS_CTL) + idx; }
DI void wg_signal(const Params& a, int idx) {
    asm volatile("s_waitcnt vmcnt(0)" ::: "memory");
    __syncthreads();
    if (threadIdx.x == 0) (void)__hip_atomic_fetch_add(ctl_word(a, idx), 1u, RLX_AGENT);
}
DI bool poll_ge(const Params& a, gu32* word, unsigned want) {
    for (unsigned spins = 0;; ++spins) {
        if (__hip_atomic_load(word, RLX_AGENT) >= want) return true;
        __builtin_amdgcn_s_sleep(8);
        if (spins > WAIT_CAP) { __hip_atomic_store(ctl_word(a, C_TMO), 1u, RLX_AGENT); return false; }
        if ((spins & 63u) == 63u && __hip_atomic_load(ctl_word(a, C_TMO), RLX_AGENT) != 0u) return false;
    }
}
template <bool ACQ> DI void wg_wait(const Params& a, gu32* word, unsigned want) {
    if (threadIdx.x == 0) {
        (void)poll_ge(a, word, want);
        if (ACQ) { __builtin_amdgcn_fence(__ATOMIC_ACQUIRE, "agent"); asm volatile("s_waitcnt vmcnt(0)" ::: "memory"); }
        else __builtin_amdgcn_fence(__ATOMIC_ACQUIRE, "wavefront");
    }
    __syncthreads();
}
template <int BM, int BN, int NST> struct GP {
    static constexpr int A_BYTES = BM * 128, B_BYTES = BN * 128, STAGE = A_BYTES + B_BYTES, LDS = NST * STAGE;
    static constexpr int TM = BM / 64, TN = BN / 64, PA = BM / 32, PB = BN / 32, LPS = PA + PB;
    static constexpr int p0(int ks) { return ks * LPS / 4; }
};
DI void dma16(unsigned m0v, int voff, rsrc_t r, int soff) {
    asm volatile("s_mov_b32 m0, %0\n\ts_nop 0\n\tbuffer_load_dwordx4 %1, %2, %3 offen lds" :: "s"(m0v), "v"(voff), "s"(r), "s"(soff) : "memory");
}
template <int N> DI void wait_vm() { asm volatile("s_waitcnt vmcnt(%0)" :: "i"(N) : "memory"); }
#define GSB() __builtin_amdgcn_sched_barrier(0)

template <int BM, int BN, int NST> struct GemmStream {
    using C = GP<BM, BN, NST>;
    LAS unsigned char* lds; unsigned lbase; int wave, ldb128;
    int voA[C::PA], voB[C::PB];
    int aoff, asw, h, boff[C::TN];
    int slot;
    DI void init(LAS unsigned char* l, int lda, int ldb) {
        lds = l; lbase = (unsigned)(size_t)l; slot = 0; ldb128 = 128 * ldb;
        const int lane = lane_id(); wave = wave_id();
        const int wm = wave >> 1, wn = wave & 1;
#pragma unroll
        for (int p = 0; p < C::PA; ++p) { const int piece = wave + 4 * p, row = piece * 8 + (lane >> 3), c = (lane & 7) ^ ((row >> 1) & 7); voA[p] = (row * lda + c * 8) * 2; }
#pragma unroll
        for (int p = 0; p < C::PB; ++p) { const int piece = wave + 4 * p, sub = piece >> 3, k = (piece & 7) * 8 + (lane >> 3), c = (lane & 7) ^ (((k >> 1) & 1) << 2); voB[p] = (k * ldb + sub * 64 + c * 8) * 2; }
        const int r31 = lane & 31; h = lane >> 5; asw = (r31 >> 1) & 7; aoff = (wm * (BM / 2) + r31) * 128;
        const int blk = (lane >> 4) & 1, q = (lane & 15) >> 2, p = lane & 3;
#pragma unroll
        for (int j = 0; j < C::TN; ++j) { const int n = wn * (BN / 2) + j * 32 + 16 * blk + 4 * p, sub = n >> 6, cn = n & 63; boff[j] = C::A_BYTES + sub * 8192 + (8 * h + q) * 128 + (((cn >> 3) ^ (((q >> 1) & 1) << 2)) << 4) + (cn & 7) * 2; }
    }
    template <int I> DI void piece(rsrc_t rA, rsrc_t rB, int t, int sl) {
        const unsigned sb = lbase + sl * C::STAGE;
        if constexpr (I < C::PA) dma16(sb + (wave + 4 * I) * 1024, voA[I], rA, t * 128);
        else dma16(sb + C::A_BYTES + (wave + 4 * (I - C::PA)) * 1024, voB[I - C::PA], rB, t * ldb128);
    }
    template <int I0, int N> DI void pieces(rsrc_t rA, rsrc_t rB, int t, int sl) {
        if constexpr (N > 0) { piece<I0>(rA, rB, t, sl); pieces<I0 + 1, N - 1>(rA, rB, t, sl); }
    }
    DI void prime(rsrc_t rA, rsrc_t rB) {
#pragma unroll
        for (int t = 0; t < NST - 1; ++t) { int sl = slot + t; if (sl >= NST) sl -= NST; pieces<0, C::LPS>(rA, rB, t, sl); }
    }
    DI void ldfrags(LAS unsigned char* cur, int ks, bf16x8 (&af)[C::TM], bf16x8 (&bf)[C::TN]) {
#pragma unroll
        for (int i = 0; i < C::TM; ++i) af[i] = *(LAS bf16x8*)(cur + aoff + i * 4096 + (((2 * ks + h) ^ asw) << 4));
#pragma unroll
        for (int j = 0; j < C::TN; ++j) bf[j] = tr2(cur + boff[j] + ks * 2048, cur + boff[j] + ks * 2048 + 512);
    }
    static constexpr int NM = C::TM * C::TN;
    template <int K, int P0> DI void job(LAS unsigned char* src, int ksn, bool rd, bf16x8 (&an)[C::TM], bf16x8 (&bn)[C::TN], bool issue, rsrc_t sA, rsrc_t sB, int ts, int sp) {
        if constexpr (K < C::TN) { if (rd) bn[K] = tr2(src + boff[K] + ksn * 2048, src + boff[K] + ksn * 2048 + 512); }
        else if constexpr (K < C::TN + C::TM) { if (rd) an[K - C::TN] = *(LAS bf16x8*)(src + aoff + (K - C::TN) * 4096 + (((2 * ksn + h) ^ asw) << 4)); }
        else { if (issue) piece<P0 + K - C::TN - C::TM>(sA, sB, ts, sp); }
    }
    template <int M, int K, int P0, int NJ> DI void jobs_after(LAS unsigned char* src, int ksn, bool rd, bf16x8 (&an)[C::TM], bf16x8 (&bn)[C::TN], bool issue, rsrc_t sA, rsrc_t sB, int ts, int sp) {
        if constexpr (K < NJ) {
            if constexpr (K * NM / NJ == M) job<K, P0>(src, ksn, rd, an, bn, issue, sA, sB, ts, sp);
            jobs_after<M, K + 1, P0, NJ>(src, ksn, rd, an, bn, issue, sA, sB, ts, sp);
        }
    }
    template <int M, int KS, bool SWAP> DI void kstep(const bf16x8 (&ac)[C::TM], const bf16x8 (&bc)[C::TN], f32x16 (&acc)[C::TM][C::TN], LAS unsigned char* src, int ksn, bool rd,
                                           bf16x8 (&an)[C::TM], bf16x8 (&bn)[C::TN], bool issue, rsrc_t sA, rsrc_t sB, int ts, int sp) {
        if constexpr (M < NM) {
            constexpr int i = M / C::TN, j = M % C::TN;
            acc[i][j] = SWAP ? __builtin_amdgcn_mfma_f32_32x32x16_bf16(bc[j], ac[i], acc[i][j], 0, 0, 0) : __builtin_amdgcn_mfma_f32_32x32x16_bf16(ac[i], bc[j], acc[i][j], 0, 0, 0);
            GSB();
            jobs_after<M, 0, C::p0(KS), C::TM + C::TN + C::p0(KS + 1) - C::p0(KS)>(src, ksn, rd, an, bn, issue, sA, sB, ts, sp);
            GSB();
            kstep<M + 1, KS, SWAP>(ac, bc, acc, src, ksn, rd, an, bn, issue, sA, sB, ts, sp);
        }
    }
    template <int EXTRA, bool SWAP>
    DI void tile(rsrc_t rA, rsrc_t rB, rsrc_t nA, rsrc_t nB, bool has_next, int nt, f32x16 (&acc)[C::TM][C::TN]) {
#pragma unroll
        for (int i = 0; i < C::TM; ++i)
#pragma unroll
            for (int j = 0; j < C::TN; ++j)
#pragma unroll
                for (int r = 0; r < 16; ++r) acc[i][j][r] = 0.f;
        bf16x8 a0[C::TM], b0[C::TN], a1[C::TM], b1[C::TN];
        wait_vm<(NST - 2) * C::LPS + EXTRA>();
        asm volatile("s_barrier" ::: "memory");
        ldfrags(lds + slot * C::STAGE, 0, a0, b0);
        for (int t = 0; t < nt; ++t) {
            LAS unsigned char* cur = lds + slot * C::STAGE;
            int sn = slot + 1; if (sn >= NST) sn -= NST;
            int sp = slot + NST - 1; if (sp >= NST) sp -= NST;
            const int tn = t + NST - 1;
            const bool in_cur = tn < nt, issue = in_cur || has_next;
            const rsrc_t sA = in_cur ? rA : nA, sB = in_cur ? rB : nB;
            const int ts = in_cur ? tn : tn - nt;
            kstep<0, 0, SWAP>(a0, b0, acc, cur, 1, true, a1, b1, issue, sA, sB, ts, sp);
            kstep<0, 1, SWAP>(a1, b1, acc, cur, 2, true, a0, b0, issue, sA, sB, ts, sp);
            kstep<0, 2, SWAP>(a0, b0, acc, cur, 3, true, a1, b1, issue, sA, sB, ts, sp);
            if (t + 1 < nt) {
                if (!issue) wait_vm<0>();
                else if (t + 1 <= NST - 2) wait_vm<(NST - 3) * C::LPS + C::p0(3) + EXTRA>();
                else wait_vm<(NST - 3) * C::LPS + C::p0(3)>();
            }
            asm volatile("s_waitcnt lgkmcnt(0)\n\ts_barrier" ::: "memory");
            kstep<0, 3, SWAP>(a1, b1, acc, lds + sn * C::STAGE, 0, t + 1 < nt, a0, b0, issue, sA, sB, ts, sp);
            slot = sn;
        }
    }
};

DI float wsum8(float v) { v += __shfl_xor(v, 1); v += __shfl_xor(v, 2); v += __shfl_xor(v, 4); return v; }
DI void unpack8(v4u w, float (&f)[8]) { f[0] = bflo(w.x); f[1] = bfhi(w.x); f[2] = bflo(w.y); f[3] = bfhi(w.y); f[4] = bflo(w.z); f[5] = bfhi(w.z); f[6] = bflo(w.w); f[7] = bfhi(w.w); }

DI void task_mod(const Params& a, int task, LAS unsigned char* lds) {
    LAS float* red = (LAS float*)lds;
    const int tid = threadIdx.x, cg = tid & 3, kr = tid >> 2;
    const int col0 = task * 32 + cg * 8;
    float acc[8];
#pragma unroll
    for (int j = 0; j < 8; ++j) acc[j] = 0.f;
    v4u w[9];
#pragma unroll
    for (int p = 0; p < 9; ++p) w[p] = *(const v4u*)(a.wmod + (size_t)(kr + 64 * p) * 3456 + col0);
#pragma unroll
    for (int p = 0; p < 9; ++p) {
        const float cv = bf2f(a.c[kr + 64 * p]);
        const float sv = rbf(cv / (1.f + __expf(-cv)));
        float f[8]; unpack8(w[p], f);
#pragma unroll
        for (int j = 0; j < 8; ++j) acc[j] += sv * f[j];
    }
#pragma unroll
    for (int j = 0; j < 8; ++j) red[kr * 33 + cg * 8 + j] = acc[j];
    __syncthreads();
    if (tid < 32) {
        float s = 0.f;
        for (int r = 0; r < 64; ++r) s += red[r * 33 + tid];
        __hip_atomic_store((gu32*)(a.ws + WS_MOD) + task * 32 + tid, f2u(rbf(s)), RLX_AGENT);
    }
}
DI void load_modtab(const Params& a, LAS float* tab, int i_shift, int i_scale, int i_gate) {
    const rsrc_t rM = mk_rsrc(a.ws + WS_MOD, 3456 * 4);
    for (int i = threadIdx.x; i < D_; i += 256) {
        tab[i] = rbf(1.f + u2f(__builtin_amdgcn_raw_buffer_load_b32(rM, (i_scale * D_ + i) * 4, 0, AUX_SC1)));
        tab[D_ + i] = u2f(__builtin_amdgcn_raw_buffer_load_b32(rM, (i_shift * D_ + i) * 4, 0, AUX_SC1));
        tab[2 * D_ + i] = i_gate >= 0 ? u2f(__builtin_amdgcn_raw_buffer_load_b32(rM, (i_gate * D_ + i) * 4, 0, AUX_SC1)) : 0.f;
    }
    __syncthreads();
}
template <bool RES>
DI void task_ln(const Params& a, int row0, LAS float* tab, size_t out_off) {
    const int lane = lane_id(), w = wave_id();
    const int row = row0 + 8 * w + (lane >> 3), sub = lane & 7;
    float v[9][8];
#pragma unroll
    for (int i = 0; i < 9; ++i) unpack8(*(const v4u*)(a.x + (size_t)row * D_ + (sub + 8 * i) * 8), v[i]);
    if (RES) {
        const rsrc_t rP = mk_rsrc(a.ws + WS_PART + (size_t)(row >> 7) * 4 * 128 * D_ * 4, 4 * 128 * D_ * 4);
        const rsrc_t rX1 = mk_rsrc(a.ws + WS_X1, S_ * D_ * 2);
#pragma unroll
        for (int i = 0; i < 9; ++i) {
            float s[8];
#pragma unroll
            for (int j = 0; j < 8; ++j) s[j] = 0.f;
#pragma unroll
            for (int kq = 0; kq < 4; ++kq) {
                const int off = ((kq * 128 + (row & 127)) * D_ + (sub + 8 * i) * 8) * 4;
                const v4u p0 = __builtin_amdgcn_raw_buffer_load_b128(rP, off, 0, AUX_SC1), p1 = __builtin_amdgcn_raw_buffer_load_b128(rP, off + 16, 0, AUX_SC1);
                s[0] += u2f(p0.x); s[1] += u2f(p0.y); s[2] += u2f(p0.z); s[3] += u2f(p0.w); s[4] += u2f(p1.x); s[5] += u2f(p1.y); s[6] += u2f(p1.z); s[7] += u2f(p1.w);
            }
#pragma unroll
            for (int j = 0; j < 8; ++j) v[i][j] = rbf(v[i][j] + rbf(tab[2 * D_ + (sub + 8 * i) * 8 + j] * rbf(s[j])));
            const v4u o = {pk2(v[i][0], v[i][1]), pk2(v[i][2], v[i][3]), pk2(v[i][4], v[i][5]), pk2(v[i][6], v[i][7])};
            __builtin_amdgcn_raw_buffer_store_b128(o, rX1, (row * D_ + (sub + 8 * i) * 8) * 2, 0, AUX_SC1);
        }
    }
    float s = 0.f;
#pragma unroll
    for (int i = 0; i < 9; ++i)
#pragma unroll
        for (int j = 0; j < 8; ++j) s += v[i][j];
    const float mean = wsum8(s) * (1.f / D_);
    float q = 0.f;
#pragma unroll
    for (int i = 0; i < 9; ++i)
#pragma unroll
        for (int j = 0; j < 8; ++j) { const float d = v[i][j] - mean; q += d * d; }
    const float rstd = 1.f / sqrtf(wsum8(q) * (1.f / D_) + 1e-5f);
    const rsrc_t rO = mk_rsrc(a.ws + out_off, S_ * D_ * 2);
#pragma unroll
    for (int i = 0; i < 9; ++i) {
        float o[8];
#pragma unroll
        for (int j = 0; j < 8; ++j) { const int cc = (sub + 8 * i) * 8 + j; o[j] = rbf(rbf((v[i][j] - mean) * rstd) * tab[cc]) + tab[D_ + cc]; }
        const v4u ov = {pk2(o[0], o[1]), pk2(o[2], o[3]), pk2(o[4], o[5]), pk2(o[6], o[7])};
        __builtin_amdgcn_raw_buffer_store_b128(ov, rO, (row * D_ + (sub + 8 * i) * 8) * 2, 0, AUX_SC1);
    }
}
DI float wsum64(float v) {
#pragma unroll
    for (int m = 1; m < 64; m <<= 1) v += __shfl_xor(v, m);
    return v;
}
DI void task_ln2(const Params& a, int row0, int nrows_per_wave, LAS float* tab) {
    const int lane = lane_id(), w = wave_id();
    const rsrc_t rX1 = mk_rsrc(a.ws + WS_X1, S_ * D_ * 2), rH2 = mk_rsrc(a.ws + WS_H2, S_ * D_ * 2);
    const bool act = lane < 48;
    for (int it = 0; it < nrows_per_wave; ++it) {
        const int row = row0 + w * nrows_per_wave + it;
        const rsrc_t rP = mk_rsrc(a.ws + WS_PART + (size_t)(row >> 7) * 4 * 128 * D_ * 4, 4 * 128 * D_ * 4);
        v4u p[3][4]; v2u xv[3];
#pragma unroll
        for (int i = 0; i < 3; ++i) {
            const int col = (lane + 48 * i) * 4;
            xv[i] = act ? *(const v2u*)(a.x + (size_t)row * D_ + col) : v2u{0u, 0u};
#pragma unroll
            for (int kq = 0; kq < 4; ++kq) p[i][kq] = __builtin_amdgcn_raw_buffer_load_b128(rP, act ? ((kq * 128 + (row & 127)) * D_ + col) * 4 : OOB, 0, AUX_SC1);
        }
        float v[3][4]; float s = 0.f;
#pragma unroll
        for (int i = 0; i < 3; ++i) {
            const int col = act ? (lane + 48 * i) * 4 : 0;
            const float xs[4] = {bflo(xv[i].x), bfhi(xv[i].x), bflo(xv[i].y), bfhi(xv[i].y)};
#pragma unroll
            for (int j = 0; j < 4; ++j) {
                const float t = (u2f(p[i][0][j]) + u2f(p[i][1][j])) + (u2f(p[i][2][j]) + u2f(p[i][3][j]));
                v[i][j] = act ? rbf(xs[j] + rbf(tab[2 * D_ + col + j] * rbf(t))) : 0.f;
                s += v[i][j];
            }
        }
        const float mean = wsum64(s) * (1.f / D_);
        float q = 0.f;
#pragma unroll
        for (int i = 0; i < 3; ++i)
#pragma unroll
            for (int j = 0; j < 4; ++j) { const float d = act ? v[i][j] - mean : 0.f; q += d * d; }
        const float rstd = 1.f / sqrtf(wsum64(q) * (1.f / D_) + 1e-5f);
#pragma unroll
        for (int i = 0; i < 3; ++i) {
            const int col = act ? (lane + 48 * i) * 4 : 0;
            float o[4];
#pragma unroll
            for (int j = 0; j < 4; ++j) o[j] = rbf(rbf((v[i][j] - mean) * rstd) * tab[col + j]) + tab[D_ + col + j];
            const unsigned a0 = pk2(v[i][0], v[i][1]), a1 = pk2(v[i][2], v[i][3]), b0 = pk2(o[0], o[1]), b1 = pk2(o[2], o[3]);
            const v4u xo = {a0, a1, (unsigned)__shfl_down((int)a0, 1), (unsigned)__shfl_down((int)a1, 1)}, ho = {b0, b1, (unsigned)__shfl_down((int)b0, 1), (unsigned)__shfl_down((int)b1, 1)};
            const int off = (act && (lane & 1) == 0) ? (row * D_ + col) * 2 : OOB;
            __builtin_amdgcn_raw_buffer_store_b128(xo, rX1, off, 0, AUX_SC1);
            __builtin_amdgcn_raw_buffer_store_b128(ho, rH2, off, 0, AUX_SC1);
        }
    }
}
DI void task_ln3(const Params& a, int row0, int nit) {
    const int lane = lane_id(), w = wave_id(), sub = lane & 31;
    const rsrc_t rZ = mk_rsrc(a.ws + WS_Z2, S_ * FH * 2), rN = mk_rsrc(a.ws + WS_Z2N, S_ * FH * 2);
    for (int it = 0; it < nit; ++it) {
        const int row = row0 + 2 * nit * w + 2 * it + (lane >> 5);
        float v[9][8];
#pragma unroll
        for (int i = 0; i < 9; ++i) unpack8(__builtin_amdgcn_raw_buffer_load_b128(rZ, (row * FH + (sub + 32 * i) * 8) * 2, 0, AUX_SC1), v[i]);
        float s = 0.f;
#pragma unroll
        for (int i = 0; i < 9; ++i)
#pragma unroll
            for (int j = 0; j < 8; ++j) s += v[i][j];
#pragma unroll
        for (int m = 1; m < 32; m <<= 1) s += __shfl_xor(s, m);
        const float mean = s * (1.f / FH);
        float q = 0.f;
#pragma unroll
        for (int i = 0; i < 9; ++i)
#pragma unroll
            for (int j = 0; j < 8; ++j) { const float d = v[i][j] - mean; q += d * d; }
#pragma unroll
        for (int m = 1; m < 32; m <<= 1) q += __shfl_xor(q, m);
        const float rstd = 1.f / sqrtf(q * (1.f / FH) + 1e-5f);
#pragma unroll
        for (int i = 0; i < 9; ++i) {
            const v4u ov = {pk2((v[i][0] - mean) * rstd, (v[i][1] - mean) * rstd), pk2((v[i][2] - mean) * rstd, (v[i][3] - mean) * rstd),
                            pk2((v[i][4] - mean) * rstd, (v[i][5] - mean) * rstd), pk2((v[i][6] - mean) * rstd, (v[i][7] - mean) * rstd)};
            __builtin_amdgcn_raw_buffer_store_b128(ov, rN, (row * FH + (sub + 32 * i) * 8) * 2, 0, AUX_SC1);
        }
    }
}

constexpr int EPI_STORES = 8;
template <class F> DI void epi_tile128(const f32x16 (&acc)[2][2], LAS unsigned char* img, bool swapped, rsrc_t rO, int dst, int rstride, F&& f) {
    const int lane = lane_id(), wave = wave_id(), wm = wave >> 1, wn = wave & 1;
#pragma unroll
    for (int i = 0; i < 2; ++i)
#pragma unroll
        for (int j = 0; j < 2; ++j)
#pragma unroll
            for (int rg = 0; rg < 4; ++rg) {
                int row, col;
                if (swapped) { row = wm * 64 + i * 32 + (lane & 31); col = wn * 64 + j * 32 + 8 * rg + 4 * (lane >> 5); }
                else {
                    row = wn * 64 + j * 32 + (lane & 31);
                    const int tk = 8 * rg + 4 * (lane >> 5);
                    col = wm * 64 + i * 32 + (tk < 16 ? 2 * tk : 2 * (tk - 16) + 4);
                }
                const v2u o = {pk2(f(acc[i][j][4 * rg]), f(acc[i][j][4 * rg + 1])), pk2(f(acc[i][j][4 * rg + 2]), f(acc[i][j][4 * rg + 3]))};
                *(LAS v2u*)(img + row * 256 + ((((col >> 3) ^ (row & 7)) << 4) | ((col & 4) << 1))) = o;
            }
    asm volatile("s_waitcnt lgkmcnt(0)\n\ts_barrier" ::: "memory");
#pragma unroll
    for (int it = 0; it < EPI_STORES; ++it) {
        const int id = it * 256 + threadIdx.x, row = id >> 4, ch = id & 15;
        const v4u v = *(LAS v4u*)(img + row * 256 + ((ch ^ (row & 7)) << 4));
        __builtin_amdgcn_raw_buffer_store_b128(v, rO, dst + row * rstride + ch * 16, 0, AUX_SC1);
    }
    asm volatile("s_waitcnt lgkmcnt(0)\n\ts_barrier" ::: "memory");
}

DI float gelu_tanh(float x) {
    const float u = 0.7978845608028654f * (x + 0.044715f * x * x * x);
    const float e = __expf(2.f * u);
    return 0.5f * x * (1.f + (1.f - 2.f / (e + 1.f)));
}

DI void qkv_decode(int t, int& rb, int& head, int& mat) {
    if (t < 2 * NRB * H_) { rb = t >> 6; head = (t >> 1) & 31; mat = 1 + (t & 1); }
    else { const int u = t - 2 * NRB * H_; rb = u >> 5; head = u & 31; mat = 0; }
}
DI void phase_qkv(const Params& a, int j, int nw, LAS unsigned char* lds) {
    using GS = GemmStream<128, 128, 3>;
    GS gs; gs.init(lds, D_, HE);
    LAS unsigned char* img = lds + GS::C::LDS;
    constexpr int NT = 3 * NRB * H_;
    auto rA = [&](int t) { int rb, h, m; qkv_decode(t, rb, h, m); return mk_rsrc(a.ws + WS_H1 + (size_t)rb * 128 * D_ * 2, 128 * D_ * 2); };
    auto rB = [&](int t) { int rb, h, m; qkv_decode(t, rb, h, m); return mk_rsrc((m == 0 ? a.Wq : (m == 1 ? a.Wk : a.Wv)) + h * 128, D_ * HE * 2); };
    if (j >= NT) return;
    gs.prime(rA(j), rB(j));
    bool first = true;
    for (int t = j; t < NT; t += nw) {
        int rb, head, mat; qkv_decode(t, rb, head, mat);
        const bool hn = t + nw < NT; const int tn = hn ? t + nw : t;
        f32x16 acc[2][2];
        if (mat == 2) { if (first) gs.template tile<0, false>(rA(t), rB(t), rA(tn), rB(tn), hn, 9, acc); else gs.template tile<EPI_STORES, false>(rA(t), rB(t), rA(tn), rB(tn), hn, 9, acc); }
        else          { if (first) gs.template tile<0, true>(rA(t), rB(t), rA(tn), rB(tn), hn, 9, acc);  else gs.template tile<EPI_STORES, true>(rA(t), rB(t), rA(tn), rB(tn), hn, 9, acc); }
        first = false;
        if (mat == 2) epi_tile128(acc, img, false, mk_rsrc(a.ws + WS_VT, S_ * HE * 2), (head * E_ * S_ + rb * 128) * 2, S_ * 2, [](float x) { return x; });
        else epi_tile128(acc, img, true, mk_rsrc(a.ws + (mat == 0 ? WS_Q : WS_K), S_ * HE * 2), (head * S_ + rb * 128) * E_ * 2, E_ * 2, [](float x) { return x; });
        wg_signal(a, (mat == 0 ? C_Q : C_KV) + (rb * H_ + head) * 16);
    }
}

DI void xattn_unit(const Params& a, int seg, int head, LAS unsigned char* wl  ) {
    const int lane = lane_id(), c = lane & 15, g = lane >> 4;
    const rsrc_t rQ = mk_rsrc(a.ws + WS_Q, S_ * HE * 2), rK = mk_rsrc(a.ws + WS_K, S_ * HE * 2), rV = mk_rsrc(a.ws + WS_VT, S_ * HE * 2);
    const rsrc_t rM = mk_rsrc(a.ws + WS_KVM, NSEG * H_ * 4096), rO = mk_rsrc(a.ws + WS_OX, S_ * HE * 2);
    const int tok0 = head * S_ + seg * 32;
    const int km = (seg * H_ + head) * 4096;
    {
        const v4u x0 = __builtin_amdgcn_raw_buffer_load_b128(rM, km + lane * 16, 0, AUX_SC1), x1 = __builtin_amdgcn_raw_buffer_load_b128(rM, km + 1024 + lane * 16, 0, AUX_SC1);
        *(LAS v4u*)(wl + lane * 16) = x0; *(LAS v4u*)(wl + 1024 + lane * 16) = x1;
    }
    v4u qf[2][4], kf[2][4];
#pragma unroll
    for (int t = 0; t < 2; ++t)
#pragma unroll
        for (int ks = 0; ks < 4; ++ks) {
            qf[t][ks] = __builtin_amdgcn_raw_buffer_load_b128(rQ, ((tok0 + 16 * t + c) * E_ + 32 * ks + 8 * g) * 2, 0, AUX_SC1);
            kf[t][ks] = __builtin_amdgcn_raw_buffer_load_b128(rK, ((tok0 + 16 * t + c) * E_ + 32 * ks + 8 * g) * 2, 0, AUX_SC1);
        }
    v4u vf[8]; v2u vmf[8];
#pragma unroll
    for (int et = 0; et < 8; ++et) {
        const int e = 16 * et + c;
        vf[et] = __builtin_amdgcn_raw_buffer_load_b128(rV, ((head * E_ + e) * S_ + seg * 32 + 8 * g) * 2, 0, AUX_SC1);
        vmf[et] = __builtin_amdgcn_raw_buffer_load_b64(rM, km + 2048 + e * 16 + (g & 1) * 8, 0, AUX_SC1);
    }
    __builtin_amdgcn_s_waitcnt(0xC07F);
    __builtin_amdgcn_wave_barrier();
    const int q = c >> 2, p = c & 3;
    f32x4 st[2][3];
#pragma unroll
    for (int t = 0; t < 2; ++t)
#pragma unroll
        for (int kt = 0; kt < 3; ++kt) st[t][kt] = f32x4{0, 0, 0, 0};
#pragma unroll
    for (int ks = 0; ks < 4; ++ks) {
        const int e0 = 32 * ks + 8 * g + q;
        const bf16x8 km8 = tr2(wl + e0 * 16 + (p & 1) * 8, wl + (e0 + 4) * 16 + (p & 1) * 8);
#pragma unroll
        for (int t = 0; t < 2; ++t) {
            const bf16x8 qq = __builtin_bit_cast(bf16x8, qf[t][ks]);
            st[t][0] = mfma16(km8, qq, st[t][0]);
            st[t][1] = mfma16(__builtin_bit_cast(bf16x8, kf[0][ks]), qq, st[t][1]);
            st[t][2] = mfma16(__builtin_bit_cast(bf16x8, kf[1][ks]), qq, st[t][2]);
        }
    }
    bf16x8 pf[2]; v2u pm[2]; float rz[2];
#pragma unroll
    for (int t = 0; t < 2; ++t) {
        const int qi = 16 * t + c;
        float s[12];
#pragma unroll
        for (int r = 0; r < 4; ++r) {
            s[r] = g < 2 ? st[t][0][r] * SCL : -INFINITY;
            s[4 + r] = (4 * g + r) <= qi ? st[t][1][r] * SCL : -INFINITY;
            s[8 + r] = (16 + 4 * g + r) <= qi ? st[t][2][r] * SCL : -INFINITY;
        }
        float m = s[0];
#pragma unroll
        for (int r = 1; r < 12; ++r) m = fmaxf(m, s[r]);
        m = xmax16_32(m);
        float z = 0.f;
#pragma unroll
        for (int r = 0; r < 12; ++r) { s[r] = __builtin_amdgcn_exp2f(s[r] - m); z += s[r]; }
        z = xsum16_32(z);
        rz[t] = 1.f / z;
        pm[t] = v2u{pk2(s[0], s[1]), pk2(s[2], s[3])};
        const v4u pp = {pk2(s[4], s[5]), pk2(s[6], s[7]), pk2(s[8], s[9]), pk2(s[10], s[11])};
        pf[t] = __builtin_bit_cast(bf16x8, pp);
    }
#pragma unroll
    for (int ep = 0; ep < 4; ++ep) {
        f32x4 o[2][2];
#pragma unroll
        for (int et = 0; et < 2; ++et)
#pragma unroll
            for (int t = 0; t < 2; ++t) {
                o[et][t] = mfma16(__builtin_bit_cast(bf16x8, vf[2 * ep + et]), pf[t], f32x4{0, 0, 0, 0});
                o[et][t] = mfma16k16(vmf[2 * ep + et], pm[t], o[et][t]);
            }
#pragma unroll
        for (int t = 0; t < 2; ++t) {
            const unsigned d0 = pk2(o[0][t][0] * rz[t], o[0][t][1] * rz[t]), d1 = pk2(o[0][t][2] * rz[t], o[0][t][3] * rz[t]);
            const unsigned e0 = pk2(o[1][t][0] * rz[t], o[1][t][1] * rz[t]), e1 = pk2(o[1][t][2] * rz[t], o[1][t][3] * rz[t]);
            auto s0 = __builtin_amdgcn_permlane16_swap(d0, e0, false, false);
            auto s1 = __builtin_amdgcn_permlane16_swap(d1, e1, false, false);
            const v4u val = {s0[0], s1[0], s0[1], s1[1]};
            const int eo = 32 * ep + ((g & 1) << 4) + ((g >> 1) << 3);
            __builtin_amdgcn_raw_buffer_store_b128(val, rO, ((seg * 32 + 16 * t + c) * HE + head * E_ + eo) * 2, 0, AUX_SC1);
        }
    }
}
DI void role_att(const Params& a, int head, LAS unsigned char* lds) {
    const int w = wave_id(), lane = lane_id();
    for (int rb = 0; rb < NRB; ++rb) {
        const int seg = 4 * rb + w;
        if (lane == 0) {
            (void)poll_ge(a, ctl_word(a, C_KV + (rb * H_ + head) * 16), 2u); (void)poll_ge(a, ctl_word(a, C_Q + (rb * H_ + head) * 16), 1u);
            (void)poll_ge(a, (gu32*)(a.xr + a.par * 8192 + X_HINT + 8192 + head * 256), (unsigned)(seg + 2));
        }
        __builtin_amdgcn_fence(__ATOMIC_ACQUIRE, "wavefront");
        __builtin_amdgcn_wave_barrier();
        xattn_unit(a, seg, head, lds + w * 2048);
        wg_signal(a, C_OX + rb * 32);
    }
}
DI void role_wo(const Params& a, int task, LAS unsigned char* lds) {
    using GS = GemmStream<128, 64, 4>;
    GS gs; gs.init(lds, HE, D_);
    const int kq = task / 9, nt = task % 9;
    const int lane = lane_id(), wave = wave_id(), wm = wave >> 1, wn = wave & 1;
    const rsrc_t rB = mk_rsrc(a.Wo + (size_t)kq * 1024 * D_ + nt * 64, 1024 * D_ * 2);
    for (int rb = 0; rb < NRB; ++rb) {
        wg_wait<true>(a, ctl_word(a, C_OX + rb * 32), 32u);
        const rsrc_t rA = mk_rsrc(a.ws + WS_OX + ((size_t)rb * 128 * HE + kq * 1024) * 2, 128 * HE * 2);
        gs.prime(rA, rB);
        f32x16 acc[2][1];
        gs.template tile<0, true>(rA, rB, rA, rB, false, 16, acc);
        const rsrc_t rP = mk_rsrc(a.ws + WS_PART + (size_t)(rb * 4 + kq) * 128 * D_ * 4, 128 * D_ * 4);
#pragma unroll
        for (int i = 0; i < 2; ++i)
#pragma unroll
            for (int rg = 0; rg < 4; ++rg) {
                const int m = wm * 64 + i * 32 + (lane & 31), n = nt * 64 + wn * 32 + 8 * rg + 4 * (lane >> 5);
                const v4u o = {f2u(acc[i][0][4 * rg]), f2u(acc[i][0][4 * rg + 1]), f2u(acc[i][0][4 * rg + 2]), f2u(acc[i][0][4 * rg + 3])};
                __builtin_amdgcn_raw_buffer_store_b128(o, rP, (m * D_ + n) * 4, 0, AUX_SC1);
            }
        wg_signal(a, C_PART + rb * 32);
    }
}
DI void role_ln2(const Params& a, int q, LAS unsigned char* lds) {
    LAS float* tab = (LAS float*)lds;
    wg_wait<false>(a, ctl_word(a, C_MODB), 108u);
    load_modtab(a, tab, 3, 4, 2);
    for (int rb = 0; rb < NRB; ++rb) {
        wg_wait<false>(a, ctl_word(a, C_PART + rb * 32), 36u);
        task_ln2(a, rb * 128 + q * 8, 2, tab);
        wg_signal(a, C_H2 + rb * 32);
    }
}
DI void role_win(const Params& a, int nt, LAS unsigned char* lds) {
    using GS = GemmStream<128, 128, 3>;
    GS gs; gs.init(lds, D_, FF);
    LAS unsigned char* img = lds + GS::C::LDS;
    const rsrc_t rB = mk_rsrc(a.Win + nt * 128, D_ * FF * 2);
    const rsrc_t rO = mk_rsrc(a.ws + (nt < 18 ? WS_Z1 : WS_Z2), S_ * FH * 2);
    for (int rb = 0; rb < NRB; ++rb) {
        wg_wait<true>(a, ctl_word(a, C_H2 + rb * 32), 16u);
        const rsrc_t rA = mk_rsrc(a.ws + WS_H2 + (size_t)rb * 128 * D_ * 2, 128 * D_ * 2);
        gs.prime(rA, rB);
        f32x16 acc[2][2];
        gs.template tile<0, true>(rA, rB, rA, rB, false, 9, acc);
        epi_tile128(acc, img, true, rO, (rb * 128 * FH + (nt % 18) * 128) * 2, FH * 2, [](float x) { return gelu_tanh(rbf(x)); });
        wg_signal(a, C_Z + rb * 32);
    }
}
DI void role_ln3(const Params& a, int q) {
    for (int rb = 0; rb < NRB; ++rb) {
        wg_wait<false>(a, ctl_word(a, C_Z + rb * 32), 36u);
        task_ln3(a, rb * 128 + q * 16, 2);
    }
}
DI void worker_role(const Params& a, int j, LAS unsigned char* lds) {
    constexpr int NW = 188;
    if (j < 108) { task_mod(a, j, lds); if (j < 36) { wg_signal(a, C_MODA); if (threadIdx.x == 0) (void)__hip_atomic_fetch_add(ctl_word(a, C_MODB), 1u, RLX_AGENT); } else wg_signal(a, C_MODB); }
    if (j >= 108 && j < 172) {
        LAS float* tab = (LAS float*)lds;
        wg_wait<false>(a, ctl_word(a, C_MODA), 36u);
        load_modtab(a, tab, 0, 1, -1);
        task_ln<false>(a, (j - 108) * 32, tab, WS_H1);
        wg_signal(a, C_H1);
    }
    wg_wait<true>(a, ctl_word(a, C_H1), 64u);
    phase_qkv(a, j, NW, lds);
    TM(1);
#define RMASK 31
    if (j < 32) { if (RMASK & 1) role_att(a, j, lds); }
    else if (j < 68) { if (RMASK & 2) role_wo(a, j - 32, lds); }
    else if (j < 84) { if (RMASK & 4) role_ln2(a, j - 68, lds); }
    else if (j < 120) { if (RMASK & 8) role_win(a, j - 84, lds); }
    else if (j < 128) { if (RMASK & 16) role_ln3(a, j - 120); }
    TM(2);
}

constexpr unsigned SPIN_CAP = 1u << 14;
DI void hint_wait(rsrc_t rX, int w, int lane, unsigned want) {
    const int off = lane < 8 ? X_HINT + (8 * w + lane) * 256 : OOB;
    for (unsigned spins = 0; spins < 8192u; ++spins) {
        CB();
        const unsigned v = __builtin_amdgcn_raw_buffer_load_b32(rX, off, 0, AUX_SC1);
        if (!__any(lane < 8 && v < want)) break;
    }
}
DI void delay_from(unsigned long long t0, unsigned d) { while ((unsigned)(cyc() - t0) + 40u < d) {} }
DI int mem_slot(int s) { return s % 3; }
DI int o_slot(int s) { return s & 1; }
constexpr int HL_Q = 0;
constexpr int HL_K = HL_Q + 2048;
constexpr int HL_KX = HL_K + 2048;
constexpr int HL_WV = HL_KX + 2 * 8192;
constexpr int HL_STAGE = HL_WV + 4 * 9 * 2048;
constexpr int HL_END = HL_STAGE + 3 * 16384;

DI int wsw(int k) { return ((k & 1) | (((k >> 2) & 1) << 1) | (((k >> 3) & 1) << 2)) << 1; }
DI void stage_w(LAS unsigned char* dst, const u16* __restrict__ src, int ld, int wave, int lane) {
#pragma unroll
    for (int p = 0; p < 4; ++p) {
        const int piece = wave + 4 * p, k = piece * 4 + (lane >> 4), ch = (lane & 15) ^ wsw(k);
        glds16(src + (size_t)k * ld + ch * 8, dst + piece * 1024 + lane * 16);
    }
}
DI bf16x16 read_wfrag(LAS unsigned char* st, int col, int lane) {
    const int gb = lane >> 4, q = (lane & 15) >> 2, p = lane & 3;
    s16x4 r[4];
#pragma unroll
    for (int m = 0; m < 4; ++m) {
        const int d0 = 32 * (m >> 1) + 16 * (gb >> 1) + 4 * (gb & 1) + 2 * (m & 1);
        const int k = d0 + (q & 1) + 8 * (q >> 1);
        const int cb = (col + 4 * p) * 2;
        const int ch = (cb >> 4) ^ wsw(k);
        r[m] = __builtin_amdgcn_ds_read_tr16_b64_v4i16((LAS s16x4*)(st + k * 256 + ch * 16 + (cb & 15)));
    }
    typedef short s16x16 __attribute__((ext_vector_type(16)));
    s16x16 v = {r[0][0], r[0][1], r[0][2], r[0][3], r[1][0], r[1][1], r[1][2], r[1][3], r[2][0], r[2][1], r[2][2], r[2][3], r[3][0], r[3][1], r[3][2], r[3][3]};
    return __builtin_bit_cast(bf16x16, v);
}

DI void head_role(const Params& a, int h, LAS unsigned char* lds) {
    const int lane = lane_id(), w = wave_id(), c = lane & 15, g = lane >> 4;
    unsigned char* ws = a.ws;
    gu32* ctl = (gu32*)(ws + WS_CTL);
    const rsrc_t rX = __builtin_amdgcn_make_buffer_rsrc((void*)(a.xr + a.par * 8192), 0, X_BYTES, RSRC3);
    const rsrc_t rKVM = __builtin_amdgcn_make_buffer_rsrc((void*)(ws + WS_KVM), 0, NSEG * H_ * 4096, RSRC3);
    bf16x16 WA[4][8];
    bf16x16 WB[4];
    bf16x16 WV[9];
    LAS unsigned char* wv1 = lds + HL_WV + w * 9 * 2048 + lane * 16;
    {
        LAS unsigned char* st = lds + HL_STAGE;
        const u16* src[3] = {a.Wq + h * 128, a.Wk + h * 128, a.Wv + h * 128};
#pragma unroll
        for (int i = 0; i < 2; ++i) stage_w(st + i * 16384, src[0] + (size_t)i * 64 * HE, HE, w, lane);
#pragma unroll
        for (int n = 0; n < 27; ++n) {
            if (n + 2 < 27) stage_w(st + ((n + 2) % 3) * 16384, src[(n + 2) / 9] + (size_t)((n + 2) % 9) * 64 * HE, HE, w, lane);
            if (n + 2 < 27) asm volatile("s_waitcnt vmcnt(8)" ::: "memory");
            else if (n + 1 < 27) asm volatile("s_waitcnt vmcnt(4)" ::: "memory");
            else asm volatile("s_waitcnt vmcnt(0)" ::: "memory");
            __builtin_amdgcn_s_barrier();
            const bf16x16 f0 = read_wfrag(st + (n % 3) * 16384, 32 * w, lane), f1 = read_wfrag(st + (n % 3) * 16384, 32 * w + 16, lane);
            const int m = n / 9, f = n % 9;
            if (m < 2) {
                if (f < 8) { WA[2 * m][f] = f0; WA[2 * m + 1][f] = f1; asm volatile("" : "+a"(WA[2 * m][f]), "+a"(WA[2 * m + 1][f])); }
                else { WB[2 * m] = f0; WB[2 * m + 1] = f1; }
            } else {
                WV[f] = f0;
                const v8u t = __builtin_bit_cast(v8u, f1);
                *(LAS v4u*)(wv1 + f * 2048) = v4u{t[0], t[1], t[2], t[3]};
                *(LAS v4u*)(wv1 + f * 2048 + 1024) = v4u{t[4], t[5], t[6], t[7]};
            }
            asm volatile("s_waitcnt lgkmcnt(0)" ::: "memory");
            __builtin_amdgcn_s_barrier();
        }
    }
    const int idx = (lane & 1) ? 0xEEEE : 0x4444;
    const rsrc_t rK = mk_rsrc(ws + WS_K + (size_t)h * S_ * E_ * 2, S_ * E_ * 2), rV = mk_rsrc(ws + WS_VT + (size_t)h * E_ * S_ * 2, E_ * S_ * 2);
    const int q4 = c >> 2, p4 = c & 3;
    LAS unsigned char* qkw = lds + HL_Q + (32 * w + c) * 16 + 4 * g;
    LAS unsigned char* qr = lds + HL_Q + (8 * g + q4) * 16 + (p4 & 1) * 8;
    LAS unsigned char* kr = lds + HL_K + (8 * g + q4) * 16 + (p4 >> 1) * 8;
    const int kxr = HL_KX + c * 256;
    int kxsrc[2];
#pragma unroll
    for (int p = 0; p < 2; ++p) { const int key = 8 * w + 4 * p + g; kxsrc[p] = key * E_ + ((c ^ (key & 15)) * 8); }
    int ooff;
    {
        const int el = 32 * w + ((g & 1) << 4) + ((g >> 1) << 3);
        const int win = el >> 6, gg = (el >> 4) & 3, hf = (el >> 3) & 1;
        const int r = 4 * (c & 3) + 2 * win + hf;
        const int p = h * 2 + ((c >> 2) & 1);
        ooff = c < 8 ? X_O + (p >> 3) * 16384 + (p & 7) * 1024 + (16 * gg + r) * 16 : 0x7F000000;
    }
    const float ninf = (g & 1) ? -INFINITY : 0.f;
    const int hoff = (w < 2 && lane == 0) ? X_HINT + w * 8192 + h * 256 : OOB;
    unsigned nfail = 0;
    bool fail = false;
    TM(1);
    for (int s = 0; s < NSEG; ++s) {
        if ((s & 3) == 0) { if (lane == 0) (void)poll_ge(a, ctl_word(a, C_KV + ((s >> 2) * H_ + h) * 16), 2u); __builtin_amdgcn_fence(__ATOMIC_ACQUIRE, "wavefront"); __builtin_amdgcn_wave_barrier(); }
        v4u vx[2], kxr_[2];
        LAS unsigned char* kxb = lds + HL_KX + (s & 1) * 8192;
#pragma unroll
        for (int p = 0; p < 2; ++p) kxr_[p] = __builtin_amdgcn_raw_buffer_load_b128(rK, (s * 32 * E_ + kxsrc[p]) * 2, 0, AUX_SC1);
#pragma unroll
        for (int t = 0; t < 2; ++t) vx[t] = __builtin_amdgcn_raw_buffer_load_b128(rV, ((32 * w + 16 * t + c) * S_ + s * 32 + 8 * g) * 2, 0, AUX_SC1);
#define KX_TO_LDS() { *(LAS v4u*)(kxb + (2 * w) * 1024 + lane * 16) = kxr_[0]; *(LAS v4u*)(kxb + (2 * w + 1) * 1024 + lane * 16) = kxr_[1]; }
        bf16x16 v1f[4];
#define LDV1(slot, f) { const v4u lo_ = *(LAS v4u*)(wv1 + (f) * 2048), hi_ = *(LAS v4u*)(wv1 + (f) * 2048 + 1024); \
                        v1f[slot] = __builtin_bit_cast(bf16x16, (v8u{lo_[0], lo_[1], lo_[2], lo_[3], hi_[0], hi_[1], hi_[2], hi_[3]})); }
#define SB() __builtin_amdgcn_sched_barrier(0)
#define QK4(f) { SMF_A(acc[0], af[f], WA[0][f], idx); SMF_A(acc[1], af[f], WA[1][f], idx); SMF_A(acc[2], af[f], WA[2][f], idx); SMF_A(acc[3], af[f], WA[3][f], idx); }
        LDV1(0, 0); LDV1(1, 1); LDV1(2, 2); LDV1(3, 3);
        v4u af[9];
        f32x4 acc[4], av[2];
        {
            unsigned spins = 0;
            if (s > 0) { hint_wait(rX, w, lane, (unsigned)s); __builtin_amdgcn_s_barrier(); }
            KX_TO_LDS();
            if (s > 0) delay_from(cyc(), (unsigned)a.lead_h);
            const int base = X_MEM + mem_slot(s) * X_MEM_SLOT + lane * 16;
            while (true) {
                CB();
                if (s == 0) {
#pragma unroll
                    for (int f = 0; f < 9; ++f) af[f] = *(const v4u*)(a.mem0 + (c >> 1) * D_ + 64 * f + 16 * g + 8 * (c & 1));
                } else {
#pragma unroll
                    for (int f = 0; f < 9; ++f) af[f] = __builtin_amdgcn_raw_buffer_load_b128(rX, base + (f < 8 ? f * 1024 : 16384), 0, AUX_SC1);
                }
#pragma unroll
                for (int T = 0; T < 4; ++T) acc[T] = zero4();
                asm volatile("s_nop 1" : "+v"(acc[0]), "+v"(acc[1]), "+v"(acc[2]), "+v"(acc[3]));
                unsigned mx = 0;
#pragma unroll
                for (int f = 0; f < 8; ++f) { QK4(f); mx = vmax(mx, af[f]); }
#pragma unroll
                for (int T = 0; T < 4; ++T) SMF_V(acc[T], af[8], WB[T], idx);
                mx = vmax(mx, af[8]);
                if (s == 0 || !__any(mx == ~0u)) break;
                ++nfail;
                __builtin_amdgcn_s_sleep(1);
                if (++spins > SPIN_CAP) { fail = true; break; }
            }
        }
        av[0] = zero4(); av[1] = zero4();
        asm volatile("s_nop 1" : "+v"(av[0]), "+v"(av[1]));
        SMF_V(av[0], af[0], WV[0], idx); SMF_V(av[1], af[0], v1f[0], idx);
        asm volatile("s_nop 3" : "+v"(acc[0]), "+v"(acc[1]), "+v"(acc[2]), "+v"(acc[3]));
        unsigned pq[2], pk[2];
#define PACK(dst, A) { float x0_, x1_; asm volatile("v_add_f32_e32 %0, %1, %2" : "=v"(x0_) : "v"((A)[0]), "v"((A)[1])); asm volatile("v_add_f32_e32 %0, %1, %2" : "=v"(x1_) : "v"((A)[2]), "v"((A)[3])); \
                       asm volatile("v_cvt_pk_bf16_f32 %0, %1, %2" : "=v"(dst) : "v"(x0_), "v"(x1_)); }
        PACK(pq[0], acc[0]); *(LAS unsigned*)(qkw) = pq[0];
        SMF_V(av[0], af[1], WV[1], idx);
        PACK(pq[1], acc[1]); *(LAS unsigned*)(qkw + 256) = pq[1];
        SMF_V(av[1], af[1], v1f[1], idx);
        PACK(pk[0], acc[2]); *(LAS unsigned*)(qkw + 2048) = pk[0];
        SMF_V(av[0], af[2], WV[2], idx);
        PACK(pk[1], acc[3]); *(LAS unsigned*)(qkw + 2048 + 256) = pk[1];
        SB();
        SMF_V(av[1], af[2], v1f[2], idx);
        __builtin_amdgcn_raw_buffer_store_b128(poison4(), rX, s > 0 ? ooff + o_slot(s - 1) * X_O_SLOT : OOB, 0, AUX_SC1);
        SB();
        LDV1(0, 4); LDV1(1, 5); LDV1(2, 6);
        SB();
        SMF_V(av[0], af[3], WV[3], idx); SMF_V(av[1], af[3], v1f[3], idx);
        SMF_V(av[0], af[4], WV[4], idx); SMF_V(av[0], af[5], WV[5], idx);
        SB();
        asm volatile("s_waitcnt lgkmcnt(6)\n\ts_barrier" ::: "memory");
        __builtin_amdgcn_raw_buffer_store_b32((unsigned)(s + 1), rX, hoff, 0, AUX_SC1);
        bf16x8 qf[4], kmf[4], kx[2][4];
#define LDS_SC(ks) { qf[ks] = tr2(qr + (ks) * 512, qr + (ks) * 512 + 64); \
                     kx[0][ks] = *(LAS bf16x8*)(lds + (s & 1) * 8192 + kxr + (((4 * (ks) + g) ^ c) << 4)); \
                     kx[1][ks] = *(LAS bf16x8*)(lds + (s & 1) * 8192 + kxr + 4096 + (((4 * (ks) + g) ^ c) << 4)); \
                     kmf[ks] = tr2(kr + (ks) * 512, kr + (ks) * 512 + 64); }
        LDS_SC(0);
        SB();
        SMF_V(av[1], af[4], v1f[0], idx); SMF_V(av[0], af[6], WV[6], idx);
        SB();
        LDS_SC(1);
        SB();
        f32x4 sp[3];
        { float c0, c1, c2, c3; VMOV(c0, ninf); VMOV(c1, ninf); VMOV(c2, ninf); VMOV(c3, ninf); sp[0] = f32x4{c0, c1, c2, c3}; }
        MFMA32_0(sp[1], kx[0][0], qf[0]); MFMA32_0(sp[2], kx[1][0], qf[0]); MFMA32_ACC(sp[0], kmf[0], qf[0]);
        SB();
        LDS_SC(2);
        LDS_SC(3);
        SB();
        MFMA32_ACC(sp[1], kx[0][1], qf[1]); MFMA32_ACC(sp[2], kx[1][1], qf[1]); MFMA32_ACC(sp[0], kmf[1], qf[1]);
        MFMA32_ACC(sp[1], kx[0][2], qf[2]); MFMA32_ACC(sp[2], kx[1][2], qf[2]); MFMA32_ACC(sp[0], kmf[2], qf[2]);
        MFMA32_ACC(sp[1], kx[0][3], qf[3]); MFMA32_ACC(sp[2], kx[1][3], qf[3]); MFMA32_ACC(sp[0], kmf[3], qf[3]);
        SB();
        LDV1(3, 7); LDV1(0, 8);
        SB();
        SMF_V(av[1], af[5], v1f[1], idx); SMF_V(av[0], af[7], WV[7], idx);
        asm volatile("" : "+v"(sp[0]), "+v"(sp[1]), "+v"(sp[2]));
        unsigned pv[2];
        {
            float m, t, t2;
            asm volatile(
                "v_max3_f32 %0, %5, %6, %7\n\tv_max3_f32 %1, %8, %9, %10\n\tv_max3_f32 %2, %11, %12, %13\n\t"
                "v_max3_f32 %0, %0, %14, %15\n\tv_max3_f32 %1, %1, %2, %16\n\tv_max_f32_e32 %0, %0, %1\n\tv_mov_b32_e32 %1, %0\n\t"
                "v_smfmac_f32_16x16x64_bf16 %3, %17, %18, %21\n\ts_nop 0\n\t"
                "v_permlane16_swap_b32_e32 %0, %1\n\tv_max_f32_e32 %0, %0, %1\n\tv_mov_b32_e32 %1, %0\n\t"
                "v_smfmac_f32_16x16x64_bf16 %4, %19, %20, %21\n\ts_nop 0\n\t"
                "v_permlane32_swap_b32_e32 %0, %1\n\tv_max_f32_e32 %0, %0, %1\n\tv_mul_f32_e32 %0, %22, %0"
                : "=&v"(m), "=&v"(t), "=&v"(t2), "+v"(av[1]), "+v"(av[0])
                : "v"(sp[1][0]), "v"(sp[1][1]), "v"(sp[1][2]), "v"(sp[1][3]), "v"(sp[2][0]), "v"(sp[2][1]), "v"(sp[2][2]), "v"(sp[2][3]), "v"(sp[0][0]),
                  "v"(sp[0][1]), "v"(sp[0][2]), "v"(sp[0][3]), "v"(af[6]), "v"(v1f[2]), "v"(af[8]), "v"(WV[8]), "v"(idx), "s"(SCL));
            const f32x2 ms = {m, m};
            const f32x2 scl2 = {SCL, SCL};
            f32x2 e[6];
            PKFMA(e[0], (f32x2{sp[0][0], sp[0][1]}), scl2, ms); PKFMA(e[1], (f32x2{sp[0][2], sp[0][3]}), scl2, ms);
            PKFMA(e[2], (f32x2{sp[1][0], sp[1][1]}), scl2, ms); PKFMA(e[3], (f32x2{sp[1][2], sp[1][3]}), scl2, ms);
            PKFMA(e[4], (f32x2{sp[2][0], sp[2][1]}), scl2, ms); PKFMA(e[5], (f32x2{sp[2][2], sp[2][3]}), scl2, ms);
            float x[12];
#pragma unroll
            for (int r = 0; r < 6; ++r) { x[2 * r] = e[r][0]; x[2 * r + 1] = e[r][1]; }
            VEXP(x[4]); VEXP(x[5]); VEXP(x[6]); VEXP(x[7]);
            SMF_V(av[1], af[7], v1f[3], idx);
            VEXP(x[8]); VEXP(x[9]); VEXP(x[10]); VEXP(x[11]);
            SMF_V(av[1], af[8], v1f[0], idx);
            VEXP(x[0]); VEXP(x[1]); VEXP(x[2]); VEXP(x[3]);
            v4u pp; v2u pm;
            VCVT(pp[0], x[4], x[5]); VCVT(pp[1], x[6], x[7]); VCVT(pp[2], x[8], x[9]); VCVT(pp[3], x[10], x[11]);
            VCVT(pm[0], x[0], x[1]); VCVT(pm[1], x[2], x[3]);
            v4u ones; { unsigned o_; asm volatile("v_mov_b32_e32 %0, 0x3f803f80" : "=v"(o_)); ones = v4u{o_, o_, o_, o_}; }
            asm volatile("s_nop 1" : "+v"(pp), "+v"(pm), "+v"(ones));
            f32x4 o0, o1, zz;
            MFMA32_0(o0, vx[0], pp); MFMA32_0(o1, vx[1], pp); MFMA32_0(zz, ones, pp);
            asm volatile("" : "+v"(av[0]), "+v"(av[1]));
            float a0, a1, a2, a3;
            VADD(a0, av[0][0], av[0][1]); VADD(a1, av[0][2], av[0][3]); VADD(a2, av[1][0], av[1][1]); VADD(a3, av[1][2], av[1][3]);
            VCVT(pv[0], a0, a1); VCVT(pv[1], a2, a3);
            unsigned u0, u1, w0, w1;
            VMOV(u0, pv[0]); VMOV(u1, pv[0]); VMOV(w0, pv[1]); VMOV(w1, pv[1]);
            NOP(0);
            SWAP16(u0, u1); SWAP16(w0, w1);
            v2u vm0 = {u0, u1}, vm1 = {w0, w1};
            asm volatile("s_nop 1" : "+v"(vm0), "+v"(vm1), "+v"(o0), "+v"(o1), "+v"(zz));
            MFMA16_ACC(zz, (v2u{ones[0], ones[1]}), pm); MFMA16_ACC(o0, vm0, pm); MFMA16_ACC(o1, vm1, pm);
            asm volatile("s_nop 5" : "+v"(zz));
            float rz; asm volatile("v_rcp_f32_e32 %0, %1" : "=v"(rz) : "v"(zz[0]));
            asm volatile("s_nop 1" : "+v"(o0), "+v"(o1), "+v"(rz));
            const f32x2 rz2 = {rz, rz};
            f32x2 n0, n1, n2, n3;
            PKMUL(n0, (f32x2{o0[0], o0[1]}), rz2); PKMUL(n1, (f32x2{o0[2], o0[3]}), rz2); PKMUL(n2, (f32x2{o1[0], o1[1]}), rz2); PKMUL(n3, (f32x2{o1[2], o1[3]}), rz2);
            unsigned d0, d1, e0_, e1;
            VCVT(d0, n0[0], n0[1]); VCVT(d1, n1[0], n1[1]); VCVT(e0_, n2[0], n2[1]); VCVT(e1, n3[0], n3[1]);
            NOP(0);
            SWAP16(d0, e0_); SWAP16(d1, e1);
            NOP(0);
            v4u val = {nosent(d0), nosent(d1), nosent(e0_), nosent(e1)};
            asm volatile("s_waitcnt vmcnt(1)" ::: "memory");
            __builtin_amdgcn_raw_buffer_store_b128(val, rX, s != NSEG - 1 ? ooff + o_slot(s) * X_O_SLOT : OOB, 0, AUX_SC1);
        }
        {
            auto a0 = __builtin_amdgcn_permlane32_swap(pk[0], pk[1], false, false);
            auto a1 = __builtin_amdgcn_permlane32_swap(pv[0], pv[1], false, false);
            auto b0 = __builtin_amdgcn_permlane16_swap(a0[0], a1[0], false, false);
            auto b1 = __builtin_amdgcn_permlane16_swap(a0[1], a1[1], false, false);
            v4u val = {b0[0], b0[1], b1[0], b1[1]};
            const int off = (s * H_ + h) * 4096 + (g & 1) * 2048 + (32 * w + 16 * (g >> 1) + c) * 16;
            __builtin_amdgcn_raw_buffer_store_b128(val, rKVM, off, 0, AUX_SC1);
        }
    }
    asm volatile("s_waitcnt vmcnt(0)\n\ts_barrier" ::: "memory");
    __builtin_amdgcn_raw_buffer_store_b32((unsigned)(NSEG + 1), rX, hoff, 0, AUX_SC1);
    TM(2);
    if (fail && threadIdx.x == 0) __hip_atomic_store(ctl, 1u, RLX_AGENT);
}

constexpr int YL_RED = 0;
constexpr int YL_STAGE = 4096;
DI void y_role(const Params& a, int tp, int rho, LAS unsigned char* lds) {
    const int lane = lane_id(), w = wave_id(), c = lane & 15, g = lane >> 4;
    unsigned char* ws = a.ws;
    gu32* ctl = (gu32*)(ws + WS_CTL);
    const rsrc_t rX = __builtin_amdgcn_make_buffer_rsrc((void*)(a.xr + a.par * 8192), 0, X_BYTES, RSRC3);
    bf16x16 W[16][2];
    {
        LAS unsigned char* st = lds + YL_STAGE + w * 16384;
        const u16* src = a.Wo + (size_t)(1024 * w) * D_ + 32 * tp;
        auto stage = [&](int win, int slot) {
#pragma unroll
            for (int p = 0; p < 4; ++p) {
                const int k = 16 * p + (lane >> 2);
                const int ch = (lane & 3) ^ 0;
                glds16(src + (size_t)(64 * win + k) * D_ + ch * 8, st + slot * 4096 + p * 1024 + lane * 16);
            }
        };
#pragma unroll
        for (int i = 0; i < 3; ++i) stage(i, i);
        const int gb = lane >> 4, q = (lane & 15) >> 2, p = lane & 3;
#pragma unroll
        for (int n = 0; n < 16; ++n) {
            if (n + 3 < 16) stage(n + 3, (n + 3) & 3);
            if (n + 3 < 16) asm volatile("s_waitcnt vmcnt(12)" ::: "memory");
            else if (n + 2 < 16) asm volatile("s_waitcnt vmcnt(8)" ::: "memory");
            else if (n + 1 < 16) asm volatile("s_waitcnt vmcnt(4)" ::: "memory");
            else asm volatile("s_waitcnt vmcnt(0)" ::: "memory");
#pragma unroll
            for (int T = 0; T < 2; ++T) {
                s16x4 r[4];
#pragma unroll
                for (int m = 0; m < 4; ++m) {
                    const int d0 = 32 * (m >> 1) + 16 * (gb >> 1) + 4 * (gb & 1) + 2 * (m & 1);
                    const int k = d0 + (q & 1) + 8 * (q >> 1);
                    r[m] = __builtin_amdgcn_ds_read_tr16_b64_v4i16((LAS s16x4*)(st + (n & 3) * 4096 + k * 64 + (16 * T + 4 * p) * 2));
                }
                typedef short s16x16 __attribute__((ext_vector_type(16)));
                s16x16 v = {r[0][0], r[0][1], r[0][2], r[0][3], r[1][0], r[1][1], r[1][2], r[1][3], r[2][0], r[2][1], r[2][2], r[2][3], r[3][0], r[3][1], r[3][2], r[3][3]};
                W[n][T] = __builtin_bit_cast(bf16x16, v);
                asm volatile("" : "+a"(W[n][T]));
            }
            asm volatile("s_waitcnt lgkmcnt(0)" ::: "memory");
        }
    }
    const int idx = (lane & 1) ? 0xEEEE : 0x4444;
    unsigned nfail = 0;
    bool fail = false;
    TM(1);
    for (int s = 0; s < NSEG - 1; ++s) {
        const int base = X_O + o_slot(s) * X_O_SLOT + 2 * w * 16384 + rho * 1024 + lane * 16;
        unsigned spins = 0;
        hint_wait(rX, w, lane, (unsigned)(s + 1)); delay_from(cyc(), (unsigned)a.lead_y);
        f32x4 D1[2], D2[2];
        while (true) {
            CB();
            v4u v[8];
#pragma unroll
            for (int i = 0; i < 8; ++i) v[i] = __builtin_amdgcn_raw_buffer_load_b128(rX, base + (i >> 2) * 16384 + (i & 3) * 2048, 0, AUX_SC1);
            D1[0] = D1[1] = D2[0] = D2[1] = f32x4{0, 0, 0, 0};
            asm volatile("s_nop 1" : "+v"(D1[0]), "+v"(D1[1]), "+v"(D2[0]), "+v"(D2[1]));
            unsigned mx = 0;
#pragma unroll
            for (int i = 0; i < 8; ++i) {
#pragma unroll
                for (int T = 0; T < 2; ++T) { SMF_A(D1[T], v[i], W[2 * i][T], idx); SMF_A(D2[T], v[i], W[2 * i + 1][T], idx); }
                mx = vmax(mx, v[i]);
            }
            if (!__any(mx == ~0u)) break;
            ++nfail;
            __builtin_amdgcn_s_sleep(1);
            if (++spins > SPIN_CAP) { fail = true; break; }
        }
        asm volatile("s_nop 7" : "+v"(D1[0]), "+v"(D1[1]), "+v"(D2[0]), "+v"(D2[1]));
        *(LAS f32x2*)(lds + YL_RED + (w * 64 + lane) * 8) = f32x2{(D1[0][0] + D1[0][1]) + (D2[0][2] + D2[0][3]), (D1[1][0] + D1[1][1]) + (D2[1][2] + D2[1][3])};
        asm volatile("s_waitcnt lgkmcnt(0)\n\ts_barrier" ::: "memory");
        if (w == 0) {
            const f32x2 t = (*(LAS f32x2*)(lds + YL_RED + lane * 8) + *(LAS f32x2*)(lds + YL_RED + (64 + lane) * 8)) + (*(LAS f32x2*)(lds + YL_RED + (128 + lane) * 8) + *(LAS f32x2*)(lds + YL_RED + (192 + lane) * 8));
            const float x0 = t.x, x1 = t.y;
            const float y0 = u2f(__builtin_amdgcn_update_dpp(0u, f2u(x0), 0xB1, 0xF, 0xF, false)), y1 = u2f(__builtin_amdgcn_update_dpp(0u, f2u(x1), 0xB1, 0xF, 0xF, false));
            const unsigned dw = nosent((c & 1) ? pk2(y1, x1) : pk2(x0, y0));
            const unsigned d1 = __builtin_amdgcn_update_dpp(0u, dw, 0x102, 0xF, 0xF, false), d2 = __builtin_amdgcn_update_dpp(0u, dw, 0x104, 0xF, 0xF, false), d3 = __builtin_amdgcn_update_dpp(0u, dw, 0x106, 0xF, 0xF, false);
            v4u val = {dw, d1, d2, d3};
            const int fo = (tp >> 1) < 8 ? (tp >> 1) * 1024 : 16384;
            const int off = (c & 6) == 0 ? X_MEM + fo + (16 * (2 * (tp & 1) + (c & 1)) + 8 * rho + 2 * g + (c >> 3)) * 16 : 0x7F000000;
            __builtin_amdgcn_raw_buffer_store_b128(val, rX, mem_slot(s + 1) * X_MEM_SLOT + off, 0, AUX_SC1);
            __builtin_amdgcn_raw_buffer_store_b128(poison4(), rX, s > 0 ? mem_slot(s) * X_MEM_SLOT + off : OOB, 0, AUX_SC1);
        }
    }
    TM(2);
    if (fail && threadIdx.x == 0) __hip_atomic_store(ctl, 1u, RLX_AGENT);
}

template <int BM, int BN> struct GemmCfg {
    static constexpr int A_BYTES = BM * 128, B_BYTES = BN * 128, STAGE = A_BYTES + B_BYTES, LDS = 2 * STAGE;
    static constexpr int TM = BM / 64, TN = BN / 64;
};

template <int BM, int BN>
DI void gemm_stage(LAS unsigned char* buf, const u16* __restrict__ A, int lda, const u16* __restrict__ B, int ldb, int k0, int wave, int lane) {
#pragma unroll
    for (int p = 0; p < BM / 32; ++p) {
        const int piece = wave + 4 * p, row = piece * 8 + (lane >> 3), c = (lane & 7) ^ ((row >> 1) & 7);
        glds16(A + (size_t)row * lda + k0 + c * 8, buf + piece * 1024 + lane * 16);
    }
#pragma unroll
    for (int p = 0; p < BN / 32; ++p) {
        const int piece = wave + 4 * p, sub = piece >> 3, k = (piece & 7) * 8 + (lane >> 3), c = (lane & 7) ^ (((k >> 1) & 1) << 2);
        glds16(B + (size_t)(k0 + k) * ldb + sub * 64 + c * 8, buf + BM * 128 + piece * 1024 + lane * 16);
    }
}
template <int BM, int BN, bool SWAP>
DI void gemm_tile(LAS unsigned char* lds, const u16* __restrict__ A, int lda, const u16* __restrict__ B, int ldb, int K,
                  f32x16 (&acc)[BM / 64][BN / 64]) {
    using C = GemmCfg<BM, BN>;
    const int lane = lane_id(), wave = wave_id(), wm = wave >> 1, wn = wave & 1;
#pragma unroll
    for (int i = 0; i < C::TM; ++i)
#pragma unroll
        for (int j = 0; j < C::TN; ++j)
#pragma unroll
            for (int r = 0; r < 16; ++r) acc[i][j][r] = 0.f;
    const int nt = K / 64;
    gemm_stage<BM, BN>(lds, A, lda, B, ldb, 0, wave, lane);
    __syncthreads();
    const int r31 = lane & 31, h = lane >> 5;
    int aoff[C::TM];
#pragma unroll
    for (int i = 0; i < C::TM; ++i) { const int row = wm * (BM / 2) + i * 32 + r31; aoff[i] = row * 128; }
    const int asw = (r31 >> 1) & 7;
    const int blk = (lane >> 4) & 1, q = (lane & 15) >> 2, p = lane & 3;
    for (int t = 0; t < nt; ++t) {
        LAS unsigned char* cur = lds + (t & 1) * C::STAGE;
        if (t + 1 < nt) gemm_stage<BM, BN>(lds + ((t + 1) & 1) * C::STAGE, A, lda, B, ldb, (t + 1) * 64, wave, lane);
#pragma unroll
        for (int ks = 0; ks < 4; ++ks) {
            bf16x8 af[C::TM], bfr[C::TN];
#pragma unroll
            for (int i = 0; i < C::TM; ++i) af[i] = *(LAS bf16x8*)(cur + aoff[i] + (((2 * ks + h) ^ asw) << 4));
#pragma unroll
            for (int j = 0; j < C::TN; ++j) {
                const int n = wn * (BN / 2) + j * 32 + 16 * blk + 4 * p;
                const int sub = n >> 6, cn = n & 63;
                s16x4 lo, hi;
                {
                    const int k = 16 * ks + 8 * h + q;
                    const int ch = (cn >> 3) ^ (((k >> 1) & 1) << 2);
                    lo = __builtin_amdgcn_ds_read_tr16_b64_v4i16((LAS s16x4*)(cur + BM * 128 + sub * 8192 + k * 128 + ch * 16 + (cn & 7) * 2));
                }
                {
                    const int k = 16 * ks + 8 * h + 4 + q;
                    const int ch = (cn >> 3) ^ (((k >> 1) & 1) << 2);
                    hi = __builtin_amdgcn_ds_read_tr16_b64_v4i16((LAS s16x4*)(cur + BM * 128 + sub * 8192 + k * 128 + ch * 16 + (cn & 7) * 2));
                }
                typedef short s16x8 __attribute__((ext_vector_type(8)));
                s16x8 v = {lo[0], lo[1], lo[2], lo[3], hi[0], hi[1], hi[2], hi[3]};
                bfr[j] = __builtin_bit_cast(bf16x8, v);
            }
#pragma unroll
            for (int i = 0; i < C::TM; ++i)
#pragma unroll
                for (int j = 0; j < C::TN; ++j)
                    acc[i][j] = SWAP ? __builtin_amdgcn_mfma_f32_32x32x16_bf16(bfr[j], af[i], acc[i][j], 0, 0, 0)
                                     : __builtin_amdgcn_mfma_f32_32x32x16_bf16(af[i], bfr[j], acc[i][j], 0, 0, 0);
        }
        __syncthreads();
    }
}

__global__ void __launch_bounds__(256, 1) k_sp(const u16* __restrict__ wsp, const u16* __restrict__ bsp, const u16* __restrict__ z2n, const u16* __restrict__ z1, u16* __restrict__ u) {
    extern __shared__ __attribute__((aligned(16))) unsigned char smem[];
    LAS unsigned char* lds = (LAS unsigned char*)smem;
    const int mt = blockIdx.x & 15, nt = blockIdx.x >> 4;
    const int lane = lane_id(), wave = wave_id(), wm = wave >> 1, wn = wave & 1;
    f32x16 acc[2][2];
    gemm_tile<128, 128, true>(lds, wsp + (size_t)mt * 128 * S_, S_, z2n + nt * 128, FH, S_, acc);
#pragma unroll
    for (int i = 0; i < 2; ++i)
#pragma unroll
        for (int j = 0; j < 2; ++j) {
            const int m = mt * 128 + wm * 64 + i * 32 + (lane & 31);
            const float b = bf2f(bsp[m]);
#pragma unroll
            for (int rg = 0; rg < 4; ++rg) {
                const int n = nt * 128 + wn * 64 + j * 32 + 8 * rg + 4 * (lane >> 5);
                const v2u zv = *(const v2u*)(z1 + (size_t)m * FH + n);
                const float zs[4] = {bflo(zv.x), bfhi(zv.x), bflo(zv.y), bfhi(zv.y)};
                float r[4];
#pragma unroll
                for (int e = 0; e < 4; ++e) r[e] = zs[e] * rbf(rbf(acc[i][j][4 * rg + e]) + b);
                v2u o = {pk2(r[0], r[1]), pk2(r[2], r[3])};
                *(v2u*)(u + (size_t)m * FH + n) = o;
            }
        }
}

__global__ void __launch_bounds__(256, 1) k_out(const u16* __restrict__ u, const u16* __restrict__ Wout, const u16* __restrict__ x1, const float* __restrict__ gb, u16* __restrict__ out) {
    extern __shared__ __attribute__((aligned(16))) unsigned char smem[];
    LAS unsigned char* lds = (LAS unsigned char*)smem;
    const int mt = blockIdx.x / 9, nt = blockIdx.x % 9;
    const int lane = lane_id(), wave = wave_id(), wm = wave >> 1, wn = wave & 1;
    f32x16 acc[1][1];
    gemm_tile<64, 64, true>(lds, u + (size_t)mt * 64 * FH, FH, Wout + nt * 64, D_, FH, acc);
    const int m = mt * 64 + wm * 32 + (lane & 31);
#pragma unroll
    for (int rg = 0; rg < 4; ++rg) {
        const int n = nt * 64 + wn * 32 + 8 * rg + 4 * (lane >> 5);
        const v2u xv = *(const v2u*)(x1 + (size_t)m * D_ + n);
        float r[4];
        const float xs[4] = {bflo(xv.x), bfhi(xv.x), bflo(xv.y), bfhi(xv.y)};
#pragma unroll
        for (int e = 0; e < 4; ++e) r[e] = xs[e] + rbf(gb[n + e] * rbf(acc[0][0][4 * rg + e]));
        v2u o = {pk2(r[0], r[1]), pk2(r[2], r[3])};
        *(v2u*)(out + (size_t)m * D_ + n) = o;
    }
}

#define HXN 4
#define YXN 2
#define PLACE 1
constexpr int MAIN_LDS = 160 * 1024;
static_assert(HL_END <= MAIN_LDS, "lds");
__global__ void __launch_bounds__(256, 1) k_main(Params a) {
    extern __shared__ __attribute__((aligned(16))) unsigned char smem[];
    LAS unsigned char* lds = (LAS unsigned char*)smem;
    const int b = blockIdx.x, l = b & 7, r = b >> 3;
    TM(0);
    if (l < 2) { if (r < 16) head_role(a, l * 16 + r, lds); else worker_role(a, 128 + l * 16 + (r - 16), lds); }
    else if (l < 4) { if (r < 18) y_role(a, r, l - 2, lds); else worker_role(a, 160 + (l - 2) * 14 + (r - 18), lds); }
    else worker_role(a, (l - 4) * 32 + r, lds);
}

#define LEAD_H 1500
#define LEAD_Y 550
#define PAR 0
extern "C" void kernel_launch(void* const* d_in, const int* in_sizes, int n_in, void* d_out, int out_size, void* d_ws, size_t ws_size, hipStream_t stream) {
    const u16* x = (const u16*)d_in[0];
    const u16* Wsp = (const u16*)d_in[9];
    const u16* bsp = (const u16*)d_in[10];
    const u16* Wout = (const u16*)d_in[11];
    unsigned char* ws = (unsigned char*)d_ws;
    float* mod = (float*)(ws + WS_MOD);
    unsigned char* xr = (unsigned char*)(((uintptr_t)ws + WS_END + (1u << 20) + (2u << 20) - 1) & ~(uintptr_t)((2u << 20) - 1));
    (void)hipMemsetAsync(ws + WS_CTL, 0, CTL_BYTES, stream);
    (void)hipMemsetAsync(xr, 0xFF, X_HINT, stream);
    (void)hipMemsetAsync(xr + X_HINT, 0, 16384 + 8192, stream);
    constexpr int L128 = GemmCfg<128, 128>::LDS, L64 = GemmCfg<64, 64>::LDS;
    (void)hipFuncSetAttribute((const void*)k_sp, hipFuncAttributeMaxDynamicSharedMemorySize, L128);
    (void)hipFuncSetAttribute((const void*)k_main, hipFuncAttributeMaxDynamicSharedMemorySize, MAIN_LDS);
    Params p;
    p.x = x; p.mem0 = (const u16*)d_in[1]; p.Wq = (const u16*)d_in[2]; p.Wk = (const u16*)d_in[3]; p.Wv = (const u16*)d_in[4]; p.Wo = (const u16*)d_in[5];
    p.Win = (const u16*)d_in[8]; p.c = (const u16*)d_in[12]; p.wmod = (const u16*)d_in[13];
    p.ws = ws; p.xr = xr;
    p.lead_h = LEAD_H; p.lead_y = LEAD_Y;
    p.par = PAR; p.pad = 0;
    k_main<<<256, 256, MAIN_LDS, stream>>>(p);
    k_sp<<<16 * 18, 256, L128, stream>>>(Wsp, bsp, (const u16*)(ws + WS_Z2N), (const u16*)(ws + WS_Z1), (u16*)(ws + WS_U));
    k_out<<<32 * 9, 256, L64, stream>>>((const u16*)(ws + WS_U), Wout, (const u16*)(ws + WS_X1), mod + 5 * D_, (u16*)d_out);
}
```

```cpp
#include <hip/hip_runtime.h>
#include <hip/hip_bf16.h>
#include <stdint.h>

#define GAS __attribute__((address_space(1)))
#define LAS __attribute__((address_space(3)))
#define DI __device__ __forceinline__
typedef unsigned short u16;
typedef __bf16 bf16x2 __attribute__((ext_vector_type(2)));
typedef __bf16 bf16x8 __attribute__((ext_vector_type(8)));
typedef __bf16 bf16x16 __attribute__((ext_vector_type(16)));
typedef short s16x4 __attribute__((ext_vector_type(4)));
typedef float f32x2 __attribute__((ext_vector_type(2)));
typedef float f32x4 __attribute__((ext_vector_type(4)));
typedef float f32x16 __attribute__((ext_vector_type(16)));
typedef unsigned v2u __attribute__((ext_vector_type(2)));
typedef unsigned v4u __attribute__((ext_vector_type(4)));
typedef unsigned v8u __attribute__((ext_vector_type(8)));
typedef GAS unsigned gu32;
typedef __amdgpu_buffer_rsrc_t rsrc_t;
#define RLX_AGENT __ATOMIC_RELAXED, __HIP_MEMORY_SCOPE_AGENT

constexpr int S_ = 2048, D_ = 576, H_ = 32, E_ = 128, HE = 4096, NSEG = 64, FF = 4608, FH = 2304, NRB = 16;
constexpr int AUX_SC1 = 16;
constexpr int RSRC3 = 0x00020000;
constexpr int OOB = 0x7FFFFF00;

constexpr size_t WS_CTL = 0, CTL_BYTES = 131072;
constexpr size_t WS_MOD = CTL_BYTES;
constexpr size_t WS_H1 = WS_MOD + 16384;
constexpr size_t WS_Q = WS_H1 + (size_t)S_ * D_ * 2;
constexpr size_t WS_K = WS_Q + (size_t)S_ * HE * 2;
constexpr size_t WS_VT = WS_K + (size_t)S_ * HE * 2;
constexpr size_t WS_KVM = WS_VT + (size_t)S_ * HE * 2;
constexpr size_t WS_OX = WS_KVM + (size_t)NSEG * H_ * 4096;
constexpr size_t WS_PART = WS_OX + (size_t)S_ * HE * 2;
constexpr size_t WS_X1 = WS_PART + (size_t)4 * S_ * D_ * 4;
constexpr size_t WS_H2 = WS_X1 + (size_t)S_ * D_ * 2;
constexpr size_t WS_Z1 = WS_H2 + (size_t)S_ * D_ * 2;
constexpr size_t WS_Z2 = WS_Z1 + (size_t)S_ * FH * 2;
constexpr size_t WS_Z2N = WS_Z2 + (size_t)S_ * FH * 2;
constexpr size_t WS_U = WS_Z2N + (size_t)S_ * FH * 2;
constexpr size_t WS_END = WS_U + (size_t)S_ * FH * 2;
constexpr int X_MEM = 0, X_MEM_SLOT = 32768;
constexpr int X_O = 3 * X_MEM_SLOT, X_O_SLOT = 131072;
constexpr int X_HINT = X_O + 2 * X_O_SLOT;
constexpr int X_BYTES = X_HINT + 16384;
constexpr int C_TMO = 0, C_MODA = 32, C_MODB = 64, C_H1 = 96, C_OX = 128, C_PART = C_OX + 32 * NRB, C_H2 = C_PART + 32 * NRB, C_Z = C_H2 + 32 * NRB,
              C_KV = 4096, C_Q = C_KV + 16 * NRB * H_, C_END = C_Q + 16 * NRB * H_;
constexpr int C_CFG = 3072, C_PAR = C_CFG + 16;
static_assert(C_Z + 32 * NRB <= 3072 && 3072 + 256 <= C_KV && C_END * 4 <= (int)CTL_BYTES, "control block");

DI unsigned f2u(float x) { return __builtin_bit_cast(unsigned, x); }
DI float u2f(unsigned x) { return __builtin_bit_cast(float, x); }
DI float bf2f(u16 b) { return __builtin_bit_cast(float, (unsigned)b << 16); }
DI float bflo(unsigned w) { return __builtin_bit_cast(float, w << 16); }
DI float bfhi(unsigned w) { return __builtin_bit_cast(float, w & 0xFFFF0000u); }
DI unsigned pk2(float lo, float hi) { f32x2 v = {lo, hi}; bf16x2 b = __builtin_convertvector(v, bf16x2); return __builtin_bit_cast(unsigned, b); }
DI float rbf(float x) { return bflo(pk2(x, 0.f) & 0xFFFFu); }
DI float fadd1(float a, float b) { float r; asm("v_add_f32_e32 %0, %1, %2" : "=v"(r) : "v"(a), "v"(b)); return r; }
DI int lane_id() { return (int)(threadIdx.x & 63); }
DI int wave_id() { return __builtin_amdgcn_readfirstlane((int)(threadIdx.x >> 6)); }

DI f32x4 mfma16(bf16x8 a, bf16x8 b, f32x4 c) { return __builtin_amdgcn_mfma_f32_16x16x32_bf16(a, b, c, 0, 0, 0); }
typedef short s16x4v __attribute__((ext_vector_type(4)));
DI f32x4 mfma16k16(v2u a, v2u b, f32x4 c) { return __builtin_amdgcn_mfma_f32_16x16x16bf16_1k(__builtin_bit_cast(s16x4v, a), __builtin_bit_cast(s16x4v, b), c, 0, 0, 0); }
DI bf16x8 tr2(LAS unsigned char* a0, LAS unsigned char* a1) {
    typedef short s16x8 __attribute__((ext_vector_type(8)));
    const s16x4 lo = __builtin_amdgcn_ds_read_tr16_b64_v4i16((LAS s16x4*)a0), hi = __builtin_amdgcn_ds_read_tr16_b64_v4i16((LAS s16x4*)a1);
    s16x8 v = {lo[0], lo[1], lo[2], lo[3], hi[0], hi[1], hi[2], hi[3]};
    return __builtin_bit_cast(bf16x8, v);
}
DI float xmax16_32(float m) {
    unsigned u = f2u(m);
    auto a = __builtin_amdgcn_permlane16_swap(u, u, false, false);
    m = fmaxf(u2f(a[0]), u2f(a[1]));
    u = f2u(m);
    auto b = __builtin_amdgcn_permlane32_swap(u, u, false, false);
    return fmaxf(u2f(b[0]), u2f(b[1]));
}
DI float xsum16_32(float m) {
    unsigned u = f2u(m);
    auto a = __builtin_amdgcn_permlane16_swap(u, u, false, false);
    m = u2f(a[0]) + u2f(a[1]);
    u = f2u(m);
    auto b = __builtin_amdgcn_permlane32_swap(u, u, false, false);
    return u2f(b[0]) + u2f(b[1]);
}
constexpr float SCALE = 0.08838834764831845f;
constexpr float SCL = 0.08838834764831845f * 1.4426950408889634f;

DI f32x4 smf(bf16x8 a, bf16x16 b, f32x4 c, int idx) { return __builtin_amdgcn_smfmac_f32_16x16x64_bf16(a, b, c, idx, 0, 0); }
DI unsigned nosent(unsigned x) { return x < 0xFFFFFFFEu ? x : 0xFFFFFFFEu; }
DI unsigned umax3(unsigned a, unsigned b, unsigned c) { unsigned r; asm("v_max3_u32 %0, %1, %2, %3" : "=v"(r) : "v"(a), "v"(b), "v"(c)); return r; }
DI unsigned vmax(unsigned acc, v4u v) { return umax3(umax3(acc, v.x, v.y), v.z, v.w); }
DI bool all_valid(v4u v) { return (int)(v.x != ~0u) & (int)(v.y != ~0u) & (int)(v.z != ~0u) & (int)(v.w != ~0u); }
#define CB() asm volatile("" ::: "memory")
#define SMF_A(acc, a, wt, idx) asm volatile("v_smfmac_f32_16x16x64_bf16 %0, %1, %2, %3" : "+v"(acc) : "v"(a), "a"(wt), "v"(idx))
#define SMF_V(acc, a, wt, idx) asm volatile("v_smfmac_f32_16x16x64_bf16 %0, %1, %2, %3" : "+v"(acc) : "v"(a), "v"(wt), "v"(idx))
#define MFMA32_0(d, a, b) asm volatile("v_mfma_f32_16x16x32_bf16 %0, %1, %2, 0" : "=&v"(d) : "v"(a), "v"(b))
#define MFMA32_C(d, a, b, cin) asm volatile("v_mfma_f32_16x16x32_bf16 %0, %1, %2, %3" : "=&v"(d) : "v"(a), "v"(b), "v"(cin))
#define MFMA16_0(d, a, b) asm volatile("v_mfma_f32_16x16x16_bf16 %0, %1, %2, 0" : "=&v"(d) : "v"(a), "v"(b))
#define VMAX3(d, a, b, c) asm volatile("v_max3_f32 %0, %1, %2, %3" : "=v"(d) : "v"(a), "v"(b), "v"(c))
#define VMAX(d, a, b) asm volatile("v_max_f32_e32 %0, %1, %2" : "=v"(d) : "v"(a), "v"(b))
#define VMOV(d, a) asm volatile("v_mov_b32_e32 %0, %1" : "=v"(d) : "v"(a))
#define VADD(d, a, b) asm volatile("v_add_f32_e32 %0, %1, %2" : "=v"(d) : "v"(a), "v"(b))
#define VCVT(d, a, b) asm volatile("v_cvt_pk_bf16_f32 %0, %1, %2" : "=v"(d) : "v"(a), "v"(b))
#define VEXP(x) asm volatile("v_exp_f32_e32 %0, %0" : "+v"(x))
#define SWAP16(a, b) asm volatile("v_permlane16_swap_b32_e32 %0, %1" : "+v"(a), "+v"(b))
#define SWAP32(a, b) asm volatile("v_permlane32_swap_b32_e32 %0, %1" : "+v"(a), "+v"(b))
#define PKFMA(d, a, sc, ms) asm volatile("v_pk_fma_f32 %0, %1, %2, %3 op_sel_hi:[1,0,0] neg_lo:[0,0,1] neg_hi:[0,0,1]" : "=v"(d) : "v"(a), "s"(sc), "v"(ms))
#define PKMUL(d, a, r) asm volatile("v_pk_mul_f32 %0, %1, %2 op_sel_hi:[1,0]" : "=v"(d) : "v"(a), "v"(r))
DI f32x4 zero4() { f32x2 a, b; asm volatile("v_mov_b64 %0, 0" : "=v"(a)); asm volatile("v_mov_b64 %0, 0" : "=v"(b)); return f32x4{a[0], a[1], b[0], b[1]}; }
DI v4u poison4() { v2u a, b; asm volatile("v_mov_b64 %0, -1" : "=v"(a)); asm volatile("v_mov_b64 %0, -1" : "=v"(b)); return v4u{a[0], a[1], b[0], b[1]}; }
#define NOP(n) asm volatile("s_nop " #n)
#define MFMA32_ACC(d, a, b) asm volatile("v_mfma_f32_16x16x32_bf16 %0, %1, %2, %0" : "+v"(d) : "v"(a), "v"(b))
#define MFMA16_ACC(d, a, b) asm volatile("v_mfma_f32_16x16x16_bf16 %0, %1, %2, %0" : "+v"(d) : "v"(a), "v"(b))

struct Params {
    const u16* x; const u16* mem0; const u16* Wq; const u16* Wk; const u16* Wv; const u16* Wo;
    const u16* Win; const u16* c; const u16* wmod;
    unsigned char* ws;
    unsigned char* xr;
    int lead_h, lead_y;
};
DI rsrc_t mk_rsrc(const void* p, unsigned bytes) { return __builtin_amdgcn_make_buffer_rsrc((void*)p, 0, (int)bytes, RSRC3); }
DI void glds16(const void* g, LAS unsigned char* l) { __builtin_amdgcn_global_load_lds((const GAS unsigned*)g, (LAS unsigned*)l, 16, 0, 0); }
DI unsigned long long cyc() { unsigned long long t; asm volatile("s_memtime %0\n\ts_waitcnt lgkmcnt(0)" : "=s"(t) :: "memory"); return t; }
#define TM(slot)

constexpr unsigned WAIT_CAP = 1u << 16;
DI gu32* ctl_word(const Params& a, int idx) { return (gu32*)(a.ws + WS_CTL) + idx; }
DI void wg_signal(const Params& a, int idx) {
    asm volatile("s_waitcnt vmcnt(0)" ::: "memory");
    __syncthreads();
    if (threadIdx.x == 0) (void)__hip_atomic_fetch_add(ctl_word(a, idx), 1u, RLX_AGENT);
}
DI bool poll_ge(const Params& a, gu32* word, unsigned want) {
    for (unsigned spins = 0;; ++spins) {
        if (__hip_atomic_load(word, RLX_AGENT) >= want) return true;
        __builtin_amdgcn_s_sleep(4);
        if (spins > WAIT_CAP) { __hip_atomic_store(ctl_word(a, C_TMO), 1u, RLX_AGENT); return false; }
        if ((spins & 63u) == 63u && __hip_atomic_load(ctl_word(a, C_TMO), RLX_AGENT) != 0u) return false;
    }
}
template <bool ACQ> DI void wg_wait(const Params& a, gu32* word, unsigned want) {
    if (threadIdx.x == 0) {
        (void)poll_ge(a, word, want);
        if (ACQ) { __builtin_amdgcn_fence(__ATOMIC_ACQUIRE, "agent"); asm volatile("s_waitcnt vmcnt(0)" ::: "memory"); }
        else __builtin_amdgcn_fence(__ATOMIC_ACQUIRE, "wavefront");
    }
    __syncthreads();
}
template <int BM, int BN, int NST> struct GP {
    static constexpr int A_BYTES = BM * 128, B_BYTES = BN * 128, STAGE = A_BYTES + B_BYTES, LDS = NST * STAGE;
    static constexpr int TM = BM / 64, TN = BN / 64, PA = BM / 32, PB = BN / 32, LPS = PA + PB;
    static constexpr int p0(int ks) { return ks * LPS / 4; }
};
template <bool SC1> DI void dma16(unsigned m0v, int voff, rsrc_t r, int soff) {
    if constexpr (SC1) asm volatile("s_mov_b32 m0, %0\n\ts_nop 0\n\tbuffer_load_dwordx4 %1, %2, %3 offen sc1 lds" :: "s"(m0v), "v"(voff), "s"(r), "s"(soff) : "memory");
    else asm volatile("s_mov_b32 m0, %0\n\ts_nop 0\n\tbuffer_load_dwordx4 %1, %2, %3 offen lds" :: "s"(m0v), "v"(voff), "s"(r), "s"(soff) : "memory");
}
template <int N> DI void wait_vm() { asm volatile("s_waitcnt vmcnt(%0)" :: "i"(N) : "memory"); }
#define GSB() __builtin_amdgcn_sched_barrier(0)

template <int BM, int BN, int NST, bool ASC1 = false> struct GemmStream {
    using C = GP<BM, BN, NST>;
    LAS unsigned char* lds; unsigned lbase; int wave, ldb128;
    int voA[C::PA], voB[C::PB];
    int aoff, asw, h, boff[C::TN];
    int slot;
    DI void init(LAS unsigned char* l, int lda, int ldb) {
        lds = l; lbase = (unsigned)(size_t)l; slot = 0; ldb128 = 128 * ldb;
        const int lane = lane_id(); wave = wave_id();
        const int wm = wave >> 1, wn = wave & 1;
#pragma unroll
        for (int p = 0; p < C::PA; ++p) { const int piece = wave + 4 * p, row = piece * 8 + (lane >> 3), c = (lane & 7) ^ ((row >> 1) & 7); voA[p] = (row * lda + c * 8) * 2; }
#pragma unroll
        for (int p = 0; p < C::PB; ++p) { const int piece = wave + 4 * p, sub = piece >> 3, k = (piece & 7) * 8 + (lane >> 3), c = (lane & 7) ^ (((k >> 1) & 1) << 2); voB[p] = (k * ldb + sub * 64 + c * 8) * 2; }
        const int r31 = lane & 31; h = lane >> 5; asw = (r31 >> 1) & 7; aoff = (wm * (BM / 2) + r31) * 128;
        const int blk = (lane >> 4) & 1, q = (lane & 15) >> 2, p = lane & 3;
#pragma unroll
        for (int j = 0; j < C::TN; ++j) { const int n = wn * (BN / 2) + j * 32 + 16 * blk + 4 * p, sub = n >> 6, cn = n & 63; boff[j] = C::A_BYTES + sub * 8192 + (8 * h + q) * 128 + (((cn >> 3) ^ (((q >> 1) & 1) << 2)) << 4) + (cn & 7) * 2; }
    }
    template <int I> DI void piece(rsrc_t rA, rsrc_t rB, int t, int sl) {
        const unsigned sb = lbase + sl * C::STAGE;
        if constexpr (I < C::PA) dma16<ASC1>(sb + (wave + 4 * I) * 1024, voA[I], rA, t * 128);
        else dma16<false>(sb + C::A_BYTES + (wave + 4 * (I - C::PA)) * 1024, voB[I - C::PA], rB, t * ldb128);
    }
    template <int I0, int N> DI void pieces(rsrc_t rA, rsrc_t rB, int t, int sl) {
        if constexpr (N > 0) { piece<I0>(rA, rB, t, sl); pieces<I0 + 1, N - 1>(rA, rB, t, sl); }
    }
    DI void prime(rsrc_t rA, rsrc_t rB) {
#pragma unroll
        for (int t = 0; t < NST - 1; ++t) { int sl = slot + t; if (sl >= NST) sl -= NST; pieces<0, C::LPS>(rA, rB, t, sl); }
    }
    DI void primeB(rsrc_t rB) {
#pragma unroll
        for (int t = 0; t < NST - 1; ++t) { int sl = slot + t; if (sl >= NST) sl -= NST; pieces<C::PA, C::PB>(rB, rB, t, sl); }
    }
    DI void primeA(rsrc_t rA) {
#pragma unroll
        for (int t = 0; t < NST - 1; ++t) { int sl = slot + t; if (sl >= NST) sl -= NST; pieces<0, C::PA>(rA, rA, t, sl); }
    }
    DI void ldfrags(LAS unsigned char* cur, int ks, bf16x8 (&af)[C::TM], bf16x8 (&bf)[C::TN]) {
#pragma unroll
        for (int i = 0; i < C::TM; ++i) af[i] = *(LAS bf16x8*)(cur + aoff + i * 4096 + (((2 * ks + h) ^ asw) << 4));
#pragma unroll
        for (int j = 0; j < C::TN; ++j) bf[j] = tr2(cur + boff[j] + ks * 2048, cur + boff[j] + ks * 2048 + 512);
    }
    static constexpr int NM = C::TM * C::TN;
    template <int K, int P0> DI void job(LAS unsigned char* src, int ksn, bool rd, bf16x8 (&an)[C::TM], bf16x8 (&bn)[C::TN], bool issue, rsrc_t sA, rsrc_t sB, int ts, int sp) {
        if constexpr (K < C::TN) { if (rd) bn[K] = tr2(src + boff[K] + ksn * 2048, src + boff[K] + ksn * 2048 + 512); }
        else if constexpr (K < C::TN + C::TM) { if (rd) an[K - C::TN] = *(LAS bf16x8*)(src + aoff + (K - C::TN) * 4096 + (((2 * ksn + h) ^ asw) << 4)); }
        else { if (issue) piece<P0 + K - C::TN - C::TM>(sA, sB, ts, sp); }
    }
    template <int M, int K, int P0, int NJ> DI void jobs_after(LAS unsigned char* src, int ksn, bool rd, bf16x8 (&an)[C::TM], bf16x8 (&bn)[C::TN], bool issue, rsrc_t sA, rsrc_t sB, int ts, int sp) {
        if constexpr (K < NJ) {
            if constexpr (K * NM / NJ == M) job<K, P0>(src, ksn, rd, an, bn, issue, sA, sB, ts, sp);
            jobs_after<M, K + 1, P0, NJ>(src, ksn, rd, an, bn, issue, sA, sB, ts, sp);
        }
    }
    template <int M, int KS, bool SWAP> DI void kstep(const bf16x8 (&ac)[C::TM], const bf16x8 (&bc)[C::TN], f32x16 (&acc)[C::TM][C::TN], LAS unsigned char* src, int ksn, bool rd,
                                           bf16x8 (&an)[C::TM], bf16x8 (&bn)[C::TN], bool issue, rsrc_t sA, rsrc_t sB, int ts, int sp) {
        if constexpr (M < NM) {
            constexpr int i = M / C::TN, j = M % C::TN;
            acc[i][j] = SWAP ? __builtin_amdgcn_mfma_f32_32x32x16_bf16(bc[j], ac[i], acc[i][j], 0, 0, 0) : __builtin_amdgcn_mfma_f32_32x32x16_bf16(ac[i], bc[j], acc[i][j], 0, 0, 0);
            GSB();
            jobs_after<M, 0, C::p0(KS), C::TM + C::TN + C::p0(KS + 1) - C::p0(KS)>(src, ksn, rd, an, bn, issue, sA, sB, ts, sp);
            GSB();
            kstep<M + 1, KS, SWAP>(ac, bc, acc, src, ksn, rd, an, bn, issue, sA, sB, ts, sp);
        }
    }
    template <int EXTRA, bool SWAP, bool SPLITP = false>
    DI void tile(rsrc_t rA, rsrc_t rB, rsrc_t nA, rsrc_t nB, bool has_next, int nt, f32x16 (&acc)[C::TM][C::TN]) {
#pragma unroll
        for (int i = 0; i < C::TM; ++i)
#pragma unroll
            for (int j = 0; j < C::TN; ++j)
#pragma unroll
                for (int r = 0; r < 16; ++r) acc[i][j][r] = 0.f;
        bf16x8 a0[C::TM], b0[C::TN], a1[C::TM], b1[C::TN];
        if constexpr (SPLITP) wait_vm<(NST - 2) * C::PA>(); else wait_vm<(NST - 2) * C::LPS + EXTRA>();
        asm volatile("s_barrier" ::: "memory");
        ldfrags(lds + slot * C::STAGE, 0, a0, b0);
        for (int t = 0; t < nt; ++t) {
            LAS unsigned char* cur = lds + slot * C::STAGE;
            int sn = slot + 1; if (sn >= NST) sn -= NST;
            int sp = slot + NST - 1; if (sp >= NST) sp -= NST;
            const int tn = t + NST - 1;
            const bool in_cur = tn < nt, issue = in_cur || has_next;
            const rsrc_t sA = in_cur ? rA : nA, sB = in_cur ? rB : nB;
            const int ts = in_cur ? tn : tn - nt;
            kstep<0, 0, SWAP>(a0, b0, acc, cur, 1, true, a1, b1, issue, sA, sB, ts, sp);
            kstep<0, 1, SWAP>(a1, b1, acc, cur, 2, true, a0, b0, issue, sA, sB, ts, sp);
            kstep<0, 2, SWAP>(a0, b0, acc, cur, 3, true, a1, b1, issue, sA, sB, ts, sp);
            if (t + 1 < nt) {
                if (!issue) wait_vm<0>();
                else if (t + 1 <= NST - 2) { if constexpr (SPLITP) { if (t == 0) wait_vm<(NST - 3) * C::PA + C::p0(3)>(); else wait_vm<C::p0(3)>(); } else wait_vm<(NST - 3) * C::LPS + C::p0(3) + EXTRA>(); }
                else wait_vm<(NST - 3) * C::LPS + C::p0(3)>();
            }
            asm volatile("s_waitcnt lgkmcnt(0)\n\ts_barrier" ::: "memory");
            kstep<0, 3, SWAP>(a1, b1, acc, lds + sn * C::STAGE, 0, t + 1 < nt, a0, b0, issue, sA, sB, ts, sp);
            slot = sn;
        }
    }
};

DI unsigned wave_poll_ge(const Params& a, gu32* word, unsigned want) {
    unsigned v;
    for (unsigned spins = 0;; ++spins) {
        v = (unsigned)__builtin_amdgcn_readfirstlane((int)__hip_atomic_load(word, RLX_AGENT));
        if (v >= want) break;
        __builtin_amdgcn_s_sleep(4);
        if (spins > WAIT_CAP) { __hip_atomic_store(ctl_word(a, C_TMO), 1u, RLX_AGENT); break; }
        if ((spins & 63u) == 63u && __builtin_amdgcn_readfirstlane((int)__hip_atomic_load(ctl_word(a, C_TMO), RLX_AGENT)) != 0) break;
    }
    __builtin_amdgcn_fence(__ATOMIC_ACQUIRE, "wavefront");
    return v;
}
DI int get_par(const Params& a) { return (int)(wave_poll_ge(a, ctl_word(a, C_PAR + (blockIdx.x & 7) * 32), 0x100u) & 1u); }
DI float wsum8(float v) { v += __shfl_xor(v, 1); v += __shfl_xor(v, 2); v += __shfl_xor(v, 4); return v; }
DI void unpack8(v4u w, float (&f)[8]) { f[0] = bflo(w.x); f[1] = bfhi(w.x); f[2] = bflo(w.y); f[3] = bfhi(w.y); f[4] = bflo(w.z); f[5] = bfhi(w.z); f[6] = bflo(w.w); f[7] = bfhi(w.w); }

DI void task_mod(const Params& a, int task, LAS unsigned char* lds) {
    LAS float* red = (LAS float*)lds;
    const int tid = threadIdx.x, cg = tid & 3, kr = tid >> 2;
    const int col0 = task * 32 + cg * 8;
    float acc[8];
#pragma unroll
    for (int j = 0; j < 8; ++j) acc[j] = 0.f;
    v4u w[9];
#pragma unroll
    for (int p = 0; p < 9; ++p) w[p] = *(const v4u*)(a.wmod + (size_t)(kr + 64 * p) * 3456 + col0);
#pragma unroll
    for (int p = 0; p < 9; ++p) {
        const float cv = bf2f(a.c[kr + 64 * p]);
        const float sv = rbf(cv / (1.f + __expf(-cv)));
        float f[8]; unpack8(w[p], f);
#pragma unroll
        for (int j = 0; j < 8; ++j) acc[j] += sv * f[j];
    }
#pragma unroll
    for (int j = 0; j < 8; ++j) red[kr * 33 + cg * 8 + j] = acc[j];
    __syncthreads();
    if (tid < 32) {
        float s = 0.f;
        for (int r = 0; r < 64; ++r) s += red[r * 33 + tid];
        __hip_atomic_store((gu32*)(a.ws + WS_MOD) + task * 32 + tid, f2u(rbf(s)), RLX_AGENT);
    }
}
DI void load_modtab(const Params& a, LAS float* tab, int i_shift, int i_scale, int i_gate) {
    const rsrc_t rM = mk_rsrc(a.ws + WS_MOD, 3456 * 4);
    for (int i = threadIdx.x; i < D_; i += 256) {
        tab[i] = rbf(1.f + u2f(__builtin_amdgcn_raw_buffer_load_b32(rM, (i_scale * D_ + i) * 4, 0, AUX_SC1)));
        tab[D_ + i] = u2f(__builtin_amdgcn_raw_buffer_load_b32(rM, (i_shift * D_ + i) * 4, 0, AUX_SC1));
        tab[2 * D_ + i] = i_gate >= 0 ? u2f(__builtin_amdgcn_raw_buffer_load_b32(rM, (i_gate * D_ + i) * 4, 0, AUX_SC1)) : 0.f;
    }
    __syncthreads();
}
template <bool RES>
DI void task_ln(const Params& a, int row0, LAS float* tab, size_t out_off) {
    const int lane = lane_id(), w = wave_id();
    const int row = row0 + 8 * w + (lane >> 3), sub = lane & 7;
    float v[9][8];
#pragma unroll
    for (int i = 0; i < 9; ++i) unpack8(*(const v4u*)(a.x + (size_t)row * D_ + (sub + 8 * i) * 8), v[i]);
    if (RES) {
        const rsrc_t rP = mk_rsrc(a.ws + WS_PART + (size_t)(row >> 7) * 4 * 128 * D_ * 4, 4 * 128 * D_ * 4);
        const rsrc_t rX1 = mk_rsrc(a.ws + WS_X1, S_ * D_ * 2);
#pragma unroll
        for (int i = 0; i < 9; ++i) {
            float s[8];
#pragma unroll
            for (int j = 0; j < 8; ++j) s[j] = 0.f;
#pragma unroll
            for (int kq = 0; kq < 4; ++kq) {
                const int off = ((kq * 128 + (row & 127)) * D_ + (sub + 8 * i) * 8) * 4;
                const v4u p0 = __builtin_amdgcn_raw_buffer_load_b128(rP, off, 0, AUX_SC1), p1 = __builtin_amdgcn_raw_buffer_load_b128(rP, off + 16, 0, AUX_SC1);
                s[0] += u2f(p0.x); s[1] += u2f(p0.y); s[2] += u2f(p0.z); s[3] += u2f(p0.w); s[4] += u2f(p1.x); s[5] += u2f(p1.y); s[6] += u2f(p1.z); s[7] += u2f(p1.w);
            }
#pragma unroll
            for (int j = 0; j < 8; ++j) v[i][j] = rbf(v[i][j] + rbf(tab[2 * D_ + (sub + 8 * i) * 8 + j] * rbf(s[j])));
            const v4u o = {pk2(v[i][0], v[i][1]), pk2(v[i][2], v[i][3]), pk2(v[i][4], v[i][5]), pk2(v[i][6], v[i][7])};
            __builtin_amdgcn_raw_buffer_store_b128(o, rX1, (row * D_ + (sub + 8 * i) * 8) * 2, 0, AUX_SC1);
        }
    }
    float s = 0.f;
#pragma unroll
    for (int i = 0; i < 9; ++i)
#pragma unroll
        for (int j = 0; j < 8; ++j) s += v[i][j];
    const float mean = wsum8(s) * (1.f / D_);
    float q = 0.f;
#pragma unroll
    for (int i = 0; i < 9; ++i)
#pragma unroll
        for (int j = 0; j < 8; ++j) { const float d = v[i][j] - mean; q += d * d; }
    const float meanb = rbf(mean), rden = 1.f / rbf(sqrtf(rbf(rbf(wsum8(q) * (1.f / D_)) + 1e-5f)));
    const rsrc_t rO = mk_rsrc(a.ws + out_off, S_ * D_ * 2);
#pragma unroll
    for (int i = 0; i < 9; ++i) {
        float o[8];
#pragma unroll
        for (int j = 0; j < 8; ++j) { const int cc = (sub + 8 * i) * 8 + j; o[j] = rbf(rbf(rbf(v[i][j] - meanb) * rden) * tab[cc]) + tab[D_ + cc]; }
        const v4u ov = {pk2(o[0], o[1]), pk2(o[2], o[3]), pk2(o[4], o[5]), pk2(o[6], o[7])};
        __builtin_amdgcn_raw_buffer_store_b128(ov, rO, (row * D_ + (sub + 8 * i) * 8) * 2, 0, AUX_SC1);
    }
}
DI float wsum64(float v) {
#pragma unroll
    for (int m = 1; m < 64; m <<= 1) v += __shfl_xor(v, m);
    return v;
}
template <int NR>
DI void task_ln2(const Params& a, int row0, LAS float* tab) {
    const int lane = lane_id(), w = wave_id();
    const rsrc_t rX1 = mk_rsrc(a.ws + WS_X1, S_ * D_ * 2), rH2 = mk_rsrc(a.ws + WS_H2, S_ * D_ * 2);
    const bool act = lane < 48;
    v4u p[NR][3][4]; v2u xv[NR][3];
#pragma unroll
    for (int it = 0; it < NR; ++it) {
        const int row = row0 + w * NR + it;
        const rsrc_t rP = mk_rsrc(a.ws + WS_PART + (size_t)(row >> 7) * 4 * 128 * D_ * 4, 4 * 128 * D_ * 4);
#pragma unroll
        for (int i = 0; i < 3; ++i) {
            const int col = (lane + 48 * i) * 4;
            xv[it][i] = act ? *(const v2u*)(a.x + (size_t)row * D_ + col) : v2u{0u, 0u};
#pragma unroll
            for (int kq = 0; kq < 4; ++kq) p[it][i][kq] = __builtin_amdgcn_raw_buffer_load_b128(rP, act ? ((kq * 128 + (row & 127)) * D_ + col) * 4 : OOB, 0, AUX_SC1);
        }
    }
#pragma unroll
    for (int it = 0; it < NR; ++it) {
        const int row = row0 + w * NR + it;
        float v[3][4]; float s = 0.f;
#pragma unroll
        for (int i = 0; i < 3; ++i) {
            const int col = act ? (lane + 48 * i) * 4 : 0;
            const float xs[4] = {bflo(xv[it][i].x), bfhi(xv[it][i].x), bflo(xv[it][i].y), bfhi(xv[it][i].y)};
#pragma unroll
            for (int j = 0; j < 4; ++j) {
                const float t = (u2f(p[it][i][0][j]) + u2f(p[it][i][1][j])) + (u2f(p[it][i][2][j]) + u2f(p[it][i][3][j]));
                v[i][j] = act ? rbf(xs[j] + rbf(tab[2 * D_ + col + j] * rbf(t))) : 0.f;
                s += v[i][j];
            }
        }
        const float mean = wsum64(s) * (1.f / D_);
        float q = 0.f;
#pragma unroll
        for (int i = 0; i < 3; ++i)
#pragma unroll
            for (int j = 0; j < 4; ++j) { const float d = act ? v[i][j] - mean : 0.f; q += d * d; }
        const float meanb = rbf(mean), rden = 1.f / rbf(sqrtf(rbf(rbf(wsum64(q) * (1.f / D_)) + 1e-5f)));
#pragma unroll
        for (int i = 0; i < 3; ++i) {
            const int col = act ? (lane + 48 * i) * 4 : 0;
            float o[4];
#pragma unroll
            for (int j = 0; j < 4; ++j) o[j] = rbf(rbf(rbf(v[i][j] - meanb) * rden) * tab[col + j]) + tab[D_ + col + j];
            const unsigned a0 = pk2(v[i][0], v[i][1]), a1 = pk2(v[i][2], v[i][3]), b0 = pk2(o[0], o[1]), b1 = pk2(o[2], o[3]);
            const v4u xo = {a0, a1, (unsigned)__shfl_down((int)a0, 1), (unsigned)__shfl_down((int)a1, 1)}, ho = {b0, b1, (unsigned)__shfl_down((int)b0, 1), (unsigned)__shfl_down((int)b1, 1)};
            const int off = (act && (lane & 1) == 0) ? (row * D_ + col) * 2 : OOB;
            __builtin_amdgcn_raw_buffer_store_b128(xo, rX1, off, 0, AUX_SC1);
            __builtin_amdgcn_raw_buffer_store_b128(ho, rH2, off, 0, AUX_SC1);
        }
    }
}
template <int NIT>
DI void task_ln3(const Params& a, int row0) {
    const int lane = lane_id(), w = wave_id(), sub = lane & 31;
    const rsrc_t rZ = mk_rsrc(a.ws + WS_Z2, S_ * FH * 2), rN = mk_rsrc(a.ws + WS_Z2N, S_ * FH * 2);
    v4u raw[NIT][9];
#pragma unroll
    for (int it = 0; it < NIT; ++it)
#pragma unroll
        for (int i = 0; i < 9; ++i) raw[it][i] = __builtin_amdgcn_raw_buffer_load_b128(rZ, ((row0 + 2 * NIT * w + 2 * it + (lane >> 5)) * FH + (sub + 32 * i) * 8) * 2, 0, AUX_SC1);
#pragma unroll
    for (int it = 0; it < NIT; ++it) {
        const int row = row0 + 2 * NIT * w + 2 * it + (lane >> 5);
        float v[9][8];
#pragma unroll
        for (int i = 0; i < 9; ++i) unpack8(raw[it][i], v[i]);
        float s = 0.f;
#pragma unroll
        for (int i = 0; i < 9; ++i)
#pragma unroll
            for (int j = 0; j < 8; ++j) s += v[i][j];
#pragma unroll
        for (int m = 1; m < 32; m <<= 1) s += __shfl_xor(s, m);
        const float mean = s * (1.f / FH);
        float q = 0.f;
#pragma unroll
        for (int i = 0; i < 9; ++i)
#pragma unroll
            for (int j = 0; j < 8; ++j) { const float d = v[i][j] - mean; q += d * d; }
#pragma unroll
        for (int m = 1; m < 32; m <<= 1) q += __shfl_xor(q, m);
        const float meanb = rbf(mean), rden = 1.f / rbf(sqrtf(rbf(rbf(q * (1.f / FH)) + 1e-5f)));
#pragma unroll
        for (int i = 0; i < 9; ++i) {
            float o[8];
#pragma unroll
            for (int j = 0; j < 8; ++j) o[j] = rbf(v[i][j] - meanb) * rden;
            const v4u ov = {pk2(o[0], o[1]), pk2(o[2], o[3]), pk2(o[4], o[5]), pk2(o[6], o[7])};
            __builtin_amdgcn_raw_buffer_store_b128(ov, rN, (row * FH + (sub + 32 * i) * 8) * 2, 0, AUX_SC1);
        }
    }
}

constexpr int EPI_STORES = 8;
template <class F> DI void epi_tile128(const f32x16 (&acc)[2][2], LAS unsigned char* img, bool swapped, rsrc_t rO, int dst, int rstride, F&& f) {
    const int lane = lane_id(), wave = wave_id(), wm = wave >> 1, wn = wave & 1;
#pragma unroll
    for (int i = 0; i < 2; ++i)
#pragma unroll
        for (int j = 0; j < 2; ++j)
#pragma unroll
            for (int rg = 0; rg < 4; ++rg) {
                int row, col;
                if (swapped) { row = wm * 64 + i * 32 + (lane & 31); col = wn * 64 + j * 32 + 8 * rg + 4 * (lane >> 5); }
                else {
                    row = wn * 64 + j * 32 + (lane & 31);
                    const int tk = 8 * rg + 4 * (lane >> 5);
                    col = wm * 64 + i * 32 + (tk < 16 ? 2 * tk : 2 * (tk - 16) + 4);
                }
                const v2u o = {pk2(f(acc[i][j][4 * rg]), f(acc[i][j][4 * rg + 1])), pk2(f(acc[i][j][4 * rg + 2]), f(acc[i][j][4 * rg + 3]))};
                *(LAS v2u*)(img + row * 256 + ((((col >> 3) ^ (row & 7)) << 4) | ((col & 4) << 1))) = o;
            }
    asm volatile("s_waitcnt lgkmcnt(0)\n\ts_barrier" ::: "memory");
#pragma unroll
    for (int it = 0; it < EPI_STORES; ++it) {
        const int id = it * 256 + threadIdx.x, row = id >> 4, ch = id & 15;
        const v4u v = *(LAS v4u*)(img + row * 256 + ((ch ^ (row & 7)) << 4));
        __builtin_amdgcn_raw_buffer_store_b128(v, rO, dst + row * rstride + ch * 16, 0, AUX_SC1);
    }
    asm volatile("s_waitcnt lgkmcnt(0)\n\ts_barrier" ::: "memory");
}

DI float gelu_tanh(float x) {
    const float t = x * (1.f + 0.044715f * x * x);
    return x * __builtin_amdgcn_rcpf(1.f + __builtin_amdgcn_exp2f(t * (-2.f * 0.7978845608028654f * 1.4426950408889634f)));
}

DI void qkv_decode(int t, int& rb, int& head, int& mat) {
    if (t < 2 * NRB * H_) { rb = t >> 6; head = (t >> 1) & 31; mat = 1 + (t & 1); }
    else { const int u = t - 2 * NRB * H_; rb = u >> 5; head = u & 31; mat = 0; }
}
DI void phase_qkv(const Params& a, int j, int nw, LAS unsigned char* lds) {
    using GS = GemmStream<128, 128, 3, true>;
    GS gs; gs.init(lds, D_, HE);
    LAS unsigned char* img = lds + GS::C::LDS;
    constexpr int NT = 3 * NRB * H_;
    auto rA = [&](int t) { int rb, h, m; qkv_decode(t, rb, h, m); return mk_rsrc(a.ws + WS_H1 + (size_t)rb * 128 * D_ * 2, 128 * D_ * 2); };
    auto rB = [&](int t) { int rb, h, m; qkv_decode(t, rb, h, m); return mk_rsrc((m == 0 ? a.Wq : (m == 1 ? a.Wk : a.Wv)) + h * 128, D_ * HE * 2); };
    if (j >= NT) return;
    gs.prime(rA(j), rB(j));
    bool first = true;
    for (int t = j; t < NT; t += nw) {
        int rb, head, mat; qkv_decode(t, rb, head, mat);
        const bool hn = t + nw < NT; const int tn = hn ? t + nw : t;
        f32x16 acc[2][2];
        if (mat == 2) { if (first) gs.template tile<0, false>(rA(t), rB(t), rA(tn), rB(tn), hn, 9, acc); else gs.template tile<EPI_STORES, false>(rA(t), rB(t), rA(tn), rB(tn), hn, 9, acc); }
        else          { if (first) gs.template tile<0, true>(rA(t), rB(t), rA(tn), rB(tn), hn, 9, acc);  else gs.template tile<EPI_STORES, true>(rA(t), rB(t), rA(tn), rB(tn), hn, 9, acc); }
        first = false;
        if (mat == 2) epi_tile128(acc, img, false, mk_rsrc(a.ws + WS_VT, S_ * HE * 2), (head * E_ * S_ + rb * 128) * 2, S_ * 2, [](float x) { return x; });
        else epi_tile128(acc, img, true, mk_rsrc(a.ws + (mat == 0 ? WS_Q : WS_K), S_ * HE * 2), (head * S_ + rb * 128) * E_ * 2, E_ * 2, [](float x) { return x; });
        wg_signal(a, (mat == 0 ? C_Q : C_KV) + (rb * H_ + head) * 16);
    }
}

DI void xattn_unit(const Params& a, int seg, int head, LAS unsigned char* wl  ) {
    const int lane = lane_id(), c = lane & 15, g = lane >> 4;
    const rsrc_t rQ = mk_rsrc(a.ws + WS_Q, S_ * HE * 2), rK = mk_rsrc(a.ws + WS_K, S_ * HE * 2), rV = mk_rsrc(a.ws + WS_VT, S_ * HE * 2);
    const rsrc_t rM = mk_rsrc(a.ws + WS_KVM, NSEG * H_ * 4096), rO = mk_rsrc(a.ws + WS_OX, S_ * HE * 2);
    const int tok0 = head * S_ + seg * 32;
    const int km = (seg * H_ + head) * 4096;
    {
        const v4u x0 = __builtin_amdgcn_raw_buffer_load_b128(rM, km + lane * 16, 0, AUX_SC1), x1 = __builtin_amdgcn_raw_buffer_load_b128(rM, km + 1024 + lane * 16, 0, AUX_SC1);
        *(LAS v4u*)(wl + lane * 16) = x0; *(LAS v4u*)(wl + 1024 + lane * 16) = x1;
    }
    v4u qf[2][4], kf[2][4];
#pragma unroll
    for (int t = 0; t < 2; ++t)
#pragma unroll
        for (int ks = 0; ks < 4; ++ks) {
            qf[t][ks] = __builtin_amdgcn_raw_buffer_load_b128(rQ, ((tok0 + 16 * t + c) * E_ + 32 * ks + 8 * g) * 2, 0, AUX_SC1);
            kf[t][ks] = __builtin_amdgcn_raw_buffer_load_b128(rK, ((tok0 + 16 * t + c) * E_ + 32 * ks + 8 * g) * 2, 0, AUX_SC1);
        }
    v4u vf[8]; v2u vmf[8];
#pragma unroll
    for (int et = 0; et < 8; ++et) {
        const int e = 16 * et + c;
        vf[et] = __builtin_amdgcn_raw_buffer_load_b128(rV, ((head * E_ + e) * S_ + seg * 32 + 8 * g) * 2, 0, AUX_SC1);
        vmf[et] = __builtin_amdgcn_raw_buffer_load_b64(rM, km + 2048 + e * 16 + (g & 1) * 8, 0, AUX_SC1);
    }
    __builtin_amdgcn_s_waitcnt(0xC07F);
    __builtin_amdgcn_wave_barrier();
    const int q = c >> 2, p = c & 3;
    f32x4 st[2][3];
#pragma unroll
    for (int t = 0; t < 2; ++t)
#pragma unroll
        for (int kt = 0; kt < 3; ++kt) st[t][kt] = f32x4{0, 0, 0, 0};
#pragma unroll
    for (int ks = 0; ks < 4; ++ks) {
        const int e0 = 32 * ks + 8 * g + q;
        const bf16x8 km8 = tr2(wl + e0 * 16 + (p & 1) * 8, wl + (e0 + 4) * 16 + (p & 1) * 8);
#pragma unroll
        for (int t = 0; t < 2; ++t) {
            const bf16x8 qq = __builtin_bit_cast(bf16x8, qf[t][ks]);
            st[t][0] = mfma16(km8, qq, st[t][0]);
            st[t][1] = mfma16(__builtin_bit_cast(bf16x8, kf[0][ks]), qq, st[t][1]);
            st[t][2] = mfma16(__builtin_bit_cast(bf16x8, kf[1][ks]), qq, st[t][2]);
        }
    }
    bf16x8 pf[2]; v2u pm[2]; float rz[2];
#pragma unroll
    for (int t = 0; t < 2; ++t) {
        const int qi = 16 * t + c;
        float s[12];
#pragma unroll
        for (int r = 0; r < 4; ++r) {
            s[r] = g < 2 ? rbf(rbf(st[t][0][r]) * SCALE) : -INFINITY;
            s[4 + r] = (4 * g + r) <= qi ? rbf(rbf(st[t][1][r]) * SCALE) : -INFINITY;
            s[8 + r] = (16 + 4 * g + r) <= qi ? rbf(rbf(st[t][2][r]) * SCALE) : -INFINITY;
        }
        float m = s[0];
#pragma unroll
        for (int r = 1; r < 12; ++r) m = fmaxf(m, s[r]);
        m = xmax16_32(m);
        float z = 0.f;
#pragma unroll
        for (int r = 0; r < 12; ++r) { s[r] = __builtin_amdgcn_exp2f((s[r] - m) * 1.4426950408889634f); z += s[r]; }
        z = xsum16_32(z);
        const float rzz = 1.f / z;
#pragma unroll
        for (int r = 0; r < 12; ++r) s[r] *= rzz;
        rz[t] = 1.f;
        pm[t] = v2u{pk2(s[0], s[1]), pk2(s[2], s[3])};
        const v4u pp = {pk2(s[4], s[5]), pk2(s[6], s[7]), pk2(s[8], s[9]), pk2(s[10], s[11])};
        pf[t] = __builtin_bit_cast(bf16x8, pp);
    }
#pragma unroll
    for (int ep = 0; ep < 4; ++ep) {
        f32x4 o[2][2];
#pragma unroll
        for (int et = 0; et < 2; ++et)
#pragma unroll
            for (int t = 0; t < 2; ++t) {
                o[et][t] = mfma16(__builtin_bit_cast(bf16x8, vf[2 * ep + et]), pf[t], f32x4{0, 0, 0, 0});
                o[et][t] = mfma16k16(vmf[2 * ep + et], pm[t], o[et][t]);
            }
#pragma unroll
        for (int t = 0; t < 2; ++t) {
            const unsigned d0 = pk2(o[0][t][0] * rz[t], o[0][t][1] * rz[t]), d1 = pk2(o[0][t][2] * rz[t], o[0][t][3] * rz[t]);
            const unsigned e0 = pk2(o[1][t][0] * rz[t], o[1][t][1] * rz[t]), e1 = pk2(o[1][t][2] * rz[t], o[1][t][3] * rz[t]);
            auto s0 = __builtin_amdgcn_permlane16_swap(d0, e0, false, false);
            auto s1 = __builtin_amdgcn_permlane16_swap(d1, e1, false, false);
            const v4u val = {s0[0], s1[0], s0[1], s1[1]};
            const int eo = 32 * ep + ((g & 1) << 4) + ((g >> 1) << 3);
            __builtin_amdgcn_raw_buffer_store_b128(val, rO, ((seg * 32 + 16 * t + c) * HE + head * E_ + eo) * 2, 0, AUX_SC1);
        }
    }
}
DI void role_att(const Params& a, int head, LAS unsigned char* lds) {
    const int w = wave_id(), lane = lane_id();
    const int par = get_par(a);
    for (int rb = 0; rb < NRB; ++rb) {
        const int seg = 4 * rb + w;
        (void)wave_poll_ge(a, ctl_word(a, C_KV + (rb * H_ + head) * 16), 2u); (void)wave_poll_ge(a, ctl_word(a, C_Q + (rb * H_ + head) * 16), 1u);
        (void)wave_poll_ge(a, (gu32*)(a.xr + par * 8192 + X_HINT + 8192 + head * 256), (unsigned)(seg + 2));
        xattn_unit(a, seg, head, lds + w * 2048);
        wg_signal(a, C_OX + rb * 32);
    }
}
DI void role_wo(const Params& a, int task, LAS unsigned char* lds) {
    using GS = GemmStream<128, 64, 4, true>;
    GS gs; gs.init(lds, HE, D_);
    const int kq = task / 9, nt = task % 9;
    const int lane = lane_id(), wave = wave_id(), wm = wave >> 1, wn = wave & 1;
    const rsrc_t rB = mk_rsrc(a.Wo + (size_t)kq * 1024 * D_ + nt * 64, 1024 * D_ * 2);
    for (int rb = 0; rb < NRB; ++rb) {
        gs.primeB(rB);
        wg_wait<false>(a, ctl_word(a, C_OX + rb * 32), 32u);
        const rsrc_t rA = mk_rsrc(a.ws + WS_OX + ((size_t)rb * 128 * HE + kq * 1024) * 2, 128 * HE * 2);
        gs.primeA(rA);
        f32x16 acc[2][1];
        gs.template tile<0, true, true>(rA, rB, rA, rB, false, 16, acc);
        const rsrc_t rP = mk_rsrc(a.ws + WS_PART + (size_t)(rb * 4 + kq) * 128 * D_ * 4, 128 * D_ * 4);
#pragma unroll
        for (int i = 0; i < 2; ++i)
#pragma unroll
            for (int rg = 0; rg < 4; ++rg) {
                const int m = wm * 64 + i * 32 + (lane & 31), n = nt * 64 + wn * 32 + 8 * rg + 4 * (lane >> 5);
                const v4u o = {f2u(acc[i][0][4 * rg]), f2u(acc[i][0][4 * rg + 1]), f2u(acc[i][0][4 * rg + 2]), f2u(acc[i][0][4 * rg + 3])};
                __builtin_amdgcn_raw_buffer_store_b128(o, rP, (m * D_ + n) * 4, 0, AUX_SC1);
            }
        wg_signal(a, C_PART + rb * 32);
    }
}
DI void role_ln2(const Params& a, int q, LAS unsigned char* lds) {
    LAS float* tab = (LAS float*)lds;
    wg_wait<false>(a, ctl_word(a, C_MODB), 108u);
    load_modtab(a, tab, 3, 4, 2);
    for (int rb = 0; rb < NRB; ++rb) {
        wg_wait<false>(a, ctl_word(a, C_PART + rb * 32), 36u);
        task_ln2<2>(a, rb * 128 + q * 8, tab);
        wg_signal(a, C_H2 + rb * 32);
    }
}
DI void role_win(const Params& a, int nt, LAS unsigned char* lds) {
    using GS = GemmStream<128, 128, 3, true>;
    GS gs; gs.init(lds, D_, FF);
    LAS unsigned char* img = lds + GS::C::LDS;
    const rsrc_t rB = mk_rsrc(a.Win + nt * 128, D_ * FF * 2);
    const rsrc_t rO = mk_rsrc(a.ws + (nt < 18 ? WS_Z1 : WS_Z2), S_ * FH * 2);
    for (int rb = 0; rb < NRB; ++rb) {
        gs.primeB(rB);
        wg_wait<false>(a, ctl_word(a, C_H2 + rb * 32), 16u);
        const rsrc_t rA = mk_rsrc(a.ws + WS_H2 + (size_t)rb * 128 * D_ * 2, 128 * D_ * 2);
        gs.primeA(rA);
        f32x16 acc[2][2];
        gs.template tile<0, true, true>(rA, rB, rA, rB, false, 9, acc);
        epi_tile128(acc, img, true, rO, (rb * 128 * FH + (nt % 18) * 128) * 2, FH * 2, [](float x) { return gelu_tanh(rbf(x)); });
        wg_signal(a, C_Z + rb * 32);
    }
}
DI void role_ln3(const Params& a, int q) {
    for (int rb = 0; rb < NRB; ++rb) {
        wg_wait<false>(a, ctl_word(a, C_Z + rb * 32), 36u);
        task_ln3<2>(a, rb * 128 + q * 16);
    }
}
DI void worker_role(const Params& a, int j, LAS unsigned char* lds) {
    if (j < 64) {
        LAS float* tab = (LAS float*)lds;
        wg_wait<false>(a, ctl_word(a, C_MODA), 36u);
        load_modtab(a, tab, 0, 1, -1);
        task_ln<false>(a, j * 32, tab, WS_H1);
        wg_signal(a, C_H1);
    }
    constexpr int NQKV = 128;
    wg_wait<false>(a, ctl_word(a, C_H1), 64u);
    phase_qkv(a, j, NQKV, lds);
    TM(1);
#define RMASK 31
    if (j < 32) { if (RMASK & 1) role_att(a, j, lds); }
    else if (j < 68) { if (RMASK & 2) role_wo(a, j - 32, lds); }
    else if (j < 84) { if (RMASK & 4) role_ln2(a, j - 68, lds); }
    else if (j < 120) { if (RMASK & 8) role_win(a, j - 84, lds); }
    else if (j < 128) { if (RMASK & 16) role_ln3(a, j - 120); }
    TM(2);
}

constexpr unsigned SPIN_CAP = 1u << 14;
#define P_INC 60
#define P_DEC 2
DI unsigned pace(unsigned lead, unsigned base, bool early) { return early ? (lead + P_INC < base + 1200u ? lead + P_INC : lead) : (lead > P_DEC && lead + 300u > base ? lead - P_DEC : lead); }
DI void hint_wait(rsrc_t rX, int w, int lane, unsigned want) {
    const int off = lane < 8 ? X_HINT + (8 * w + lane) * 256 : OOB;
    for (unsigned spins = 0; spins < 8192u; ++spins) {
        CB();
        const unsigned v = __builtin_amdgcn_raw_buffer_load_b32(rX, off, 0, AUX_SC1);
        if (!__any(lane < 8 && v < want)) break;
    }
}
DI void delay_from(unsigned long long t0, unsigned d) { while ((unsigned)(cyc() - t0) + 40u < d) {} }
DI int mem_slot(int s) { return s % 3; }
DI int o_slot(int s) { return s & 1; }
constexpr int HL_Q = 0;
constexpr int HL_K = HL_Q + 2048;
constexpr int HL_KX = HL_K + 2048;
constexpr int HL_WV = HL_KX + 2 * 8192;
constexpr int HL_STAGE = HL_WV + 4 * 9 * 2048;
constexpr int HL_END = HL_STAGE + 3 * 16384;

DI int wsw(int k) { return ((k & 1) | (((k >> 2) & 1) << 1) | (((k >> 3) & 1) << 2)) << 1; }
DI void stage_w(LAS unsigned char* dst, const u16* __restrict__ src, int ld, int wave, int lane) {
#pragma unroll
    for (int p = 0; p < 4; ++p) {
        const int piece = wave + 4 * p, k = piece * 4 + (lane >> 4), ch = (lane & 15) ^ wsw(k);
        glds16(src + (size_t)k * ld + ch * 8, dst + piece * 1024 + lane * 16);
    }
}
DI bf16x16 read_wfrag(LAS unsigned char* st, int col, int lane) {
    const int gb = lane >> 4, q = (lane & 15) >> 2, p = lane & 3;
    s16x4 r[4];
#pragma unroll
    for (int m = 0; m < 4; ++m) {
        const int d0 = 32 * (m >> 1) + 16 * (gb >> 1) + 4 * (gb & 1) + 2 * (m & 1);
        const int k = d0 + (q & 1) + 8 * (q >> 1);
        const int cb = (col + 4 * p) * 2;
        const int ch = (cb >> 4) ^ wsw(k);
        r[m] = __builtin_amdgcn_ds_read_tr16_b64_v4i16((LAS s16x4*)(st + k * 256 + ch * 16 + (cb & 15)));
    }
    typedef short s16x16 __attribute__((ext_vector_type(16)));
    s16x16 v = {r[0][0], r[0][1], r[0][2], r[0][3], r[1][0], r[1][1], r[1][2], r[1][3], r[2][0], r[2][1], r[2][2], r[2][3], r[3][0], r[3][1], r[3][2], r[3][3]};
    return __builtin_bit_cast(bf16x16, v);
}

DI void head_role(const Params& a, int h, LAS unsigned char* lds) {
    const int lane = lane_id(), w = wave_id(), c = lane & 15, g = lane >> 4;
    unsigned char* ws = a.ws;
    gu32* ctl = (gu32*)(ws + WS_CTL);
    const rsrc_t rKVM = __builtin_amdgcn_make_buffer_rsrc((void*)(ws + WS_KVM), 0, NSEG * H_ * 4096, RSRC3);
    bf16x16 WA[4][8];
    bf16x16 WB[4];
    bf16x16 WV[9];
    LAS unsigned char* wv1 = lds + HL_WV + w * 9 * 2048 + lane * 16;
    {
        LAS unsigned char* st = lds + HL_STAGE;
        const u16* src[3] = {a.Wq + h * 128, a.Wk + h * 128, a.Wv + h * 128};
#pragma unroll
        for (int i = 0; i < 2; ++i) stage_w(st + i * 16384, src[0] + (size_t)i * 64 * HE, HE, w, lane);
#pragma unroll
        for (int n = 0; n < 27; ++n) {
            if (n + 2 < 27) stage_w(st + ((n + 2) % 3) * 16384, src[(n + 2) / 9] + (size_t)((n + 2) % 9) * 64 * HE, HE, w, lane);
            if (n + 2 < 27) asm volatile("s_waitcnt vmcnt(8)" ::: "memory");
            else if (n + 1 < 27) asm volatile("s_waitcnt vmcnt(4)" ::: "memory");
            else asm volatile("s_waitcnt vmcnt(0)" ::: "memory");
            __builtin_amdgcn_s_barrier();
            const bf16x16 f0 = read_wfrag(st + (n % 3) * 16384, 32 * w, lane), f1 = read_wfrag(st + (n % 3) * 16384, 32 * w + 16, lane);
            const int m = n / 9, f = n % 9;
            if (m < 2) {
                if (f < 8) { WA[2 * m][f] = f0; WA[2 * m + 1][f] = f1; asm volatile("" : "+a"(WA[2 * m][f]), "+a"(WA[2 * m + 1][f])); }
                else { WB[2 * m] = f0; WB[2 * m + 1] = f1; }
            } else {
                WV[f] = f0;
                const v8u t = __builtin_bit_cast(v8u, f1);
                *(LAS v4u*)(wv1 + f * 2048) = v4u{t[0], t[1], t[2], t[3]};
                *(LAS v4u*)(wv1 + f * 2048 + 1024) = v4u{t[4], t[5], t[6], t[7]};
            }
            asm volatile("s_waitcnt lgkmcnt(0)" ::: "memory");
            __builtin_amdgcn_s_barrier();
        }
    }
    const int idx = (lane & 1) ? 0xEEEE : 0x4444;
    const rsrc_t rX = mk_rsrc(a.xr + get_par(a) * 8192, X_BYTES);
    const rsrc_t rK = mk_rsrc(ws + WS_K + (size_t)h * S_ * E_ * 2, S_ * E_ * 2), rV = mk_rsrc(ws + WS_VT + (size_t)h * E_ * S_ * 2, E_ * S_ * 2);
    const int q4 = c >> 2, p4 = c & 3;
    LAS unsigned char* qkw = lds + HL_Q + (32 * w + c) * 16 + 4 * g;
    LAS unsigned char* qr = lds + HL_Q + (8 * g + q4) * 16 + (p4 & 1) * 8;
    LAS unsigned char* kr = lds + HL_K + (8 * g + q4) * 16 + (p4 >> 1) * 8;
    const int kxr = HL_KX + c * 256;
    int kxsrc[2];
#pragma unroll
    for (int p = 0; p < 2; ++p) { const int key = 8 * w + 4 * p + g; kxsrc[p] = key * E_ + ((c ^ (key & 15)) * 8); }
    int ooff;
    {
        const int el = 32 * w + ((g & 1) << 4) + ((g >> 1) << 3);
        const int win = el >> 6, gg = (el >> 4) & 3, hf = (el >> 3) & 1;
        const int r = 4 * (c & 3) + 2 * win + hf;
        const int p = h * 2 + ((c >> 2) & 1);
        ooff = c < 8 ? X_O + (p >> 3) * 16384 + (p & 7) * 1024 + (16 * gg + r) * 16 : 0x7F000000;
    }
    const float ninf = (g & 1) ? -INFINITY : 0.f;
    const int hoff = (w < 2 && lane == 0) ? X_HINT + w * 8192 + h * 256 : OOB;
    unsigned nfail = 0, lead = (unsigned)a.lead_h;
    bool fail = false;
    TM(1);
    for (int s = 0; s < NSEG; ++s) {
        if ((s & 3) == 0) (void)wave_poll_ge(a, ctl_word(a, C_KV + ((s >> 2) * H_ + h) * 16), 2u);
        v4u vx[2], kxr_[2];
        LAS unsigned char* kxb = lds + HL_KX + (s & 1) * 8192;
#pragma unroll
        for (int p = 0; p < 2; ++p) kxr_[p] = __builtin_amdgcn_raw_buffer_load_b128(rK, (s * 32 * E_ + kxsrc[p]) * 2, 0, AUX_SC1);
#pragma unroll
        for (int t = 0; t < 2; ++t) vx[t] = __builtin_amdgcn_raw_buffer_load_b128(rV, ((32 * w + 16 * t + c) * S_ + s * 32 + 8 * g) * 2, 0, AUX_SC1);
#define KX_TO_LDS() { *(LAS v4u*)(kxb + (2 * w) * 1024 + lane * 16) = kxr_[0]; *(LAS v4u*)(kxb + (2 * w + 1) * 1024 + lane * 16) = kxr_[1]; }
        bf16x16 v1f[4];
#define LDV1(slot, f) { const v4u lo_ = *(LAS v4u*)(wv1 + (f) * 2048), hi_ = *(LAS v4u*)(wv1 + (f) * 2048 + 1024); \
                        v1f[slot] = __builtin_bit_cast(bf16x16, (v8u{lo_[0], lo_[1], lo_[2], lo_[3], hi_[0], hi_[1], hi_[2], hi_[3]})); }
#define SB() __builtin_amdgcn_sched_barrier(0)
#define QK4(f) { SMF_A(acc[0], af[f], WA[0][f], idx); SMF_A(acc[1], af[f], WA[1][f], idx); SMF_A(acc[2], af[f], WA[2][f], idx); SMF_A(acc[3], af[f], WA[3][f], idx); }
        LDV1(0, 0); LDV1(1, 1); LDV1(2, 2); LDV1(3, 3);
        v4u af[9];
        f32x4 acc[4], av[2];
        {
            unsigned spins = 0;
            if (s > 0) { hint_wait(rX, w, lane, (unsigned)s); __builtin_amdgcn_s_barrier(); }
            KX_TO_LDS();
            if (s > 0) delay_from(cyc(), lead);
            const int base = X_MEM + mem_slot(s) * X_MEM_SLOT + lane * 16;
            while (true) {
                if (s == 0) {
#pragma unroll
                    for (int f = 0; f < 9; ++f) af[f] = *(const v4u*)(a.mem0 + (c >> 1) * D_ + 64 * f + 16 * g + 8 * (c & 1));
                    asm volatile("s_waitcnt vmcnt(0)" : "+v"(af[0]), "+v"(af[1]), "+v"(af[2]), "+v"(af[3]), "+v"(af[4]), "+v"(af[5]), "+v"(af[6]), "+v"(af[7]), "+v"(af[8]) :: "memory");
                } else {
                    while (true) {
                        asm volatile(
                            "buffer_load_dwordx4 %0, %9, %10, 0 offen sc1\n\tbuffer_load_dwordx4 %1, %9, %10, 0 offen offset:1024 sc1\n\t"
                            "buffer_load_dwordx4 %2, %9, %10, 0 offen offset:2048 sc1\n\tbuffer_load_dwordx4 %3, %9, %10, 0 offen offset:3072 sc1\n\t"
                            "buffer_load_dwordx4 %4, %9, %10, %11 offen sc1\n\tbuffer_load_dwordx4 %5, %9, %10, %11 offen offset:1024 sc1\n\t"
                            "buffer_load_dwordx4 %6, %9, %10, %11 offen offset:2048 sc1\n\tbuffer_load_dwordx4 %7, %9, %10, %11 offen offset:3072 sc1\n\t"
                            "buffer_load_dwordx4 %8, %9, %10, %12 offen sc1\n\ts_waitcnt vmcnt(8)"
                            : "=&v"(af[0]), "=&v"(af[1]), "=&v"(af[2]), "=&v"(af[3]), "=&v"(af[4]), "=&v"(af[5]), "=&v"(af[6]), "=&v"(af[7]), "=&v"(af[8])
                            : "v"(base), "s"(rX), "s"(4096), "s"(16384) : "memory");
                        if (!__any(vmax(0u, af[0]) == ~0u)) break;
                        ++nfail;
                        if (++spins > SPIN_CAP) { fail = true; break; }
                    }
                }
#pragma unroll
                for (int T = 0; T < 4; ++T) acc[T] = zero4();
                asm volatile("s_nop 1" : "+v"(acc[0]), "+v"(acc[1]), "+v"(acc[2]), "+v"(acc[3]));
                unsigned mx = 0;
#define QKF(f, n) { asm volatile("s_waitcnt vmcnt(" #n ")" : "+v"(af[f]) :: "memory"); QK4(f); mx = vmax(mx, af[f]); }
                QK4(0); QKF(1, 7); QKF(2, 6); QKF(3, 5); QKF(4, 4); QKF(5, 3); QKF(6, 2); QKF(7, 1);
                asm volatile("s_waitcnt vmcnt(0)" : "+v"(af[8]) :: "memory");
#pragma unroll
                for (int T = 0; T < 4; ++T) SMF_V(acc[T], af[8], WB[T], idx);
                mx = vmax(mx, af[8]);
                if (s == 0 || fail || !__any(mx == ~0u)) break;
                ++nfail;
                if (++spins > SPIN_CAP) { fail = true; break; }
            }
            if (s > 0) lead = pace(lead, (unsigned)a.lead_h, spins > 0);
        }
        av[0] = zero4(); av[1] = zero4();
        asm volatile("s_nop 1" : "+v"(av[0]), "+v"(av[1]));
        SMF_V(av[0], af[0], WV[0], idx); SMF_V(av[1], af[0], v1f[0], idx);
        asm volatile("s_nop 3" : "+v"(acc[0]), "+v"(acc[1]), "+v"(acc[2]), "+v"(acc[3]));
        unsigned pq[2], pk[2];
#define PACK(dst, A) { float x0_, x1_; asm volatile("v_add_f32_e32 %0, %1, %2" : "=v"(x0_) : "v"((A)[0]), "v"((A)[1])); asm volatile("v_add_f32_e32 %0, %1, %2" : "=v"(x1_) : "v"((A)[2]), "v"((A)[3])); \
                       asm volatile("v_cvt_pk_bf16_f32 %0, %1, %2" : "=v"(dst) : "v"(x0_), "v"(x1_)); }
        PACK(pq[0], acc[0]); *(LAS unsigned*)(qkw) = pq[0];
        SMF_V(av[0], af[1], WV[1], idx);
        PACK(pq[1], acc[1]); *(LAS unsigned*)(qkw + 256) = pq[1];
        SMF_V(av[1], af[1], v1f[1], idx);
        PACK(pk[0], acc[2]); *(LAS unsigned*)(qkw + 2048) = pk[0];
        SMF_V(av[0], af[2], WV[2], idx);
        PACK(pk[1], acc[3]); *(LAS unsigned*)(qkw + 2048 + 256) = pk[1];
        SB();
        SMF_V(av[1], af[2], v1f[2], idx);
        __builtin_amdgcn_raw_buffer_store_b128(poison4(), rX, s > 0 ? ooff + o_slot(s - 1) * X_O_SLOT : OOB, 0, AUX_SC1);
        SB();
        LDV1(0, 4); LDV1(1, 5); LDV1(2, 6);
        SB();
        SMF_V(av[0], af[3], WV[3], idx); SMF_V(av[1], af[3], v1f[3], idx);
        SMF_V(av[0], af[4], WV[4], idx); SMF_V(av[0], af[5], WV[5], idx);
        SB();
        asm volatile("s_waitcnt lgkmcnt(6)\n\ts_barrier" ::: "memory");
        __builtin_amdgcn_raw_buffer_store_b32((unsigned)(s + 1), rX, hoff, 0, AUX_SC1);
        bf16x8 qf[4], kmf[4], kx[2][4];
#define LDS_SC(ks) { qf[ks] = tr2(qr + (ks) * 512, qr + (ks) * 512 + 64); \
                     kx[0][ks] = *(LAS bf16x8*)(lds + (s & 1) * 8192 + kxr + (((4 * (ks) + g) ^ c) << 4)); \
                     kx[1][ks] = *(LAS bf16x8*)(lds + (s & 1) * 8192 + kxr + 4096 + (((4 * (ks) + g) ^ c) << 4)); \
                     kmf[ks] = tr2(kr + (ks) * 512, kr + (ks) * 512 + 64); }
        LDS_SC(0);
        SB();
        SMF_V(av[1], af[4], v1f[0], idx); SMF_V(av[0], af[6], WV[6], idx);
        SB();
        LDS_SC(1);
        SB();
        f32x4 sp[3];
        { float c0, c1, c2, c3; VMOV(c0, ninf); VMOV(c1, ninf); VMOV(c2, ninf); VMOV(c3, ninf); sp[0] = f32x4{c0, c1, c2, c3}; }
        MFMA32_0(sp[1], kx[0][0], qf[0]); MFMA32_0(sp[2], kx[1][0], qf[0]); MFMA32_ACC(sp[0], kmf[0], qf[0]);
        SB();
        LDS_SC(2);
        LDS_SC(3);
        SB();
        MFMA32_ACC(sp[1], kx[0][1], qf[1]); MFMA32_ACC(sp[2], kx[1][1], qf[1]); MFMA32_ACC(sp[0], kmf[1], qf[1]);
        MFMA32_ACC(sp[1], kx[0][2], qf[2]); MFMA32_ACC(sp[2], kx[1][2], qf[2]); MFMA32_ACC(sp[0], kmf[2], qf[2]);
        MFMA32_ACC(sp[1], kx[0][3], qf[3]); MFMA32_ACC(sp[2], kx[1][3], qf[3]); MFMA32_ACC(sp[0], kmf[3], qf[3]);
        SB();
        LDV1(3, 7); LDV1(0, 8);
        SB();
        SMF_V(av[1], af[5], v1f[1], idx); SMF_V(av[0], af[7], WV[7], idx);
        asm volatile("" : "+v"(sp[0]), "+v"(sp[1]), "+v"(sp[2]));
        unsigned pv[2];
        {
            float m, t, t2;
            asm volatile(
                "v_max3_f32 %0, %5, %6, %7\n\tv_max3_f32 %1, %8, %9, %10\n\tv_max3_f32 %2, %11, %12, %13\n\t"
                "v_max3_f32 %0, %0, %14, %15\n\tv_max3_f32 %1, %1, %2, %16\n\tv_max_f32_e32 %0, %0, %1\n\tv_mov_b32_e32 %1, %0\n\t"
                "v_smfmac_f32_16x16x64_bf16 %3, %17, %18, %21\n\ts_nop 0\n\t"
                "v_permlane16_swap_b32_e32 %0, %1\n\tv_max_f32_e32 %0, %0, %1\n\tv_mov_b32_e32 %1, %0\n\t"
                "v_smfmac_f32_16x16x64_bf16 %4, %19, %20, %21\n\ts_nop 0\n\t"
                "v_permlane32_swap_b32_e32 %0, %1\n\tv_max_f32_e32 %0, %0, %1\n\tv_mul_f32_e32 %0, %22, %0"
                : "=&v"(m), "=&v"(t), "=&v"(t2), "+v"(av[1]), "+v"(av[0])
                : "v"(sp[1][0]), "v"(sp[1][1]), "v"(sp[1][2]), "v"(sp[1][3]), "v"(sp[2][0]), "v"(sp[2][1]), "v"(sp[2][2]), "v"(sp[2][3]), "v"(sp[0][0]),
                  "v"(sp[0][1]), "v"(sp[0][2]), "v"(sp[0][3]), "v"(af[6]), "v"(v1f[2]), "v"(af[8]), "v"(WV[8]), "v"(idx), "s"(SCL));
            const f32x2 ms = {m, m};
            const f32x2 scl2 = {SCL, SCL};
            f32x2 e[6];
            PKFMA(e[0], (f32x2{sp[0][0], sp[0][1]}), scl2, ms); PKFMA(e[1], (f32x2{sp[0][2], sp[0][3]}), scl2, ms);
            PKFMA(e[2], (f32x2{sp[1][0], sp[1][1]}), scl2, ms); PKFMA(e[3], (f32x2{sp[1][2], sp[1][3]}), scl2, ms);
            PKFMA(e[4], (f32x2{sp[2][0], sp[2][1]}), scl2, ms); PKFMA(e[5], (f32x2{sp[2][2], sp[2][3]}), scl2, ms);
            float x[12];
#pragma unroll
            for (int r = 0; r < 6; ++r) { x[2 * r] = e[r][0]; x[2 * r + 1] = e[r][1]; }
            VEXP(x[4]); VEXP(x[5]); VEXP(x[6]); VEXP(x[7]);
            SMF_V(av[1], af[7], v1f[3], idx);
            VEXP(x[8]); VEXP(x[9]); VEXP(x[10]); VEXP(x[11]);
            SMF_V(av[1], af[8], v1f[0], idx);
            VEXP(x[0]); VEXP(x[1]); VEXP(x[2]); VEXP(x[3]);
            v4u pp; v2u pm;
            VCVT(pp[0], x[4], x[5]); VCVT(pp[1], x[6], x[7]); VCVT(pp[2], x[8], x[9]); VCVT(pp[3], x[10], x[11]);
            VCVT(pm[0], x[0], x[1]); VCVT(pm[1], x[2], x[3]);
            v4u ones; { unsigned o_; asm volatile("v_mov_b32_e32 %0, 0x3f803f80" : "=v"(o_)); ones = v4u{o_, o_, o_, o_}; }
            asm volatile("s_nop 1" : "+v"(pp), "+v"(pm), "+v"(ones));
            f32x4 o0, o1, zz;
            MFMA32_0(o0, vx[0], pp); MFMA32_0(o1, vx[1], pp); MFMA32_0(zz, ones, pp);
            asm volatile("" : "+v"(av[0]), "+v"(av[1]));
            float a0, a1, a2, a3;
            VADD(a0, av[0][0], av[0][1]); VADD(a1, av[0][2], av[0][3]); VADD(a2, av[1][0], av[1][1]); VADD(a3, av[1][2], av[1][3]);
            VCVT(pv[0], a0, a1); VCVT(pv[1], a2, a3);
            unsigned u0, u1, w0, w1;
            VMOV(u0, pv[0]); VMOV(u1, pv[0]); VMOV(w0, pv[1]); VMOV(w1, pv[1]);
            NOP(0);
            SWAP16(u0, u1); SWAP16(w0, w1);
            v2u vm0 = {u0, u1}, vm1 = {w0, w1};
            asm volatile("s_nop 1" : "+v"(vm0), "+v"(vm1), "+v"(o0), "+v"(o1), "+v"(zz));
            MFMA16_ACC(zz, (v2u{ones[0], ones[1]}), pm); MFMA16_ACC(o0, vm0, pm); MFMA16_ACC(o1, vm1, pm);
            asm volatile("s_nop 5" : "+v"(zz));
            float rz; asm volatile("v_rcp_f32_e32 %0, %1" : "=v"(rz) : "v"(zz[0]));
            asm volatile("s_nop 1" : "+v"(o0), "+v"(o1), "+v"(rz));
            const f32x2 rz2 = {rz, rz};
            f32x2 n0, n1, n2, n3;
            PKMUL(n0, (f32x2{o0[0], o0[1]}), rz2); PKMUL(n1, (f32x2{o0[2], o0[3]}), rz2); PKMUL(n2, (f32x2{o1[0], o1[1]}), rz2); PKMUL(n3, (f32x2{o1[2], o1[3]}), rz2);
            unsigned d0, d1, e0_, e1;
            VCVT(d0, n0[0], n0[1]); VCVT(d1, n1[0], n1[1]); VCVT(e0_, n2[0], n2[1]); VCVT(e1, n3[0], n3[1]);
            NOP(0);
            SWAP16(d0, e0_); SWAP16(d1, e1);
            NOP(0);
            v4u val = {nosent(d0), nosent(d1), nosent(e0_), nosent(e1)};
            asm volatile("s_waitcnt vmcnt(1)" ::: "memory");
            __builtin_amdgcn_raw_buffer_store_b128(val, rX, s != NSEG - 1 ? ooff + o_slot(s) * X_O_SLOT : OOB, 0, AUX_SC1);
        }
        {
            auto a0 = __builtin_amdgcn_permlane32_swap(pk[0], pk[1], false, false);
            auto a1 = __builtin_amdgcn_permlane32_swap(pv[0], pv[1], false, false);
            auto b0 = __builtin_amdgcn_permlane16_swap(a0[0], a1[0], false, false);
            auto b1 = __builtin_amdgcn_permlane16_swap(a0[1], a1[1], false, false);
            v4u val = {b0[0], b0[1], b1[0], b1[1]};
            const int off = (s * H_ + h) * 4096 + (g & 1) * 2048 + (32 * w + 16 * (g >> 1) + c) * 16;
            __builtin_amdgcn_raw_buffer_store_b128(val, rKVM, off, 0, AUX_SC1);
        }
    }
    asm volatile("s_waitcnt vmcnt(0)\n\ts_barrier" ::: "memory");
    __builtin_amdgcn_raw_buffer_store_b32((unsigned)(NSEG + 1), rX, hoff, 0, AUX_SC1);
    TM(2);
    if (fail && threadIdx.x == 0) __hip_atomic_store(ctl, 1u, RLX_AGENT);
}

constexpr int YL_RED = 0;
constexpr int YL_STAGE = 4096;
DI void y_role(const Params& a, int tp, int rho, LAS unsigned char* lds) {
    const int lane = lane_id(), w = wave_id(), c = lane & 15, g = lane >> 4;
    unsigned char* ws = a.ws;
    gu32* ctl = (gu32*)(ws + WS_CTL);
    bf16x16 W[16][2];
    {
        LAS unsigned char* st = lds + YL_STAGE + w * 16384;
        const u16* src = a.Wo + (size_t)(1024 * w) * D_ + 32 * tp;
        auto stage = [&](int win, int slot) {
#pragma unroll
            for (int p = 0; p < 4; ++p) {
                const int k = 16 * p + (lane >> 2);
                const int ch = (lane & 3) ^ 0;
                glds16(src + (size_t)(64 * win + k) * D_ + ch * 8, st + slot * 4096 + p * 1024 + lane * 16);
            }
        };
#pragma unroll
        for (int i = 0; i < 3; ++i) stage(i, i);
        const int gb = lane >> 4, q = (lane & 15) >> 2, p = lane & 3;
#pragma unroll
        for (int n = 0; n < 16; ++n) {
            if (n + 3 < 16) stage(n + 3, (n + 3) & 3);
            if (n + 3 < 16) asm volatile("s_waitcnt vmcnt(12)" ::: "memory");
            else if (n + 2 < 16) asm volatile("s_waitcnt vmcnt(8)" ::: "memory");
            else if (n + 1 < 16) asm volatile("s_waitcnt vmcnt(4)" ::: "memory");
            else asm volatile("s_waitcnt vmcnt(0)" ::: "memory");
#pragma unroll
            for (int T = 0; T < 2; ++T) {
                s16x4 r[4];
#pragma unroll
                for (int m = 0; m < 4; ++m) {
                    const int d0 = 32 * (m >> 1) + 16 * (gb >> 1) + 4 * (gb & 1) + 2 * (m & 1);
                    const int k = d0 + (q & 1) + 8 * (q >> 1);
                    r[m] = __builtin_amdgcn_ds_read_tr16_b64_v4i16((LAS s16x4*)(st + (n & 3) * 4096 + k * 64 + (16 * T + 4 * p) * 2));
                }
                typedef short s16x16 __attribute__((ext_vector_type(16)));
                s16x16 v = {r[0][0], r[0][1], r[0][2], r[0][3], r[1][0], r[1][1], r[1][2], r[1][3], r[2][0], r[2][1], r[2][2], r[2][3], r[3][0], r[3][1], r[3][2], r[3][3]};
                W[n][T] = __builtin_bit_cast(bf16x16, v);
                asm volatile("" : "+a"(W[n][T]));
            }
            asm volatile("s_waitcnt lgkmcnt(0)" ::: "memory");
        }
    }
    const int idx = (lane & 1) ? 0xEEEE : 0x4444;
    const rsrc_t rX = mk_rsrc(a.xr + get_par(a) * 8192, X_BYTES);
    unsigned nfail = 0, lead = (unsigned)a.lead_y;
    bool fail = false;
    TM(1);
    for (int s = 0; s < NSEG - 1; ++s) {
        const int base = X_O + o_slot(s) * X_O_SLOT + 2 * w * 16384 + rho * 1024 + lane * 16;
        unsigned spins = 0;
        hint_wait(rX, w, lane, (unsigned)(s + 1)); delay_from(cyc(), lead);
        f32x4 D1[2], D2[2];
        while (true) {
            v4u v[8];
            while (true) {
                asm volatile(
                    "buffer_load_dwordx4 %0, %8, %9, 0 offen sc1\n\tbuffer_load_dwordx4 %1, %8, %9, 0 offen offset:2048 sc1\n\t"
                    "buffer_load_dwordx4 %2, %8, %9, %10 offen sc1\n\tbuffer_load_dwordx4 %3, %8, %9, %10 offen offset:2048 sc1\n\t"
                    "buffer_load_dwordx4 %4, %8, %9, %11 offen sc1\n\tbuffer_load_dwordx4 %5, %8, %9, %11 offen offset:2048 sc1\n\t"
                    "buffer_load_dwordx4 %6, %8, %9, %12 offen sc1\n\tbuffer_load_dwordx4 %7, %8, %9, %12 offen offset:2048 sc1\n\ts_waitcnt vmcnt(7)"
                    : "=&v"(v[0]), "=&v"(v[1]), "=&v"(v[2]), "=&v"(v[3]), "=&v"(v[4]), "=&v"(v[5]), "=&v"(v[6]), "=&v"(v[7])
                    : "v"(base), "s"(rX), "s"(4096), "s"(16384), "s"(16384 + 4096) : "memory");
                if (!__any(vmax(0u, v[0]) == ~0u)) break;
                ++nfail;
                if (++spins > SPIN_CAP) { fail = true; break; }
            }
            D1[0] = zero4(); D1[1] = zero4(); D2[0] = zero4(); D2[1] = zero4();
            asm volatile("s_nop 1" : "+v"(D1[0]), "+v"(D1[1]), "+v"(D2[0]), "+v"(D2[1]));
            unsigned mx = 0;
#define YP(i, n) { asm volatile("s_waitcnt vmcnt(" #n ")" : "+v"(v[i]) :: "memory"); \
                   SMF_A(D1[0], v[i], W[2 * (i)][0], idx); SMF_A(D2[0], v[i], W[2 * (i) + 1][0], idx); SMF_A(D1[1], v[i], W[2 * (i)][1], idx); SMF_A(D2[1], v[i], W[2 * (i) + 1][1], idx); mx = vmax(mx, v[i]); }
            YP(0, 7); YP(1, 6); YP(2, 5); YP(3, 4); YP(4, 3); YP(5, 2); YP(6, 1); YP(7, 0);
            if (fail || !__any(mx == ~0u)) break;
            ++nfail;
            if (++spins > SPIN_CAP) { fail = true; break; }
        }
        lead = pace(lead, (unsigned)a.lead_y, spins > 0);
        asm volatile("s_nop 7" : "+v"(D1[0]), "+v"(D1[1]), "+v"(D2[0]), "+v"(D2[1]));
        *(LAS f32x2*)(lds + YL_RED + (w * 64 + lane) * 8) = f32x2{(D1[0][0] + D1[0][1]) + (D2[0][2] + D2[0][3]), (D1[1][0] + D1[1][1]) + (D2[1][2] + D2[1][3])};
        asm volatile("s_waitcnt lgkmcnt(0)\n\ts_barrier" ::: "memory");
        if (w == 0) {
            const f32x2 t = (*(LAS f32x2*)(lds + YL_RED + lane * 8) + *(LAS f32x2*)(lds + YL_RED + (64 + lane) * 8)) + (*(LAS f32x2*)(lds + YL_RED + (128 + lane) * 8) + *(LAS f32x2*)(lds + YL_RED + (192 + lane) * 8));
            const float x0 = t.x, x1 = t.y;
            const float y0 = u2f(__builtin_amdgcn_update_dpp(0u, f2u(x0), 0xB1, 0xF, 0xF, false)), y1 = u2f(__builtin_amdgcn_update_dpp(0u, f2u(x1), 0xB1, 0xF, 0xF, false));
            const unsigned dw = nosent((c & 1) ? pk2(y1, x1) : pk2(x0, y0));
            const unsigned d1 = __builtin_amdgcn_update_dpp(0u, dw, 0x102, 0xF, 0xF, false), d2 = __builtin_amdgcn_update_dpp(0u, dw, 0x104, 0xF, 0xF, false), d3 = __builtin_amdgcn_update_dpp(0u, dw, 0x106, 0xF, 0xF, false);
            v4u val = {dw, d1, d2, d3};
            const int fo = (tp >> 1) < 8 ? (tp >> 1) * 1024 : 16384;
            const int off = (c & 6) == 0 ? X_MEM + fo + (16 * (2 * (tp & 1) + (c & 1)) + 8 * rho + 2 * g + (c >> 3)) * 16 : 0x7F000000;
            __builtin_amdgcn_raw_buffer_store_b128(val, rX, mem_slot(s + 1) * X_MEM_SLOT + off, 0, AUX_SC1);
            __builtin_amdgcn_raw_buffer_store_b128(poison4(), rX, s > 0 ? mem_slot(s) * X_MEM_SLOT + off : OOB, 0, AUX_SC1);
        }
    }
    TM(2);
    if (fail && threadIdx.x == 0) __hip_atomic_store(ctl, 1u, RLX_AGENT);
}

#define SP_NST 4
#define OUT_NST 6
constexpr int SP_LDS = GP<128, 192, SP_NST>::LDS;
__global__ void __launch_bounds__(256, 1) k_sp(const u16* __restrict__ wsp, const u16* __restrict__ bsp, const u16* __restrict__ z2n, const u16* __restrict__ z1, u16* __restrict__ u) {
    extern __shared__ __attribute__((aligned(16))) unsigned char smem[];
    LAS unsigned char* lds = (LAS unsigned char*)smem;
    const int b = blockIdx.x, l = b & 7, r = b >> 3, mt = 4 * (l & 3) + (r & 3), nt = 6 * (l >> 2) + (r >> 2);
    const int lane = lane_id(), wave = wave_id(), wm = wave >> 1, wn = wave & 1;
    GemmStream<128, 192, SP_NST> gs; gs.init(lds, S_, FH);
    const rsrc_t rA = mk_rsrc(wsp + (size_t)mt * 128 * S_, 128 * S_ * 2), rB = mk_rsrc(z2n + nt * 192, S_ * FH * 2);
    gs.prime(rA, rB);
    f32x16 acc[2][3];
    gs.template tile<0, true>(rA, rB, rA, rB, false, 32, acc);
#pragma unroll
    for (int i = 0; i < 2; ++i) {
        const int row = wm * 64 + i * 32 + (lane & 31);
        const float bb = bf2f(bsp[mt * 128 + row]);
#pragma unroll
        for (int j = 0; j < 3; ++j)
#pragma unroll
            for (int rg = 0; rg < 4; ++rg) {
                const int col = wn * 96 + j * 32 + 8 * rg + 4 * (lane >> 5);
                const v2u o = {pk2(rbf(acc[i][j][4 * rg]) + bb, rbf(acc[i][j][4 * rg + 1]) + bb), pk2(rbf(acc[i][j][4 * rg + 2]) + bb, rbf(acc[i][j][4 * rg + 3]) + bb)};
                *(LAS v2u*)(lds + row * 384 + ((((col >> 3) ^ (row & 7)) << 4) | ((col & 4) << 1))) = o;
            }
    }
    __syncthreads();
#pragma unroll
    for (int it = 0; it < 12; ++it) {
        const int id = it * 256 + threadIdx.x, row = id / 24, ch = id % 24;
        const v4u gv = *(LAS v4u*)(lds + row * 384 + ((ch ^ (row & 7)) << 4));
        const size_t o = (size_t)(mt * 128 + row) * FH + nt * 192 + ch * 8;
        const v4u zv = *(const v4u*)(z1 + o);
        float gf[8], zf[8]; unpack8(gv, gf); unpack8(zv, zf);
        *(v4u*)(u + o) = v4u{pk2(zf[0] * gf[0], zf[1] * gf[1]), pk2(zf[2] * gf[2], zf[3] * gf[3]), pk2(zf[4] * gf[4], zf[5] * gf[5]), pk2(zf[6] * gf[6], zf[7] * gf[7])};
    }
}
constexpr int OUT_LDS = GP<128, 64, OUT_NST>::LDS;
__global__ void __launch_bounds__(256, 1) k_out(const u16* __restrict__ u, const u16* __restrict__ Wout, const u16* __restrict__ x1, const float* __restrict__ gb, u16* __restrict__ out) {
    extern __shared__ __attribute__((aligned(16))) unsigned char smem[];
    LAS unsigned char* lds = (LAS unsigned char*)smem;
    const int b = blockIdx.x, mt = (b & 7) * 2 + ((b >> 3) & 1), nt = b >> 4;
    const int lane = lane_id(), wave = wave_id(), wm = wave >> 1, wn = wave & 1;
    GemmStream<128, 64, OUT_NST> gs; gs.init(lds, FH, D_);
    const rsrc_t rA = mk_rsrc(u + (size_t)mt * 128 * FH, 128 * FH * 2), rB = mk_rsrc(Wout + nt * 64, FH * D_ * 2);
    gs.prime(rA, rB);
    f32x16 acc[2][1];
    gs.template tile<0, true>(rA, rB, rA, rB, false, 36, acc);
#pragma unroll
    for (int i = 0; i < 2; ++i) {
        const int row = wm * 64 + i * 32 + (lane & 31);
#pragma unroll
        for (int rg = 0; rg < 4; ++rg) {
            const int col = wn * 32 + 8 * rg + 4 * (lane >> 5);
            const v2u o = {pk2(acc[i][0][4 * rg], acc[i][0][4 * rg + 1]), pk2(acc[i][0][4 * rg + 2], acc[i][0][4 * rg + 3])};
            *(LAS v2u*)(lds + row * 128 + ((((col >> 3) ^ (row & 7)) << 4) | ((col & 4) << 1))) = o;
        }
    }
    __syncthreads();
#pragma unroll
    for (int it = 0; it < 4; ++it) {
        const int id = it * 256 + threadIdx.x, row = id >> 3, ch = id & 7;
        const v4u yv = *(LAS v4u*)(lds + row * 128 + ((ch ^ (row & 7)) << 4));
        const int n = nt * 64 + ch * 8;
        const size_t o = (size_t)(mt * 128 + row) * D_ + n;
        float yf[8], xf[8]; unpack8(yv, yf); unpack8(*(const v4u*)(x1 + o), xf);
        float r[8];
#pragma unroll
        for (int e = 0; e < 8; ++e) r[e] = xf[e] + rbf(gb[n + e] * yf[e]);
        *(v4u*)(out + o) = v4u{pk2(r[0], r[1]), pk2(r[2], r[3]), pk2(r[4], r[5]), pk2(r[6], r[7])};
    }
}

#define HXN 4
#define YXN 2
#define PLACE 1
constexpr int MAIN_LDS = 160 * 1024;
static_assert(HL_END <= MAIN_LDS, "lds");
DI void publish_par(const Params& a) {
    if (threadIdx.x < 64) {
        const int lane = lane_id();
        const rsrc_t rP = mk_rsrc(a.xr + X_BYTES + 8192, 65536);
        unsigned lat[2] = {~0u, ~0u};
        for (int it = 0; it < 10; ++it) {
#pragma unroll
            for (int p = 0; p < 2; ++p) {
                const int off = lane < 8 ? p * 8192 + (it & 7) * 1024 + lane * 16 : OOB;
                __builtin_amdgcn_raw_buffer_store_b128(v4u{0u, 0u, 0u, 0u}, rP, off, 0, AUX_SC1);
                asm volatile("s_waitcnt vmcnt(0)" ::: "memory");
                const unsigned long long t0 = cyc();
                const v4u z = __builtin_amdgcn_raw_buffer_load_b128(rP, off, 0, AUX_SC1);
                asm volatile("s_waitcnt vmcnt(0)" :: "v"(z) : "memory");
                const unsigned d = (unsigned)(cyc() - t0);
                lat[p] = d < lat[p] ? d : lat[p];
            }
        }
        if (lane < 8) __hip_atomic_store(ctl_word(a, C_PAR + lane * 32), 0x100u | (lat[0] < lat[1] ? 1u : 0u), RLX_AGENT);
    }
}
__global__ void __launch_bounds__(256, 1) k_main(Params a) {
    extern __shared__ __attribute__((aligned(16))) unsigned char smem[];
    LAS unsigned char* lds = (LAS unsigned char*)smem;
    const int b = blockIdx.x;
    TM(0);
    if (b == 0 && threadIdx.x < 8) __hip_atomic_store(ctl_word(a, C_CFG + threadIdx.x * 32), 0x100u | ((unsigned)__builtin_amdgcn_s_getreg(20 | (3 << 11)) & 7u), RLX_AGENT);
    {
        const int j = b;
    if (j < 108) { task_mod(a, j, lds); if (j < 36) { wg_signal(a, C_MODA); if (threadIdx.x == 0) (void)__hip_atomic_fetch_add(ctl_word(a, C_MODB), 1u, RLX_AGENT); } else wg_signal(a, C_MODB); }
    }
    if (threadIdx.x == 0) { (void)poll_ge(a, ctl_word(a, C_CFG + (b & 7) * 32), 0x100u); *(LAS unsigned*)lds = __hip_atomic_load(ctl_word(a, C_CFG + (b & 7) * 32), RLX_AGENT); }
    __syncthreads();
    const unsigned cfg = (unsigned)__builtin_amdgcn_readfirstlane((int)*(LAS unsigned*)lds);
    __syncthreads();
    const int x0 = cfg & 7;
    const int l = ((x0 + (b & 7)) & 7) ^ (x0 & 4), r = b >> 3;
    if (l < 2) { if (r < 16) head_role(a, l * 16 + r, lds); else if (l == 0 && r == 16) publish_par(a); }
    else if (l < 4) { if (r < 18) y_role(a, r, l - 2, lds); }
    else worker_role(a, (l - 4) * 32 + r, lds);
}

#define LEAD_H 1100
#define LEAD_Y 600
extern "C" void kernel_launch(void* const* d_in, const int* in_sizes, int n_in, void* d_out, int out_size, void* d_ws, size_t ws_size, hipStream_t stream) {
    const u16* x = (const u16*)d_in[0];
    const u16* Wsp = (const u16*)d_in[9];
    const u16* bsp = (const u16*)d_in[10];
    const u16* Wout = (const u16*)d_in[11];
    unsigned char* ws = (unsigned char*)d_ws;
    float* mod = (float*)(ws + WS_MOD);
    unsigned char* xr = (unsigned char*)(((uintptr_t)ws + WS_END + (1u << 20) + (2u << 20) - 1) & ~(uintptr_t)((2u << 20) - 1));
    (void)hipMemsetAsync(ws + WS_CTL, 0, CTL_BYTES, stream);
    (void)hipMemsetAsync(xr, 0xFF, X_HINT, stream);
    (void)hipMemsetAsync(xr + X_HINT, 0, 16384 + 8192, stream);
    (void)hipFuncSetAttribute((const void*)k_sp, hipFuncAttributeMaxDynamicSharedMemorySize, SP_LDS);
    (void)hipFuncSetAttribute((const void*)k_out, hipFuncAttributeMaxDynamicSharedMemorySize, OUT_LDS);
    (void)hipFuncSetAttribute((const void*)k_main, hipFuncAttributeMaxDynamicSharedMemorySize, MAIN_LDS);
    Params p;
    p.x = x; p.mem0 = (const u16*)d_in[1]; p.Wq = (const u16*)d_in[2]; p.Wk = (const u16*)d_in[3]; p.Wv = (const u16*)d_in[4]; p.Wo = (const u16*)d_in[5];
    p.Win = (const u16*)d_in[8]; p.c = (const u16*)d_in[12]; p.wmod = (const u16*)d_in[13];
    p.ws = ws; p.xr = xr;
    p.lead_h = LEAD_H; p.lead_y = LEAD_Y;
    k_main<<<256, 256, MAIN_LDS, stream>>>(p);
    k_sp<<<192, 256, SP_LDS, stream>>>(Wsp, bsp, (const u16*)(ws + WS_Z2N), (const u16*)(ws + WS_Z1), (u16*)(ws + WS_U));
    k_out<<<144, 256, OUT_LDS, stream>>>((const u16*)(ws + WS_U), Wout, (const u16*)(ws + WS_X1), mod + 5 * D_, (u16*)d_out);
}
```

```cpp
#include <hip/hip_runtime.h>
#include <hip/hip_bf16.h>
#include <stdint.h>

#define GAS __attribute__((address_space(1)))
#define LAS __attribute__((address_space(3)))
#define DI __device__ __forceinline__
typedef unsigned short u16;
typedef __bf16 bf16x2 __attribute__((ext_vector_type(2)));
typedef __bf16 bf16x8 __attribute__((ext_vector_type(8)));
typedef __bf16 bf16x16 __attribute__((ext_vector_type(16)));
typedef short s16x4 __attribute__((ext_vector_type(4)));
typedef float f32x2 __attribute__((ext_vector_type(2)));
typedef float f32x4 __attribute__((ext_vector_type(4)));
typedef float f32x16 __attribute__((ext_vector_type(16)));
typedef unsigned v2u __attribute__((ext_vector_type(2)));
typedef unsigned v4u __attribute__((ext_vector_type(4)));
typedef unsigned v8u __attribute__((ext_vector_type(8)));
typedef GAS unsigned gu32;
typedef __amdgpu_buffer_rsrc_t rsrc_t;
#define RLX_AGENT __ATOMIC_RELAXED, __HIP_MEMORY_SCOPE_AGENT

constexpr int S_ = 2048, D_ = 576, H_ = 32, E_ = 128, HE = 4096, NSEG = 64, FF = 4608, FH = 2304, NRB = 16;
constexpr int AUX_SC1 = 16;
constexpr int RSRC3 = 0x00020000;
constexpr int OOB = 0x7FFFFF00;

constexpr size_t WS_CTL = 0, CTL_BYTES = 131072;
constexpr size_t WS_MOD = CTL_BYTES;
constexpr size_t WS_H1 = WS_MOD + 16384;
constexpr size_t WS_Q = WS_H1 + (size_t)S_ * D_ * 2;
constexpr size_t WS_K = WS_Q + (size_t)S_ * HE * 2;
constexpr size_t WS_VT = WS_K + (size_t)S_ * HE * 2;
constexpr size_t WS_KVM = WS_VT + (size_t)S_ * HE * 2;
constexpr size_t WS_OX = WS_KVM + (size_t)NSEG * H_ * 4096;
constexpr size_t WS_PART = WS_OX + (size_t)S_ * HE * 2;
constexpr size_t WS_X1 = WS_PART + (size_t)4 * S_ * D_ * 4;
constexpr size_t WS_H2 = WS_X1 + (size_t)S_ * D_ * 2;
constexpr size_t WS_Z1 = WS_H2 + (size_t)S_ * D_ * 2;
constexpr size_t WS_Z2 = WS_Z1 + (size_t)S_ * FH * 2;
constexpr size_t WS_Z2N = WS_Z2 + (size_t)S_ * FH * 2;
constexpr size_t WS_U = WS_Z2N + (size_t)S_ * FH * 2;
constexpr size_t WS_END = WS_U + (size_t)S_ * FH * 2;
#define NREP 1
constexpr int X_MEM = 0, X_MEM_REP = 32768, X_MEM_SLOT = NREP * X_MEM_REP;
constexpr int X_O = 3 * X_MEM_SLOT, X_O_SLOT = 131072;
constexpr int X_HINT = X_O + 2 * X_O_SLOT;
constexpr int X_BYTES = X_HINT + 16384;
constexpr int C_TMO = 0, C_MODA = 32, C_MODB = 64, C_H1 = 96, C_OX = 128, C_PART = C_OX + 32 * NRB, C_H2 = C_PART + 32 * NRB, C_Z = C_H2 + 32 * NRB,
              C_KV = 4096, C_Q = C_KV + 16 * NRB * H_, C_END = C_Q + 16 * NRB * H_;
constexpr int C_CFG = 3072, C_PAR = C_CFG + 16;
static_assert(C_Z + 32 * NRB <= 3072 && 3072 + 256 <= C_KV && C_END * 4 <= (int)CTL_BYTES, "control block");

DI unsigned f2u(float x) { return __builtin_bit_cast(unsigned, x); }
DI float u2f(unsigned x) { return __builtin_bit_cast(float, x); }
DI float bf2f(u16 b) { return __builtin_bit_cast(float, (unsigned)b << 16); }
DI float bflo(unsigned w) { return __builtin_bit_cast(float, w << 16); }
DI float bfhi(unsigned w) { return __builtin_bit_cast(float, w & 0xFFFF0000u); }
DI unsigned pk2(float lo, float hi) { f32x2 v = {lo, hi}; bf16x2 b = __builtin_convertvector(v, bf16x2); return __builtin_bit_cast(unsigned, b); }
DI float rbf(float x) { return bflo(pk2(x, 0.f) & 0xFFFFu); }
DI float fadd1(float a, float b) { float r; asm("v_add_f32_e32 %0, %1, %2" : "=v"(r) : "v"(a), "v"(b)); return r; }
DI int lane_id() { return (int)(threadIdx.x & 63); }
DI int wave_id() { return __builtin_amdgcn_readfirstlane((int)(threadIdx.x >> 6)); }

DI f32x4 mfma16(bf16x8 a, bf16x8 b, f32x4 c) { return __builtin_amdgcn_mfma_f32_16x16x32_bf16(a, b, c, 0, 0, 0); }
typedef short s16x4v __attribute__((ext_vector_type(4)));
DI f32x4 mfma16k16(v2u a, v2u b, f32x4 c) { return __builtin_amdgcn_mfma_f32_16x16x16bf16_1k(__builtin_bit_cast(s16x4v, a), __builtin_bit_cast(s16x4v, b), c, 0, 0, 0); }
DI bf16x8 tr2(LAS unsigned char* a0, LAS unsigned char* a1) {
    typedef short s16x8 __attribute__((ext_vector_type(8)));
    const s16x4 lo = __builtin_amdgcn_ds_read_tr16_b64_v4i16((LAS s16x4*)a0), hi = __builtin_amdgcn_ds_read_tr16_b64_v4i16((LAS s16x4*)a1);
    s16x8 v = {lo[0], lo[1], lo[2], lo[3], hi[0], hi[1], hi[2], hi[3]};
    return __builtin_bit_cast(bf16x8, v);
}
DI float xmax16_32(float m) {
    unsigned u = f2u(m);
    auto a = __builtin_amdgcn_permlane16_swap(u, u, false, false);
    m = fmaxf(u2f(a[0]), u2f(a[1]));
    u = f2u(m);
    auto b = __builtin_amdgcn_permlane32_swap(u, u, false, false);
    return fmaxf(u2f(b[0]), u2f(b[1]));
}
DI float xsum16_32(float m) {
    unsigned u = f2u(m);
    auto a = __builtin_amdgcn_permlane16_swap(u, u, false, false);
    m = u2f(a[0]) + u2f(a[1]);
    u = f2u(m);
    auto b = __builtin_amdgcn_permlane32_swap(u, u, false, false);
    return u2f(b[0]) + u2f(b[1]);
}
constexpr float SCALE = 0.08838834764831845f;
constexpr float SCL = 0.08838834764831845f * 1.4426950408889634f;

DI f32x4 smf(bf16x8 a, bf16x16 b, f32x4 c, int idx) { return __builtin_amdgcn_smfmac_f32_16x16x64_bf16(a, b, c, idx, 0, 0); }
DI unsigned nosent(unsigned x) { return x < 0xFFFFFFFEu ? x : 0xFFFFFFFEu; }
DI unsigned umax3(unsigned a, unsigned b, unsigned c) { unsigned r; asm("v_max3_u32 %0, %1, %2, %3" : "=v"(r) : "v"(a), "v"(b), "v"(c)); return r; }
DI unsigned vmax(unsigned acc, v4u v) { return umax3(umax3(acc, v.x, v.y), v.z, v.w); }
DI bool all_valid(v4u v) { return (int)(v.x != ~0u) & (int)(v.y != ~0u) & (int)(v.z != ~0u) & (int)(v.w != ~0u); }
#define CB() asm volatile("" ::: "memory")
#define SMF_A(acc, a, wt, idx) asm volatile("v_smfmac_f32_16x16x64_bf16 %0, %1, %2, %3" : "+v"(acc) : "v"(a), "a"(wt), "v"(idx))
#define SMF_V(acc, a, wt, idx) asm volatile("v_smfmac_f32_16x16x64_bf16 %0, %1, %2, %3" : "+v"(acc) : "v"(a), "v"(wt), "v"(idx))
#define MFMA32_0(d, a, b) asm volatile("v_mfma_f32_16x16x32_bf16 %0, %1, %2, 0" : "=&v"(d) : "v"(a), "v"(b))
#define MFMA32_C(d, a, b, cin) asm volatile("v_mfma_f32_16x16x32_bf16 %0, %1, %2, %3" : "=&v"(d) : "v"(a), "v"(b), "v"(cin))
#define MFMA16_0(d, a, b) asm volatile("v_mfma_f32_16x16x16_bf16 %0, %1, %2, 0" : "=&v"(d) : "v"(a), "v"(b))
#define VMAX3(d, a, b, c) asm volatile("v_max3_f32 %0, %1, %2, %3" : "=v"(d) : "v"(a), "v"(b), "v"(c))
#define VMAX(d, a, b) asm volatile("v_max_f32_e32 %0, %1, %2" : "=v"(d) : "v"(a), "v"(b))
#define VMOV(d, a) asm volatile("v_mov_b32_e32 %0, %1" : "=v"(d) : "v"(a))
#define VADD(d, a, b) asm volatile("v_add_f32_e32 %0, %1, %2" : "=v"(d) : "v"(a), "v"(b))
#define VCVT(d, a, b) asm volatile("v_cvt_pk_bf16_f32 %0, %1, %2" : "=v"(d) : "v"(a), "v"(b))
#define VEXP(x) asm volatile("v_exp_f32_e32 %0, %0" : "+v"(x))
#define SWAP16(a, b) asm volatile("v_permlane16_swap_b32_e32 %0, %1" : "+v"(a), "+v"(b))
#define SWAP32(a, b) asm volatile("v_permlane32_swap_b32_e32 %0, %1" : "+v"(a), "+v"(b))
#define PKFMA(d, a, sc, ms) asm volatile("v_pk_fma_f32 %0, %1, %2, %3 op_sel_hi:[1,0,0] neg_lo:[0,0,1] neg_hi:[0,0,1]" : "=v"(d) : "v"(a), "s"(sc), "v"(ms))
#define PKMUL(d, a, r) asm volatile("v_pk_mul_f32 %0, %1, %2 op_sel_hi:[1,0]" : "=v"(d) : "v"(a), "v"(r))
DI f32x4 zero4() { f32x2 a, b; asm volatile("v_mov_b64 %0, 0" : "=v"(a)); asm volatile("v_mov_b64 %0, 0" : "=v"(b)); return f32x4{a[0], a[1], b[0], b[1]}; }
DI v4u poison4() { v2u a, b; asm volatile("v_mov_b64 %0, -1" : "=v"(a)); asm volatile("v_mov_b64 %0, -1" : "=v"(b)); return v4u{a[0], a[1], b[0], b[1]}; }
#define NOP(n) asm volatile("s_nop " #n)
#define MFMA32_ACC(d, a, b) asm volatile("v_mfma_f32_16x16x32_bf16 %0, %1, %2, %0" : "+v"(d) : "v"(a), "v"(b))
#define MFMA16_ACC(d, a, b) asm volatile("v_mfma_f32_16x16x16_bf16 %0, %1, %2, %0" : "+v"(d) : "v"(a), "v"(b))

struct Params {
    const u16* x; const u16* mem0; const u16* Wq; const u16* Wk; const u16* Wv; const u16* Wo;
    const u16* Win; const u16* c; const u16* wmod;
    unsigned char* ws;
    unsigned char* xr;
    int lead_h, lead_y;
};
DI rsrc_t mk_rsrc(const void* p, unsigned bytes) { return __builtin_amdgcn_make_buffer_rsrc((void*)p, 0, (int)bytes, RSRC3); }
DI void glds16(const void* g, LAS unsigned char* l) { __builtin_amdgcn_global_load_lds((const GAS unsigned*)g, (LAS unsigned*)l, 16, 0, 0); }
DI unsigned long long cyc() { unsigned long long t; asm volatile("s_memtime %0\n\ts_waitcnt lgkmcnt(0)" : "=s"(t) :: "memory"); return t; }
#define TM(slot)
#define TR(base, i)

constexpr unsigned WAIT_CAP = 1u << 16;
DI gu32* ctl_word(const Params& a, int idx) { return (gu32*)(a.ws + WS_CTL) + idx; }
DI void wg_signal(const Params& a, int idx) {
    asm volatile("s_waitcnt vmcnt(0)" ::: "memory");
    __syncthreads();
    if (threadIdx.x == 0) (void)__hip_atomic_fetch_add(ctl_word(a, idx), 1u, RLX_AGENT);
}
DI bool poll_ge(const Params& a, gu32* word, unsigned want) {
    for (unsigned spins = 0;; ++spins) {
        if (__hip_atomic_load(word, RLX_AGENT) >= want) return true;
        __builtin_amdgcn_s_sleep(4);
        if (spins > WAIT_CAP) { __hip_atomic_store(ctl_word(a, C_TMO), 1u, RLX_AGENT); return false; }
        if ((spins & 63u) == 63u && __hip_atomic_load(ctl_word(a, C_TMO), RLX_AGENT) != 0u) return false;
    }
}
template <bool ACQ> DI void wg_wait(const Params& a, gu32* word, unsigned want) {
    if (threadIdx.x == 0) {
        (void)poll_ge(a, word, want);
        if (ACQ) { __builtin_amdgcn_fence(__ATOMIC_ACQUIRE, "agent"); asm volatile("s_waitcnt vmcnt(0)" ::: "memory"); }
        else __builtin_amdgcn_fence(__ATOMIC_ACQUIRE, "wavefront");
    }
    __syncthreads();
}
template <int BM, int BN, int NST> struct GP {
    static constexpr int A_BYTES = BM * 128, B_BYTES = BN * 128, STAGE = A_BYTES + B_BYTES, LDS = NST * STAGE;
    static constexpr int TM = BM / 64, TN = BN / 64, PA = BM / 32, PB = BN / 32, LPS = PA + PB;
    static constexpr int p0(int ks) { return ks * LPS / 4; }
};
template <bool SC1> DI void dma16(unsigned m0v, int voff, rsrc_t r, int soff) {
    if constexpr (SC1) asm volatile("s_mov_b32 m0, %0\n\ts_nop 0\n\tbuffer_load_dwordx4 %1, %2, %3 offen sc1 lds" :: "s"(m0v), "v"(voff), "s"(r), "s"(soff) : "memory");
    else asm volatile("s_mov_b32 m0, %0\n\ts_nop 0\n\tbuffer_load_dwordx4 %1, %2, %3 offen lds" :: "s"(m0v), "v"(voff), "s"(r), "s"(soff) : "memory");
}
template <int N> DI void wait_vm() { asm volatile("s_waitcnt vmcnt(%0)" :: "i"(N) : "memory"); }
#define GSB() __builtin_amdgcn_sched_barrier(0)

template <int BM, int BN, int NST, bool ASC1 = false> struct GemmStream {
    using C = GP<BM, BN, NST>;
    LAS unsigned char* lds; unsigned lbase; int wave, ldb128;
    int voA[C::PA], voB[C::PB];
    int aoff, asw, h, boff[C::TN];
    int slot;
    DI void init(LAS unsigned char* l, int lda, int ldb) {
        lds = l; lbase = (unsigned)(size_t)l; slot = 0; ldb128 = 128 * ldb;
        const int lane = lane_id(); wave = wave_id();
        const int wm = wave >> 1, wn = wave & 1;
#pragma unroll
        for (int p = 0; p < C::PA; ++p) { const int piece = wave + 4 * p, row = piece * 8 + (lane >> 3), c = (lane & 7) ^ ((row >> 1) & 7); voA[p] = (row * lda + c * 8) * 2; }
#pragma unroll
        for (int p = 0; p < C::PB; ++p) { const int piece = wave + 4 * p, sub = piece >> 3, k = (piece & 7) * 8 + (lane >> 3), c = (lane & 7) ^ (((k >> 1) & 1) << 2); voB[p] = (k * ldb + sub * 64 + c * 8) * 2; }
        const int r31 = lane & 31; h = lane >> 5; asw = (r31 >> 1) & 7; aoff = (wm * (BM / 2) + r31) * 128;
        const int blk = (lane >> 4) & 1, q = (lane & 15) >> 2, p = lane & 3;
#pragma unroll
        for (int j = 0; j < C::TN; ++j) { const int n = wn * (BN / 2) + j * 32 + 16 * blk + 4 * p, sub = n >> 6, cn = n & 63; boff[j] = C::A_BYTES + sub * 8192 + (8 * h + q) * 128 + (((cn >> 3) ^ (((q >> 1) & 1) << 2)) << 4) + (cn & 7) * 2; }
    }
    template <int I> DI void piece(rsrc_t rA, rsrc_t rB, int t, int sl) {
        const unsigned sb = lbase + sl * C::STAGE;
        if constexpr (I < C::PA) dma16<ASC1>(sb + (wave + 4 * I) * 1024, voA[I], rA, t * 128);
        else dma16<false>(sb + C::A_BYTES + (wave + 4 * (I - C::PA)) * 1024, voB[I - C::PA], rB, t * ldb128);
    }
    template <int I0, int N> DI void pieces(rsrc_t rA, rsrc_t rB, int t, int sl) {
        if constexpr (N > 0) { piece<I0>(rA, rB, t, sl); pieces<I0 + 1, N - 1>(rA, rB, t, sl); }
    }
    DI void prime(rsrc_t rA, rsrc_t rB) {
#pragma unroll
        for (int t = 0; t < NST - 1; ++t) { int sl = slot + t; if (sl >= NST) sl -= NST; pieces<0, C::LPS>(rA, rB, t, sl); }
    }
    DI void primeB(rsrc_t rB) {
#pragma unroll
        for (int t = 0; t < NST - 1; ++t) { int sl = slot + t; if (sl >= NST) sl -= NST; pieces<C::PA, C::PB>(rB, rB, t, sl); }
    }
    DI void primeA(rsrc_t rA) {
#pragma unroll
        for (int t = 0; t < NST - 1; ++t) { int sl = slot + t; if (sl >= NST) sl -= NST; pieces<0, C::PA>(rA, rA, t, sl); }
    }
    DI void ldfrags(LAS unsigned char* cur, int ks, bf16x8 (&af)[C::TM], bf16x8 (&bf)[C::TN]) {
#pragma unroll
        for (int i = 0; i < C::TM; ++i) af[i] = *(LAS bf16x8*)(cur + aoff + i * 4096 + (((2 * ks + h) ^ asw) << 4));
#pragma unroll
        for (int j = 0; j < C::TN; ++j) bf[j] = tr2(cur + boff[j] + ks * 2048, cur + boff[j] + ks * 2048 + 512);
    }
    static constexpr int NM = C::TM * C::TN;
    template <int K, int P0> DI void job(LAS unsigned char* src, int ksn, bool rd, bf16x8 (&an)[C::TM], bf16x8 (&bn)[C::TN], bool issue, rsrc_t sA, rsrc_t sB, int ts, int sp) {
        if constexpr (K < C::TN) { if (rd) bn[K] = tr2(src + boff[K] + ksn * 2048, src + boff[K] + ksn * 2048 + 512); }
        else if constexpr (K < C::TN + C::TM) { if (rd) an[K - C::TN] = *(LAS bf16x8*)(src + aoff + (K - C::TN) * 4096 + (((2 * ksn + h) ^ asw) << 4)); }
        else { if (issue) piece<P0 + K - C::TN - C::TM>(sA, sB, ts, sp); }
    }
    template <int M, int K, int P0, int NJ> DI void jobs_after(LAS unsigned char* src, int ksn, bool rd, bf16x8 (&an)[C::TM], bf16x8 (&bn)[C::TN], bool issue, rsrc_t sA, rsrc_t sB, int ts, int sp) {
        if constexpr (K < NJ) {
            if constexpr (K * NM / NJ == M) job<K, P0>(src, ksn, rd, an, bn, issue, sA, sB, ts, sp);
            jobs_after<M, K + 1, P0, NJ>(src, ksn, rd, an, bn, issue, sA, sB, ts, sp);
        }
    }
    template <int M, int KS, bool SWAP> DI void kstep(const bf16x8 (&ac)[C::TM], const bf16x8 (&bc)[C::TN], f32x16 (&acc)[C::TM][C::TN], LAS unsigned char* src, int ksn, bool rd,
                                           bf16x8 (&an)[C::TM], bf16x8 (&bn)[C::TN], bool issue, rsrc_t sA, rsrc_t sB, int ts, int sp) {
        if constexpr (M < NM) {
            constexpr int i = M / C::TN, j = M % C::TN;
            acc[i][j] = SWAP ? __builtin_amdgcn_mfma_f32_32x32x16_bf16(bc[j], ac[i], acc[i][j], 0, 0, 0) : __builtin_amdgcn_mfma_f32_32x32x16_bf16(ac[i], bc[j], acc[i][j], 0, 0, 0);
            GSB();
            jobs_after<M, 0, C::p0(KS), C::TM + C::TN + C::p0(KS + 1) - C::p0(KS)>(src, ksn, rd, an, bn, issue, sA, sB, ts, sp);
            GSB();
            kstep<M + 1, KS, SWAP>(ac, bc, acc, src, ksn, rd, an, bn, issue, sA, sB, ts, sp);
        }
    }
    template <bool SWAP, bool SPLITP>
    DI void tile2(rsrc_t rA, rsrc_t rB, int nt, f32x16 (&acc)[C::TM][C::TN]) {
#pragma unroll
        for (int i = 0; i < C::TM; ++i)
#pragma unroll
            for (int j = 0; j < C::TN; ++j)
#pragma unroll
                for (int r = 0; r < 16; ++r) acc[i][j][r] = 0.f;
        bf16x8 a0[C::TM], b0[C::TN], a1[C::TM], b1[C::TN], a2[C::TM], b2[C::TN], a3[C::TM], b3[C::TN];
        if constexpr (SPLITP) wait_vm<(NST - 2) * C::PA>(); else wait_vm<(NST - 2) * C::LPS>();
        asm volatile("s_barrier" ::: "memory");
        ldfrags(lds + slot * C::STAGE, 0, a0, b0);
        ldfrags(lds + slot * C::STAGE, 1, a1, b1);
        for (int t = 0; t < nt; ++t) {
            LAS unsigned char* cur = lds + slot * C::STAGE;
            int sn = slot + 1; if (sn >= NST) sn -= NST;
            int sp = slot + NST - 1; if (sp >= NST) sp -= NST;
            const int tn = t + NST - 1;
            const bool issue = tn < nt, more = t + 1 < nt;
            kstep<0, 0, SWAP>(a0, b0, acc, cur, 2, true, a2, b2, issue, rA, rB, tn, sp);
            kstep<0, 1, SWAP>(a1, b1, acc, cur, 3, true, a3, b3, issue, rA, rB, tn, sp);
            if (more) {
                if (!issue) wait_vm<0>();
                else if (SPLITP && t + 1 <= NST - 2) { if (t == 0) wait_vm<(NST - 3) * C::PA + C::p0(2)>(); else wait_vm<C::p0(2)>(); }
                else wait_vm<(NST - 3) * C::LPS + C::p0(2)>();
            }
            asm volatile("s_waitcnt lgkmcnt(0)\n\ts_barrier" ::: "memory");
            kstep<0, 2, SWAP>(a2, b2, acc, lds + sn * C::STAGE, 0, more, a0, b0, issue, rA, rB, tn, sp);
            kstep<0, 3, SWAP>(a3, b3, acc, lds + sn * C::STAGE, 1, more, a1, b1, issue, rA, rB, tn, sp);
            slot = sn;
        }
    }
    template <int EXTRA, bool SWAP, bool SPLITP = false>
    DI void tile(rsrc_t rA, rsrc_t rB, rsrc_t nA, rsrc_t nB, bool has_next, int nt, f32x16 (&acc)[C::TM][C::TN]) {
#pragma unroll
        for (int i = 0; i < C::TM; ++i)
#pragma unroll
            for (int j = 0; j < C::TN; ++j)
#pragma unroll
                for (int r = 0; r < 16; ++r) acc[i][j][r] = 0.f;
        bf16x8 a0[C::TM], b0[C::TN], a1[C::TM], b1[C::TN];
        if constexpr (SPLITP) wait_vm<(NST - 2) * C::PA>(); else wait_vm<(NST - 2) * C::LPS + EXTRA>();
        asm volatile("s_barrier" ::: "memory");
        ldfrags(lds + slot * C::STAGE, 0, a0, b0);
        for (int t = 0; t < nt; ++t) {
            LAS unsigned char* cur = lds + slot * C::STAGE;
            int sn = slot + 1; if (sn >= NST) sn -= NST;
            int sp = slot + NST - 1; if (sp >= NST) sp -= NST;
            const int tn = t + NST - 1;
            const bool in_cur = tn < nt, issue = in_cur || has_next;
            const rsrc_t sA = in_cur ? rA : nA, sB = in_cur ? rB : nB;
            const int ts = in_cur ? tn : tn - nt;
            kstep<0, 0, SWAP>(a0, b0, acc, cur, 1, true, a1, b1, issue, sA, sB, ts, sp);
            kstep<0, 1, SWAP>(a1, b1, acc, cur, 2, true, a0, b0, issue, sA, sB, ts, sp);
            kstep<0, 2, SWAP>(a0, b0, acc, cur, 3, true, a1, b1, issue, sA, sB, ts, sp);
            if (t + 1 < nt) {
                if (!issue) wait_vm<0>();
                else if (t + 1 <= NST - 2) { if constexpr (SPLITP) { if (t == 0) wait_vm<(NST - 3) * C::PA + C::p0(3)>(); else wait_vm<C::p0(3)>(); } else wait_vm<(NST - 3) * C::LPS + C::p0(3) + EXTRA>(); }
                else wait_vm<(NST - 3) * C::LPS + C::p0(3)>();
            }
            asm volatile("s_waitcnt lgkmcnt(0)\n\ts_barrier" ::: "memory");
            kstep<0, 3, SWAP>(a1, b1, acc, lds + sn * C::STAGE, 0, t + 1 < nt, a0, b0, issue, sA, sB, ts, sp);
            slot = sn;
        }
    }
};

DI unsigned wave_poll_ge(const Params& a, gu32* word, unsigned want) {
    unsigned v;
    for (unsigned spins = 0;; ++spins) {
        v = (unsigned)__builtin_amdgcn_readfirstlane((int)__hip_atomic_load(word, RLX_AGENT));
        if (v >= want) break;
        __builtin_amdgcn_s_sleep(4);
        if (spins > WAIT_CAP) { __hip_atomic_store(ctl_word(a, C_TMO), 1u, RLX_AGENT); break; }
        if ((spins & 63u) == 63u && __builtin_amdgcn_readfirstlane((int)__hip_atomic_load(ctl_word(a, C_TMO), RLX_AGENT)) != 0) break;
    }
    __builtin_amdgcn_fence(__ATOMIC_ACQUIRE, "wavefront");
    return v;
}
DI int get_par(const Params& a) { return (int)(wave_poll_ge(a, ctl_word(a, C_PAR + (blockIdx.x & 7) * 32), 0x100u) & 1u); }
DI float wsum8(float v) { v += __shfl_xor(v, 1); v += __shfl_xor(v, 2); v += __shfl_xor(v, 4); return v; }
DI void unpack8(v4u w, float (&f)[8]) { f[0] = bflo(w.x); f[1] = bfhi(w.x); f[2] = bflo(w.y); f[3] = bfhi(w.y); f[4] = bflo(w.z); f[5] = bfhi(w.z); f[6] = bflo(w.w); f[7] = bfhi(w.w); }

DI void task_mod(const Params& a, int task, LAS unsigned char* lds) {
    LAS float* red = (LAS float*)lds;
    const int tid = threadIdx.x, cg = tid & 3, kr = tid >> 2;
    const int col0 = task * 32 + cg * 8;
    float acc[8];
#pragma unroll
    for (int j = 0; j < 8; ++j) acc[j] = 0.f;
    v4u w[9];
#pragma unroll
    for (int p = 0; p < 9; ++p) w[p] = *(const v4u*)(a.wmod + (size_t)(kr + 64 * p) * 3456 + col0);
#pragma unroll
    for (int p = 0; p < 9; ++p) {
        const float cv = bf2f(a.c[kr + 64 * p]);
        const float sv = rbf(cv / (1.f + __expf(-cv)));
        float f[8]; unpack8(w[p], f);
#pragma unroll
        for (int j = 0; j < 8; ++j) acc[j] += sv * f[j];
    }
#pragma unroll
    for (int j = 0; j < 8; ++j) red[kr * 33 + cg * 8 + j] = acc[j];
    __syncthreads();
    if (tid < 32) {
        float s = 0.f;
        for (int r = 0; r < 64; ++r) s += red[r * 33 + tid];
        __hip_atomic_store((gu32*)(a.ws + WS_MOD) + task * 32 + tid, f2u(rbf(s)), RLX_AGENT);
    }
}
DI void load_modtab(const Params& a, LAS float* tab, int i_shift, int i_scale, int i_gate) {
    const rsrc_t rM = mk_rsrc(a.ws + WS_MOD, 3456 * 4);
    for (int i = threadIdx.x; i < D_; i += 256) {
        tab[i] = rbf(1.f + u2f(__builtin_amdgcn_raw_buffer_load_b32(rM, (i_scale * D_ + i) * 4, 0, AUX_SC1)));
        tab[D_ + i] = u2f(__builtin_amdgcn_raw_buffer_load_b32(rM, (i_shift * D_ + i) * 4, 0, AUX_SC1));
        tab[2 * D_ + i] = i_gate >= 0 ? u2f(__builtin_amdgcn_raw_buffer_load_b32(rM, (i_gate * D_ + i) * 4, 0, AUX_SC1)) : 0.f;
    }
    __syncthreads();
}
template <bool RES>
DI void task_ln(const Params& a, int row0, LAS float* tab, size_t out_off) {
    const int lane = lane_id(), w = wave_id();
    const int row = row0 + 8 * w + (lane >> 3), sub = lane & 7;
    float v[9][8];
#pragma unroll
    for (int i = 0; i < 9; ++i) unpack8(*(const v4u*)(a.x + (size_t)row * D_ + (sub + 8 * i) * 8), v[i]);
    if (RES) {
        const rsrc_t rP = mk_rsrc(a.ws + WS_PART + (size_t)(row >> 7) * 4 * 128 * D_ * 4, 4 * 128 * D_ * 4);
        const rsrc_t rX1 = mk_rsrc(a.ws + WS_X1, S_ * D_ * 2);
#pragma unroll
        for (int i = 0; i < 9; ++i) {
            float s[8];
#pragma unroll
            for (int j = 0; j < 8; ++j) s[j] = 0.f;
#pragma unroll
            for (int kq = 0; kq < 4; ++kq) {
                const int off = ((kq * 128 + (row & 127)) * D_ + (sub + 8 * i) * 8) * 4;
                const v4u p0 = __builtin_amdgcn_raw_buffer_load_b128(rP, off, 0, AUX_SC1), p1 = __builtin_amdgcn_raw_buffer_load_b128(rP, off + 16, 0, AUX_SC1);
                s[0] += u2f(p0.x); s[1] += u2f(p0.y); s[2] += u2f(p0.z); s[3] += u2f(p0.w); s[4] += u2f(p1.x); s[5] += u2f(p1.y); s[6] += u2f(p1.z); s[7] += u2f(p1.w);
            }
#pragma unroll
            for (int j = 0; j < 8; ++j) v[i][j] = rbf(v[i][j] + rbf(tab[2 * D_ + (sub + 8 * i) * 8 + j] * rbf(s[j])));
            const v4u o = {pk2(v[i][0], v[i][1]), pk2(v[i][2], v[i][3]), pk2(v[i][4], v[i][5]), pk2(v[i][6], v[i][7])};
            __builtin_amdgcn_raw_buffer_store_b128(o, rX1, (row * D_ + (sub + 8 * i) * 8) * 2, 0, AUX_SC1);
        }
    }
    float s = 0.f;
#pragma unroll
    for (int i = 0; i < 9; ++i)
#pragma unroll
        for (int j = 0; j < 8; ++j) s += v[i][j];
    const float mean = wsum8(s) * (1.f / D_);
    float q = 0.f;
#pragma unroll
    for (int i = 0; i < 9; ++i)
#pragma unroll
        for (int j = 0; j < 8; ++j) { const float d = v[i][j] - mean; q += d * d; }
    const float meanb = rbf(mean), rden = 1.f / rbf(sqrtf(rbf(rbf(wsum8(q) * (1.f / D_)) + 1e-5f)));
    const rsrc_t rO = mk_rsrc(a.ws + out_off, S_ * D_ * 2);
#pragma unroll
    for (int i = 0; i < 9; ++i) {
        float o[8];
#pragma unroll
        for (int j = 0; j < 8; ++j) { const int cc = (sub + 8 * i) * 8 + j; o[j] = rbf(rbf(rbf(v[i][j] - meanb) * rden) * tab[cc]) + tab[D_ + cc]; }
        const v4u ov = {pk2(o[0], o[1]), pk2(o[2], o[3]), pk2(o[4], o[5]), pk2(o[6], o[7])};
        __builtin_amdgcn_raw_buffer_store_b128(ov, rO, (row * D_ + (sub + 8 * i) * 8) * 2, 0, AUX_SC1);
    }
}
DI float wsum64(float v) {
#pragma unroll
    for (int m = 1; m < 64; m <<= 1) v += __shfl_xor(v, m);
    return v;
}
template <int NR>
DI void task_ln2(const Params& a, int row0, LAS float* tab) {
    const int lane = lane_id(), w = wave_id();
    const rsrc_t rX1 = mk_rsrc(a.ws + WS_X1, S_ * D_ * 2), rH2 = mk_rsrc(a.ws + WS_H2, S_ * D_ * 2);
    const bool act = lane < 48;
    v4u p[NR][3][4]; v2u xv[NR][3];
#pragma unroll
    for (int it = 0; it < NR; ++it) {
        const int row = row0 + w * NR + it;
        const rsrc_t rP = mk_rsrc(a.ws + WS_PART + (size_t)(row >> 7) * 4 * 128 * D_ * 4, 4 * 128 * D_ * 4);
#pragma unroll
        for (int i = 0; i < 3; ++i) {
            const int col = (lane + 48 * i) * 4;
            xv[it][i] = act ? *(const v2u*)(a.x + (size_t)row * D_ + col) : v2u{0u, 0u};
#pragma unroll
            for (int kq = 0; kq < 4; ++kq) p[it][i][kq] = __builtin_amdgcn_raw_buffer_load_b128(rP, act ? ((kq * 128 + (row & 127)) * D_ + col) * 4 : OOB, 0, AUX_SC1);
        }
    }
#pragma unroll
    for (int it = 0; it < NR; ++it) {
        const int row = row0 + w * NR + it;
        float v[3][4]; float s = 0.f;
#pragma unroll
        for (int i = 0; i < 3; ++i) {
            const int col = act ? (lane + 48 * i) * 4 : 0;
            const float xs[4] = {bflo(xv[it][i].x), bfhi(xv[it][i].x), bflo(xv[it][i].y), bfhi(xv[it][i].y)};
#pragma unroll
            for (int j = 0; j < 4; ++j) {
                const float t = (u2f(p[it][i][0][j]) + u2f(p[it][i][1][j])) + (u2f(p[it][i][2][j]) + u2f(p[it][i][3][j]));
                v[i][j] = act ? rbf(xs[j] + rbf(tab[2 * D_ + col + j] * rbf(t))) : 0.f;
                s += v[i][j];
            }
        }
        const float mean = wsum64(s) * (1.f / D_);
        float q = 0.f;
#pragma unroll
        for (int i = 0; i < 3; ++i)
#pragma unroll
            for (int j = 0; j < 4; ++j) { const float d = act ? v[i][j] - mean : 0.f; q += d * d; }
        const float meanb = rbf(mean), rden = 1.f / rbf(sqrtf(rbf(rbf(wsum64(q) * (1.f / D_)) + 1e-5f)));
#pragma unroll
        for (int i = 0; i < 3; ++i) {
            const int col = act ? (lane + 48 * i) * 4 : 0;
            float o[4];
#pragma unroll
            for (int j = 0; j < 4; ++j) o[j] = rbf(rbf(rbf(v[i][j] - meanb) * rden) * tab[col + j]) + tab[D_ + col + j];
            const unsigned a0 = pk2(v[i][0], v[i][1]), a1 = pk2(v[i][2], v[i][3]), b0 = pk2(o[0], o[1]), b1 = pk2(o[2], o[3]);
            const v4u xo = {a0, a1, (unsigned)__shfl_down((int)a0, 1), (unsigned)__shfl_down((int)a1, 1)}, ho = {b0, b1, (unsigned)__shfl_down((int)b0, 1), (unsigned)__shfl_down((int)b1, 1)};
            const int off = (act && (lane & 1) == 0) ? (row * D_ + col) * 2 : OOB;
            __builtin_amdgcn_raw_buffer_store_b128(xo, rX1, off, 0, AUX_SC1);
            __builtin_amdgcn_raw_buffer_store_b128(ho, rH2, off, 0, AUX_SC1);
        }
    }
}
template <int NIT>
DI void task_ln3(const Params& a, int row0) {
    const int lane = lane_id(), w = wave_id(), sub = lane & 31;
    const rsrc_t rZ = mk_rsrc(a.ws + WS_Z2, S_ * FH * 2), rN = mk_rsrc(a.ws + WS_Z2N, S_ * FH * 2);
    v4u raw[NIT][9];
#pragma unroll
    for (int it = 0; it < NIT; ++it)
#pragma unroll
        for (int i = 0; i < 9; ++i) raw[it][i] = __builtin_amdgcn_raw_buffer_load_b128(rZ, ((row0 + 2 * NIT * w + 2 * it + (lane >> 5)) * FH + (sub + 32 * i) * 8) * 2, 0, AUX_SC1);
#pragma unroll
    for (int it = 0; it < NIT; ++it) {
        const int row = row0 + 2 * NIT * w + 2 * it + (lane >> 5);
        float v[9][8];
#pragma unroll
        for (int i = 0; i < 9; ++i) unpack8(raw[it][i], v[i]);
        float s = 0.f;
#pragma unroll
        for (int i = 0; i < 9; ++i)
#pragma unroll
            for (int j = 0; j < 8; ++j) s += v[i][j];
#pragma unroll
        for (int m = 1; m < 32; m <<= 1) s += __shfl_xor(s, m);
        const float mean = s * (1.f / FH);
        float q = 0.f;
#pragma unroll
        for (int i = 0; i < 9; ++i)
#pragma unroll
            for (int j = 0; j < 8; ++j) { const float d = v[i][j] - mean; q += d * d; }
#pragma unroll
        for (int m = 1; m < 32; m <<= 1) q += __shfl_xor(q, m);
        const float meanb = rbf(mean), rden = 1.f / rbf(sqrtf(rbf(rbf(q * (1.f / FH)) + 1e-5f)));
#pragma unroll
        for (int i = 0; i < 9; ++i) {
            float o[8];
#pragma unroll
            for (int j = 0; j < 8; ++j) o[j] = rbf(v[i][j] - meanb) * rden;
            const v4u ov = {pk2(o[0], o[1]), pk2(o[2], o[3]), pk2(o[4], o[5]), pk2(o[6], o[7])};
            __builtin_amdgcn_raw_buffer_store_b128(ov, rN, (row * FH + (sub + 32 * i) * 8) * 2, 0, AUX_SC1);
        }
    }
}

constexpr int EPI_STORES = 8;
template <class F> DI void epi_tile128(const f32x16 (&acc)[2][2], LAS unsigned char* img, bool swapped, rsrc_t rO, int dst, int rstride, F&& f) {
    const int lane = lane_id(), wave = wave_id(), wm = wave >> 1, wn = wave & 1;
#pragma unroll
    for (int i = 0; i < 2; ++i)
#pragma unroll
        for (int j = 0; j < 2; ++j)
#pragma unroll
            for (int rg = 0; rg < 4; ++rg) {
                int row, col;
                if (swapped) { row = wm * 64 + i * 32 + (lane & 31); col = wn * 64 + j * 32 + 8 * rg + 4 * (lane >> 5); }
                else {
                    row = wn * 64 + j * 32 + (lane & 31);
                    const int tk = 8 * rg + 4 * (lane >> 5);
                    col = wm * 64 + i * 32 + (tk < 16 ? 2 * tk : 2 * (tk - 16) + 4);
                }
                const v2u o = {pk2(f(acc[i][j][4 * rg]), f(acc[i][j][4 * rg + 1])), pk2(f(acc[i][j][4 * rg + 2]), f(acc[i][j][4 * rg + 3]))};
                *(LAS v2u*)(img + row * 256 + ((((col >> 3) ^ (row & 7)) << 4) | ((col & 4) << 1))) = o;
            }
    asm volatile("s_waitcnt lgkmcnt(0)\n\ts_barrier" ::: "memory");
#pragma unroll
    for (int it = 0; it < EPI_STORES; ++it) {
        const int id = it * 256 + threadIdx.x, row = id >> 4, ch = id & 15;
        const v4u v = *(LAS v4u*)(img + row * 256 + ((ch ^ (row & 7)) << 4));
        __builtin_amdgcn_raw_buffer_store_b128(v, rO, dst + row * rstride + ch * 16, 0, AUX_SC1);
    }
    asm volatile("s_waitcnt lgkmcnt(0)\n\ts_barrier" ::: "memory");
}

DI float gelu_tanh(float x) {
    const float t = x * (1.f + 0.044715f * x * x);
    return x * __builtin_amdgcn_rcpf(1.f + __builtin_amdgcn_exp2f(t * (-2.f * 0.7978845608028654f * 1.4426950408889634f)));
}

DI void qkv_decode(int t, int& rb, int& head, int& mat) {
    if (t < 2 * NRB * H_) { rb = t >> 6; head = (t >> 1) & 31; mat = 1 + (t & 1); }
    else { const int u = t - 2 * NRB * H_; rb = u >> 5; head = u & 31; mat = 0; }
}
DI void phase_qkv(const Params& a, int j, int nw, LAS unsigned char* lds) {
    using GS = GemmStream<128, 128, 3, true>;
    GS gs; gs.init(lds, D_, HE);
    LAS unsigned char* img = lds + GS::C::LDS;
    constexpr int NT = 3 * NRB * H_;
    auto rA = [&](int t) { int rb, h, m; qkv_decode(t, rb, h, m); return mk_rsrc(a.ws + WS_H1 + (size_t)rb * 128 * D_ * 2, 128 * D_ * 2); };
    auto rB = [&](int t) { int rb, h, m; qkv_decode(t, rb, h, m); return mk_rsrc((m == 0 ? a.Wq : (m == 1 ? a.Wk : a.Wv)) + h * 128, D_ * HE * 2); };
    if (j >= NT) return;
    gs.prime(rA(j), rB(j));
    bool first = true;
    for (int t = j; t < NT; t += nw) {
        int rb, head, mat; qkv_decode(t, rb, head, mat);
        const bool hn = t + nw < NT; const int tn = hn ? t + nw : t;
        f32x16 acc[2][2];
        if (mat == 2) { if (first) gs.template tile<0, false>(rA(t), rB(t), rA(tn), rB(tn), hn, 9, acc); else gs.template tile<EPI_STORES, false>(rA(t), rB(t), rA(tn), rB(tn), hn, 9, acc); }
        else          { if (first) gs.template tile<0, true>(rA(t), rB(t), rA(tn), rB(tn), hn, 9, acc);  else gs.template tile<EPI_STORES, true>(rA(t), rB(t), rA(tn), rB(tn), hn, 9, acc); }
        first = false;
        if (mat == 2) epi_tile128(acc, img, false, mk_rsrc(a.ws + WS_VT, S_ * HE * 2), (head * E_ * S_ + rb * 128) * 2, S_ * 2, [](float x) { return x; });
        else epi_tile128(acc, img, true, mk_rsrc(a.ws + (mat == 0 ? WS_Q : WS_K), S_ * HE * 2), (head * S_ + rb * 128) * E_ * 2, E_ * 2, [](float x) { return x; });
        wg_signal(a, (mat == 0 ? C_Q : C_KV) + (rb * H_ + head) * 16);
    }
}

DI void xattn_unit(const Params& a, int seg, int head, LAS unsigned char* wl  ) {
    const int lane = lane_id(), c = lane & 15, g = lane >> 4;
    const rsrc_t rQ = mk_rsrc(a.ws + WS_Q, S_ * HE * 2), rK = mk_rsrc(a.ws + WS_K, S_ * HE * 2), rV = mk_rsrc(a.ws + WS_VT, S_ * HE * 2);
    const rsrc_t rM = mk_rsrc(a.ws + WS_KVM, NSEG * H_ * 4096), rO = mk_rsrc(a.ws + WS_OX, S_ * HE * 2);
    const int tok0 = head * S_ + seg * 32;
    const int km = (seg * H_ + head) * 4096;
    {
        const v4u x0 = __builtin_amdgcn_raw_buffer_load_b128(rM, km + lane * 16, 0, AUX_SC1), x1 = __builtin_amdgcn_raw_buffer_load_b128(rM, km + 1024 + lane * 16, 0, AUX_SC1);
        *(LAS v4u*)(wl + lane * 16) = x0; *(LAS v4u*)(wl + 1024 + lane * 16) = x1;
    }
    v4u qf[2][4], kf[2][4];
#pragma unroll
    for (int t = 0; t < 2; ++t)
#pragma unroll
        for (int ks = 0; ks < 4; ++ks) {
            qf[t][ks] = __builtin_amdgcn_raw_buffer_load_b128(rQ, ((tok0 + 16 * t + c) * E_ + 32 * ks + 8 * g) * 2, 0, AUX_SC1);
            kf[t][ks] = __builtin_amdgcn_raw_buffer_load_b128(rK, ((tok0 + 16 * t + c) * E_ + 32 * ks + 8 * g) * 2, 0, AUX_SC1);
        }
    v4u vf[8]; v2u vmf[8];
#pragma unroll
    for (int et = 0; et < 8; ++et) {
        const int e = 16 * et + c;
        vf[et] = __builtin_amdgcn_raw_buffer_load_b128(rV, ((head * E_ + e) * S_ + seg * 32 + 8 * g) * 2, 0, AUX_SC1);
        vmf[et] = __builtin_amdgcn_raw_buffer_load_b64(rM, km + 2048 + e * 16 + (g & 1) * 8, 0, AUX_SC1);
    }
    __builtin_amdgcn_s_waitcnt(0xC07F);
    __builtin_amdgcn_wave_barrier();
    const int q = c >> 2, p = c & 3;
    f32x4 st[2][3];
#pragma unroll
    for (int t = 0; t < 2; ++t)
#pragma unroll
        for (int kt = 0; kt < 3; ++kt) st[t][kt] = f32x4{0, 0, 0, 0};
#pragma unroll
    for (int ks = 0; ks < 4; ++ks) {
        const int e0 = 32 * ks + 8 * g + q;
        const bf16x8 km8 = tr2(wl + e0 * 16 + (p & 1) * 8, wl + (e0 + 4) * 16 + (p & 1) * 8);
#pragma unroll
        for (int t = 0; t < 2; ++t) {
            const bf16x8 qq = __builtin_bit_cast(bf16x8, qf[t][ks]);
            st[t][0] = mfma16(km8, qq, st[t][0]);
            st[t][1] = mfma16(__builtin_bit_cast(bf16x8, kf[0][ks]), qq, st[t][1]);
            st[t][2] = mfma16(__builtin_bit_cast(bf16x8, kf[1][ks]), qq, st[t][2]);
        }
    }
    bf16x8 pf[2]; v2u pm[2]; float rz[2];
#pragma unroll
    for (int t = 0; t < 2; ++t) {
        const int qi = 16 * t + c;
        float s[12];
#pragma unroll
        for (int r = 0; r < 4; ++r) {
            s[r] = g < 2 ? rbf(rbf(st[t][0][r]) * SCALE) : -INFINITY;
            s[4 + r] = (4 * g + r) <= qi ? rbf(rbf(st[t][1][r]) * SCALE) : -INFINITY;
            s[8 + r] = (16 + 4 * g + r) <= qi ? rbf(rbf(st[t][2][r]) * SCALE) : -INFINITY;
        }
        float m = s[0];
#pragma unroll
        for (int r = 1; r < 12; ++r) m = fmaxf(m, s[r]);
        m = xmax16_32(m);
        float z = 0.f;
#pragma unroll
        for (int r = 0; r < 12; ++r) { s[r] = __builtin_amdgcn_exp2f((s[r] - m) * 1.4426950408889634f); z += s[r]; }
        z = xsum16_32(z);
        const float rzz = 1.f / z;
#pragma unroll
        for (int r = 0; r < 12; ++r) s[r] *= rzz;
        rz[t] = 1.f;
        pm[t] = v2u{pk2(s[0], s[1]), pk2(s[2], s[3])};
        const v4u pp = {pk2(s[4], s[5]), pk2(s[6], s[7]), pk2(s[8], s[9]), pk2(s[10], s[11])};
        pf[t] = __builtin_bit_cast(bf16x8, pp);
    }
#pragma unroll
    for (int ep = 0; ep < 4; ++ep) {
        f32x4 o[2][2];
#pragma unroll
        for (int et = 0; et < 2; ++et)
#pragma unroll
            for (int t = 0; t < 2; ++t) {
                o[et][t] = mfma16(__builtin_bit_cast(bf16x8, vf[2 * ep + et]), pf[t], f32x4{0, 0, 0, 0});
                o[et][t] = mfma16k16(vmf[2 * ep + et], pm[t], o[et][t]);
            }
#pragma unroll
        for (int t = 0; t < 2; ++t) {
            const unsigned d0 = pk2(o[0][t][0] * rz[t], o[0][t][1] * rz[t]), d1 = pk2(o[0][t][2] * rz[t], o[0][t][3] * rz[t]);
            const unsigned e0 = pk2(o[1][t][0] * rz[t], o[1][t][1] * rz[t]), e1 = pk2(o[1][t][2] * rz[t], o[1][t][3] * rz[t]);
            auto s0 = __builtin_amdgcn_permlane16_swap(d0, e0, false, false);
            auto s1 = __builtin_amdgcn_permlane16_swap(d1, e1, false, false);
            const v4u val = {s0[0], s1[0], s0[1], s1[1]};
            const int eo = 32 * ep + ((g & 1) << 4) + ((g >> 1) << 3);
            __builtin_amdgcn_raw_buffer_store_b128(val, rO, ((seg * 32 + 16 * t + c) * HE + head * E_ + eo) * 2, 0, AUX_SC1);
        }
    }
}
DI void role_att(const Params& a, int head, LAS unsigned char* lds) {
    const int w = wave_id(), lane = lane_id();
    const int par = get_par(a);
    for (int rb = 0; rb < NRB; ++rb) {
        const int seg = 4 * rb + w;
        (void)wave_poll_ge(a, ctl_word(a, C_KV + (rb * H_ + head) * 16), 2u); (void)wave_poll_ge(a, ctl_word(a, C_Q + (rb * H_ + head) * 16), 1u);
        (void)wave_poll_ge(a, (gu32*)(a.xr + par * 8192 + X_HINT + 8192 + head * 256), (unsigned)(seg + 2));
        xattn_unit(a, seg, head, lds + w * 2048);
        wg_signal(a, C_OX + rb * 32);
    }
}
DI void role_wo(const Params& a, int task, LAS unsigned char* lds) {
    using GS = GemmStream<128, 64, 4, true>;
    GS gs; gs.init(lds, HE, D_);
    const int kq = task & 3, nt = task >> 2;
    const int lane = lane_id(), wave = wave_id(), wm = wave >> 1, wn = wave & 1;
    const rsrc_t rB = mk_rsrc(a.Wo + (size_t)kq * 1024 * D_ + nt * 64, 1024 * D_ * 2);
    for (int rb = 0; rb < NRB; ++rb) {
        gs.primeB(rB);
        wg_wait<false>(a, ctl_word(a, C_OX + rb * 32), 32u);
        if (task == 0) TR(2100, rb * 4);
        const rsrc_t rA = mk_rsrc(a.ws + WS_OX + ((size_t)rb * 128 * HE + kq * 1024) * 2, 128 * HE * 2);
        gs.primeA(rA);
        f32x16 acc[2][1];
        gs.template tile2<true, true>(rA, rB, 16, acc);
        if (task == 0) TR(2100, rb * 4 + 1);
        const rsrc_t rP = mk_rsrc(a.ws + WS_PART + (size_t)(rb * 4 + kq) * 128 * D_ * 4, 128 * D_ * 4);
#pragma unroll
        for (int i = 0; i < 2; ++i)
#pragma unroll
            for (int rg = 0; rg < 4; ++rg) {
                const int m = wm * 64 + i * 32 + (lane & 31), n = nt * 64 + wn * 32 + 8 * rg + 4 * (lane >> 5);
                const v4u o = {f2u(acc[i][0][4 * rg]), f2u(acc[i][0][4 * rg + 1]), f2u(acc[i][0][4 * rg + 2]), f2u(acc[i][0][4 * rg + 3])};
                __builtin_amdgcn_raw_buffer_store_b128(o, rP, (m * D_ + n) * 4, 0, AUX_SC1);
            }
        wg_signal(a, C_PART + rb * 32);
        if (task == 0) TR(2100, rb * 4 + 2);
    }
}
DI void role_ln2(const Params& a, int q, LAS unsigned char* lds) {
    LAS float* tab = (LAS float*)lds;
    wg_wait<false>(a, ctl_word(a, C_MODB), 108u);
    load_modtab(a, tab, 3, 4, 2);
    for (int rb = 0; rb < NRB; ++rb) {
        wg_wait<false>(a, ctl_word(a, C_PART + rb * 32), 36u);
        task_ln2<2>(a, rb * 128 + q * 8, tab);
        wg_signal(a, C_H2 + rb * 32);
    }
}
DI void role_win(const Params& a, int nt, LAS unsigned char* lds) {
    using GS = GemmStream<128, 128, 3, true>;
    GS gs; gs.init(lds, D_, FF);
    LAS unsigned char* img = lds + GS::C::LDS;
    const rsrc_t rB = mk_rsrc(a.Win + nt * 128, D_ * FF * 2);
    const rsrc_t rO = mk_rsrc(a.ws + (nt < 18 ? WS_Z1 : WS_Z2), S_ * FH * 2);
    for (int rb = 0; rb < NRB; ++rb) {
        gs.primeB(rB);
        wg_wait<false>(a, ctl_word(a, C_H2 + rb * 32), 16u);
        const rsrc_t rA = mk_rsrc(a.ws + WS_H2 + (size_t)rb * 128 * D_ * 2, 128 * D_ * 2);
        gs.primeA(rA);
        f32x16 acc[2][2];
        gs.template tile<0, true, true>(rA, rB, rA, rB, false, 9, acc);
        epi_tile128(acc, img, true, rO, (rb * 128 * FH + (nt % 18) * 128) * 2, FH * 2, [](float x) { return gelu_tanh(rbf(x)); });
        wg_signal(a, C_Z + rb * 32);
    }
}
DI void role_ln3(const Params& a, int q) {
    for (int rb = 0; rb < NRB; ++rb) {
        wg_wait<false>(a, ctl_word(a, C_Z + rb * 32), 36u);
        task_ln3<2>(a, rb * 128 + q * 16);
    }
}
DI void worker_role(const Params& a, int j, LAS unsigned char* lds) {
    if (j < 64) {
        LAS float* tab = (LAS float*)lds;
        wg_wait<false>(a, ctl_word(a, C_MODA), 36u);
        load_modtab(a, tab, 0, 1, -1);
        task_ln<false>(a, j * 32, tab, WS_H1);
        wg_signal(a, C_H1);
    }
    constexpr int NQKV = 128;
    wg_wait<false>(a, ctl_word(a, C_H1), 64u);
    phase_qkv(a, j, NQKV, lds);
    TM(1);
#define RMASK 31
    if (j < 32) { if (RMASK & 1) role_att(a, j, lds); }
    else if (j < 68) { if (RMASK & 2) role_wo(a, j - 32, lds); }
    else if (j < 84) { if (RMASK & 4) role_ln2(a, j - 68, lds); }
    else if (j < 120) { if (RMASK & 8) role_win(a, j - 84, lds); }
    else if (j < 128) { if (RMASK & 16) role_ln3(a, j - 120); }
    TM(2);
}

constexpr unsigned SPIN_CAP = 1u << 14;
#define P_INC 60
#define P_DEC 2
DI unsigned pace(unsigned lead, unsigned base, bool early) { return early ? (lead + P_INC < base + 1200u ? lead + P_INC : lead) : (lead > P_DEC && lead + 300u > base ? lead - P_DEC : lead); }
DI bool hint_wait(rsrc_t rX, int w, int lane, unsigned want) {
    const int off = lane < 8 ? X_HINT + (8 * w + lane) * 256 : OOB;
    for (unsigned spins = 0; spins < (1u << 16); ++spins) {
        CB();
        const unsigned v = __builtin_amdgcn_raw_buffer_load_b32(rX, off, 0, AUX_SC1);
        if (!__any(lane < 8 && v < want)) return true;
    }
    return false;
}
DI void delay_from(unsigned long long t0, unsigned d) { while ((unsigned)(cyc() - t0) + 40u < d) {} }
#define CS(i)
#define CS_DECL
#define CS_OUT(base)
#define STR2_(x) #x
#define STR_(x) STR2_(x)
#define LDGAP ""
DI int mem_slot(int s) { return s % 3; }
DI int o_slot(int s) { return s & 1; }
constexpr int HL_Q = 0;
constexpr int HL_K = HL_Q + 2048;
constexpr int HL_KX = HL_K + 2048;
constexpr int HL_WV = HL_KX + 2 * 8192;
constexpr int HL_STAGE = HL_WV + 4 * 9 * 2048;
constexpr int HL_END = HL_STAGE + 4 * 16384;

DI int wsw(int k) { return ((k & 1) | (((k >> 2) & 1) << 1) | (((k >> 3) & 1) << 2)) << 1; }
constexpr int w_slot(int n) {
    const int k = n < 18 ? n % 9 : (n - 18) % 5;
    return k < 4 ? HL_STAGE + k * 16384 : (k == 4 ? HL_KX : HL_WV + (k - 5) * 16384);
}
constexpr int w_issue(int n) { return n <= 22 ? n - 8 : n - 4; }
constexpr int w_younger(int n) { int y = 0; for (int m = n + 1; m < 27; ++m) if (m < 8 || w_issue(m) <= n) ++y; return y; }
DI void stage_w(LAS unsigned char* dst, rsrc_t r, const int (&vo)[4], int soff, int wave) {
#pragma unroll
    for (int p = 0; p < 4; ++p) dma16<false>((unsigned)(size_t)dst + (wave + 4 * p) * 1024, vo[p], r, soff);
}
DI bf16x16 read_wfrag(LAS unsigned char* st, int col, int lane) {
    const int gb = lane >> 4, q = (lane & 15) >> 2, p = lane & 3;
    s16x4 r[4];
#pragma unroll
    for (int m = 0; m < 4; ++m) {
        const int d0 = 32 * (m >> 1) + 16 * (gb >> 1) + 4 * (gb & 1) + 2 * (m & 1);
        const int k = d0 + (q & 1) + 8 * (q >> 1);
        const int cb = (col + 4 * p) * 2;
        const int ch = (cb >> 4) ^ wsw(k);
        r[m] = __builtin_amdgcn_ds_read_tr16_b64_v4i16((LAS s16x4*)(st + k * 256 + ch * 16 + (cb & 15)));
    }
    typedef short s16x16 __attribute__((ext_vector_type(16)));
    s16x16 v = {r[0][0], r[0][1], r[0][2], r[0][3], r[1][0], r[1][1], r[1][2], r[1][3], r[2][0], r[2][1], r[2][2], r[2][3], r[3][0], r[3][1], r[3][2], r[3][3]};
    return __builtin_bit_cast(bf16x16, v);
}

DI void head_role(const Params& a, int h, LAS unsigned char* lds) {
    const int lane = lane_id(), w = wave_id(), c = lane & 15, g = lane >> 4;
    unsigned char* ws = a.ws;
    gu32* ctl = (gu32*)(ws + WS_CTL);
    const rsrc_t rKVM = __builtin_amdgcn_make_buffer_rsrc((void*)(ws + WS_KVM), 0, NSEG * H_ * 4096, RSRC3);
    bf16x16 WA[4][8];
    bf16x16 WB[4];
    bf16x16 WV[9];
    LAS unsigned char* wv1 = lds + HL_WV + w * 9 * 2048 + lane * 16;
    {
        const rsrc_t src[3] = {mk_rsrc(a.Wq + h * 128, D_ * HE * 2), mk_rsrc(a.Wk + h * 128, D_ * HE * 2), mk_rsrc(a.Wv + h * 128, D_ * HE * 2)};
        int vo[4];
#pragma unroll
        for (int p = 0; p < 4; ++p) { const int k = (w + 4 * p) * 4 + (lane >> 4); vo[p] = (k * HE + ((lane & 15) ^ wsw(k)) * 8) * 2; }
#define W_STAGE(n) stage_w(lds + w_slot(n), src[(n) / 9], vo, ((n) % 9) * 64 * HE * 2, w)
#define W_READ(n) { wait_vm<4 * w_younger(n)>(); __builtin_amdgcn_s_barrier(); \
            const bf16x16 f0 = read_wfrag(lds + w_slot(n), 32 * w, lane), f1 = read_wfrag(lds + w_slot(n), 32 * w + 16, lane); \
            constexpr int m_ = (n) / 9, f_ = (n) % 9; \
            if constexpr (m_ < 2 && f_ < 8) { WA[2 * m_][f_] = f0; WA[2 * m_ + 1][f_] = f1; asm volatile("" : "+a"(WA[2 * m_][f_]), "+a"(WA[2 * m_ + 1][f_])); } \
            else if constexpr (m_ < 2) { WB[2 * m_] = f0; WB[2 * m_ + 1] = f1; } \
            else { WV[f_] = f0; const v8u t = __builtin_bit_cast(v8u, f1); \
                   *(LAS v4u*)(wv1 + f_ * 2048) = v4u{t[0], t[1], t[2], t[3]}; *(LAS v4u*)(wv1 + f_ * 2048 + 1024) = v4u{t[4], t[5], t[6], t[7]}; } \
            asm volatile("s_waitcnt lgkmcnt(0)" ::: "memory"); __builtin_amdgcn_s_barrier(); }
        W_STAGE(0); W_STAGE(1); W_STAGE(2); W_STAGE(3); W_STAGE(4); W_STAGE(5); W_STAGE(6); W_STAGE(7);
        W_STAGE(8); W_READ(0); W_STAGE(9); W_READ(1); W_STAGE(10); W_READ(2); W_STAGE(11); W_READ(3); W_STAGE(12); W_READ(4); W_STAGE(13); W_READ(5);
        W_STAGE(14); W_READ(6); W_STAGE(15); W_READ(7); W_STAGE(16); W_READ(8); W_STAGE(17); W_READ(9); W_STAGE(18); W_READ(10); W_STAGE(19); W_READ(11);
        W_STAGE(20); W_READ(12); W_STAGE(21); W_READ(13); W_STAGE(22); W_READ(14); W_READ(15); W_READ(16); W_READ(17); W_READ(18);
        W_STAGE(23); W_READ(19); W_STAGE(24); W_READ(20); W_STAGE(25); W_READ(21); W_STAGE(26); W_READ(22); W_READ(23); W_READ(24); W_READ(25); W_READ(26);
    }
    const int idx = (lane & 1) ? 0xEEEE : 0x4444;
    const rsrc_t rX = mk_rsrc(a.xr + get_par(a) * 8192, X_BYTES);
    const rsrc_t rK = mk_rsrc(ws + WS_K + (size_t)h * S_ * E_ * 2, S_ * E_ * 2), rV = mk_rsrc(ws + WS_VT + (size_t)h * E_ * S_ * 2, E_ * S_ * 2);
    const int q4 = c >> 2, p4 = c & 3;
    LAS unsigned char* qkw = lds + HL_Q + (32 * w + c) * 16 + 4 * g;
    LAS unsigned char* qr = lds + HL_Q + (8 * g + q4) * 16 + (p4 & 1) * 8;
    LAS unsigned char* kr = lds + HL_K + (8 * g + q4) * 16 + (p4 >> 1) * 8;
    const int kxr = HL_KX + c * 256;
    int kxsrc[2];
#pragma unroll
    for (int p = 0; p < 2; ++p) { const int key = 8 * w + 4 * p + g; kxsrc[p] = key * E_ + ((c ^ (key & 15)) * 8); }
    int ooff;
    {
        const int el = 32 * w + ((g & 1) << 4) + ((g >> 1) << 3);
        const int win = el >> 6, gg = (el >> 4) & 3, hf = (el >> 3) & 1;
        const int r = 4 * (c & 3) + 2 * win + hf;
        const int p = h * 2 + ((c >> 2) & 1);
        ooff = c < 8 ? X_O + (p >> 3) * 16384 + (p & 7) * 1024 + (16 * gg + r) * 16 : 0x7F000000;
    }
    const float ninf = (g & 1) ? -INFINITY : 0.f;
    const int hoff = (w < 2 && lane == 0) ? X_HINT + w * 8192 + h * 256 : OOB;
    unsigned nfail = 0, lead = (unsigned)a.lead_h;
    bool fail = false;
    TM(1);
    CS_DECL;
    for (int s = 0; s < NSEG; ++s) {
        if ((s & 3) == 0) (void)wave_poll_ge(a, ctl_word(a, C_KV + ((s >> 2) * H_ + h) * 16), 2u);
        v4u vx[2], kxr_[2];
        LAS unsigned char* kxb = lds + HL_KX + (s & 1) * 8192;
#pragma unroll
        for (int p = 0; p < 2; ++p) kxr_[p] = __builtin_amdgcn_raw_buffer_load_b128(rK, (s * 32 * E_ + kxsrc[p]) * 2, 0, AUX_SC1);
#pragma unroll
        for (int t = 0; t < 2; ++t) vx[t] = __builtin_amdgcn_raw_buffer_load_b128(rV, ((32 * w + 16 * t + c) * S_ + s * 32 + 8 * g) * 2, 0, AUX_SC1);
#define KX_TO_LDS() { *(LAS v4u*)(kxb + (2 * w) * 1024 + lane * 16) = kxr_[0]; *(LAS v4u*)(kxb + (2 * w + 1) * 1024 + lane * 16) = kxr_[1]; }
        bf16x16 v1f[4];
#define LDV1(slot, f) { const v4u lo_ = *(LAS v4u*)(wv1 + (f) * 2048), hi_ = *(LAS v4u*)(wv1 + (f) * 2048 + 1024); \
                        v1f[slot] = __builtin_bit_cast(bf16x16, (v8u{lo_[0], lo_[1], lo_[2], lo_[3], hi_[0], hi_[1], hi_[2], hi_[3]})); }
#define SB() __builtin_amdgcn_sched_barrier(0)
#define QK4(f) { SMF_A(acc[0], af[f], WA[0][f], idx); SMF_A(acc[1], af[f], WA[1][f], idx); SMF_A(acc[2], af[f], WA[2][f], idx); SMF_A(acc[3], af[f], WA[3][f], idx); }
        LDV1(0, 0); LDV1(1, 1); LDV1(2, 2); LDV1(3, 3);
        v4u af[9];
        f32x4 acc[4], av[2];
        unsigned pq[2], pk[2];
        {
            unsigned spins = 0;
            CS(0);
            if (s > 0) { if (!fail && !hint_wait(rX, w, lane, (unsigned)s)) fail = true; __builtin_amdgcn_s_barrier(); }
            CS(1);
            KX_TO_LDS();
            if (s > 0) delay_from(cyc(), lead);
            CS(2);
            const int base = X_MEM + mem_slot(s) * X_MEM_SLOT + (w % NREP) * X_MEM_REP + lane * 16;
#define QK4S(a_, w0, w1, w2, w3) "v_smfmac_f32_16x16x64_bf16 %0, " a_ ", " w0 ", %4\n\tv_smfmac_f32_16x16x64_bf16 %1, " a_ ", " w1 ", %4\n\t" \
                                 "v_smfmac_f32_16x16x64_bf16 %2, " a_ ", " w2 ", %4\n\tv_smfmac_f32_16x16x64_bf16 %3, " a_ ", " w3 ", %4\n\t"
            float qa0, qa1;
            while (true) {
                if (s == 0) {
#pragma unroll
                    for (int f = 0; f < 9; ++f) af[f] = *(const v4u*)(a.mem0 + (c >> 1) * D_ + 64 * f + 16 * g + 8 * (c & 1));
                    asm volatile("s_waitcnt vmcnt(0)" : "+v"(af[0]), "+v"(af[1]), "+v"(af[2]), "+v"(af[3]), "+v"(af[4]), "+v"(af[5]), "+v"(af[6]), "+v"(af[7]), "+v"(af[8]) :: "memory");
#pragma unroll
                    for (int T = 0; T < 4; ++T) acc[T] = zero4();
                    av[0] = zero4(); av[1] = zero4();
                } else {
                    while (true) {
                        asm volatile(
                            "buffer_load_dwordx4 %0, %9, %10, 0 offen sc1\n\tbuffer_load_dwordx4 %1, %9, %10, 0 offen offset:1024 sc1\n\t"
                            "buffer_load_dwordx4 %2, %9, %10, 0 offen offset:2048 sc1\n\tbuffer_load_dwordx4 %3, %9, %10, 0 offen offset:3072 sc1\n\t"
                            "buffer_load_dwordx4 %4, %9, %10, %11 offen sc1\n\tbuffer_load_dwordx4 %5, %9, %10, %11 offen offset:1024 sc1\n\t"
                            "buffer_load_dwordx4 %6, %9, %10, %11 offen offset:2048 sc1\n\tbuffer_load_dwordx4 %7, %9, %10, %11 offen offset:3072 sc1\n\t"
                            "buffer_load_dwordx4 %8, %9, %10, %12 offen sc1"
                            : "=&v"(af[0]), "=&v"(af[1]), "=&v"(af[2]), "=&v"(af[3]), "=&v"(af[4]), "=&v"(af[5]), "=&v"(af[6]), "=&v"(af[7]), "=&v"(af[8])
                            : "v"(base), "s"(rX), "s"(4096), "s"(16384) : "memory");
#pragma unroll
                        for (int T = 0; T < 4; ++T) acc[T] = zero4();
                        av[0] = zero4(); av[1] = zero4();
                        asm volatile("s_waitcnt vmcnt(8)" : "+v"(af[0]), "+v"(acc[0]), "+v"(acc[1]), "+v"(acc[2]), "+v"(acc[3]), "+v"(av[0]), "+v"(av[1]) :: "memory");
                        if (!__any(vmax(0u, af[0]) == ~0u)) break;
                        ++nfail;
                        if (++spins > SPIN_CAP) { fail = true; break; }
                    }
                }
                CS(3);
                asm volatile(QK4S("%5", "%10", "%11", "%12", "%13") "s_waitcnt vmcnt(7)\n\t" QK4S("%6", "%14", "%15", "%16", "%17") "s_waitcnt vmcnt(6)\n\t" QK4S("%7", "%18", "%19", "%20", "%21")
                             "s_waitcnt vmcnt(5)\n\t" QK4S("%8", "%22", "%23", "%24", "%25") "s_waitcnt vmcnt(4)\n\t" QK4S("%9", "%26", "%27", "%28", "%29")
                             : "+v"(acc[0]), "+v"(acc[1]), "+v"(acc[2]), "+v"(acc[3])
                             : "v"(idx), "v"(af[0]), "v"(af[1]), "v"(af[2]), "v"(af[3]), "v"(af[4]),
                               "a"(WA[0][0]), "a"(WA[1][0]), "a"(WA[2][0]), "a"(WA[3][0]), "a"(WA[0][1]), "a"(WA[1][1]), "a"(WA[2][1]), "a"(WA[3][1]),
                               "a"(WA[0][2]), "a"(WA[1][2]), "a"(WA[2][2]), "a"(WA[3][2]), "a"(WA[0][3]), "a"(WA[1][3]), "a"(WA[2][3]), "a"(WA[3][3]),
                               "a"(WA[0][4]), "a"(WA[1][4]), "a"(WA[2][4]), "a"(WA[3][4]) : "memory");
                asm volatile("s_waitcnt vmcnt(3)\n\t" QK4S("%5", "%9", "%10", "%11", "%12") "s_waitcnt vmcnt(2)\n\t" QK4S("%6", "%13", "%14", "%15", "%16") "s_waitcnt vmcnt(1)\n\t" QK4S("%7", "%17", "%18", "%19", "%20")
                             "s_waitcnt vmcnt(0)\n\t" QK4S("%8", "%21", "%22", "%23", "%24")
                             : "+v"(acc[0]), "+v"(acc[1]), "+v"(acc[2]), "+v"(acc[3])
                             : "v"(idx), "v"(af[5]), "v"(af[6]), "v"(af[7]), "v"(af[8]),
                               "a"(WA[0][5]), "a"(WA[1][5]), "a"(WA[2][5]), "a"(WA[3][5]), "a"(WA[0][6]), "a"(WA[1][6]), "a"(WA[2][6]), "a"(WA[3][6]),
                               "a"(WA[0][7]), "a"(WA[1][7]), "a"(WA[2][7]), "a"(WA[3][7]), "v"(WB[0]), "v"(WB[1]), "v"(WB[2]), "v"(WB[3]) : "memory");
                asm volatile("v_smfmac_f32_16x16x64_bf16 %0, %4, %5, %7\n\tv_smfmac_f32_16x16x64_bf16 %1, %4, %6, %7\n\t"
                             "v_add_f32_e32 %2, %8, %9\n\tv_add_f32_e32 %3, %10, %11"
                             : "+v"(av[0]), "+v"(av[1]), "=&v"(qa0), "=&v"(qa1)
                             : "v"(af[0]), "v"(WV[0]), "v"(v1f[0]), "v"(idx), "v"(acc[0][0]), "v"(acc[0][1]), "v"(acc[0][2]), "v"(acc[0][3]));
                if (s == 0 || fail || !__any(__builtin_isunordered(qa0, qa1))) break;
                ++nfail;
                if (++spins > SPIN_CAP) { fail = true; break; }
            }
            if (s > 0) lead = pace(lead, (unsigned)a.lead_h, spins > 0);
            CS(4);
            float x0, x1, x2, x3, x4, x5;
            asm volatile("v_add_f32_e32 %4, %12, %13\n\tv_add_f32_e32 %5, %14, %15\n\tv_cvt_pk_bf16_f32 %0, %10, %11\n\t"
                         "v_add_f32_e32 %6, %16, %17\n\tv_add_f32_e32 %7, %18, %19\n\tv_cvt_pk_bf16_f32 %1, %4, %5\n\t"
                         "v_add_f32_e32 %8, %20, %21\n\tv_add_f32_e32 %9, %22, %23\n\tv_cvt_pk_bf16_f32 %2, %6, %7\n\tv_cvt_pk_bf16_f32 %3, %8, %9"
                         : "=&v"(pq[0]), "=&v"(pq[1]), "=&v"(pk[0]), "=&v"(pk[1]), "=&v"(x0), "=&v"(x1), "=&v"(x2), "=&v"(x3), "=&v"(x4), "=&v"(x5)
                         : "v"(qa0), "v"(qa1), "v"(acc[1][0]), "v"(acc[1][1]), "v"(acc[1][2]), "v"(acc[1][3]), "v"(acc[2][0]), "v"(acc[2][1]), "v"(acc[2][2]), "v"(acc[2][3]),
                           "v"(acc[3][0]), "v"(acc[3][1]), "v"(acc[3][2]), "v"(acc[3][3]));
        }
        *(LAS unsigned*)(qkw) = pq[0]; *(LAS unsigned*)(qkw + 256) = pq[1];
        *(LAS unsigned*)(qkw + 2048) = pk[0]; *(LAS unsigned*)(qkw + 2048 + 256) = pk[1];
        SB();
        asm volatile("v_smfmac_f32_16x16x64_bf16 %0, %3, %6, %2\n\tv_smfmac_f32_16x16x64_bf16 %1, %3, %9, %2\n\tv_smfmac_f32_16x16x64_bf16 %0, %4, %7, %2\n\t"
                     "v_smfmac_f32_16x16x64_bf16 %1, %4, %10, %2\n\tv_smfmac_f32_16x16x64_bf16 %0, %5, %8, %2\n\tv_smfmac_f32_16x16x64_bf16 %1, %5, %11, %2"
                     : "+v"(av[0]), "+v"(av[1]) : "v"(idx), "v"(af[1]), "v"(af[2]), "v"(af[3]), "v"(WV[1]), "v"(WV[2]), "v"(WV[3]), "v"(v1f[1]), "v"(v1f[2]), "v"(v1f[3]));
        SB();
        LDV1(0, 4); LDV1(1, 5); LDV1(2, 6);
        SB();
        asm volatile("s_waitcnt lgkmcnt(6)\n\ts_barrier" ::: "memory");
        __builtin_amdgcn_raw_buffer_store_b32((unsigned)(s + 1), rX, hoff, 0, AUX_SC1);
        CS(5);
        bf16x8 qf[4], kmf[4], kx[2][4];
#define LDS_SC(ks) { qf[ks] = tr2(qr + (ks) * 512, qr + (ks) * 512 + 64); \
                     kx[0][ks] = *(LAS bf16x8*)(lds + (s & 1) * 8192 + kxr + (((4 * (ks) + g) ^ c) << 4)); \
                     kx[1][ks] = *(LAS bf16x8*)(lds + (s & 1) * 8192 + kxr + 4096 + (((4 * (ks) + g) ^ c) << 4)); \
                     kmf[ks] = tr2(kr + (ks) * 512, kr + (ks) * 512 + 64); }
        LDS_SC(0); LDS_SC(1); LDV1(3, 7);
        SB();
        asm volatile("v_smfmac_f32_16x16x64_bf16 %0, %3, %6, %2\n\tv_smfmac_f32_16x16x64_bf16 %1, %3, %9, %2\n\tv_smfmac_f32_16x16x64_bf16 %0, %4, %7, %2\n\t"
                     "v_smfmac_f32_16x16x64_bf16 %1, %4, %10, %2\n\tv_smfmac_f32_16x16x64_bf16 %0, %5, %8, %2\n\tv_smfmac_f32_16x16x64_bf16 %1, %5, %11, %2"
                     : "+v"(av[0]), "+v"(av[1]) : "v"(idx), "v"(af[4]), "v"(af[5]), "v"(af[6]), "v"(WV[4]), "v"(WV[5]), "v"(WV[6]), "v"(v1f[0]), "v"(v1f[1]), "v"(v1f[2]));
        f32x4 sp[3];
        { f32x2 c0, c1; const f32x2 ni = {ninf, ninf}; asm volatile("v_mov_b64 %0, %2\n\tv_mov_b64 %1, %2" : "=&v"(c0), "=&v"(c1) : "v"(ni)); sp[0] = f32x4{c0[0], c0[1], c1[0], c1[1]}; }
        SB();
        LDV1(0, 8); LDS_SC(2);
        SB();
        asm volatile("v_mfma_f32_16x16x32_bf16 %1, %3, %6, 0\n\tv_mfma_f32_16x16x32_bf16 %2, %4, %6, 0\n\tv_mfma_f32_16x16x32_bf16 %0, %5, %6, %0"
                     : "+v"(sp[0]), "=&v"(sp[1]), "=&v"(sp[2]) : "v"(kx[0][0]), "v"(kx[1][0]), "v"(kmf[0]), "v"(qf[0]));
        SB();
        LDS_SC(3);
        SB();
        asm volatile("v_mfma_f32_16x16x32_bf16 %1, %6, %9, %1\n\tv_mfma_f32_16x16x32_bf16 %2, %7, %9, %2\n\tv_mfma_f32_16x16x32_bf16 %0, %8, %9, %0\n\t"
                     "v_smfmac_f32_16x16x64_bf16 %3, %10, %12, %5\n\tv_smfmac_f32_16x16x64_bf16 %4, %10, %14, %5\n\tv_smfmac_f32_16x16x64_bf16 %3, %11, %13, %5"
                     : "+v"(sp[0]), "+v"(sp[1]), "+v"(sp[2]), "+v"(av[0]), "+v"(av[1])
                     : "v"(idx), "v"(kx[0][1]), "v"(kx[1][1]), "v"(kmf[1]), "v"(qf[1]), "v"(af[7]), "v"(af[8]), "v"(WV[7]), "v"(WV[8]), "v"(v1f[3]));
        asm volatile("v_mfma_f32_16x16x32_bf16 %1, %3, %6, %1\n\tv_mfma_f32_16x16x32_bf16 %2, %4, %6, %2\n\tv_mfma_f32_16x16x32_bf16 %0, %5, %6, %0"
                     : "+v"(sp[0]), "+v"(sp[1]), "+v"(sp[2]) : "v"(kx[0][2]), "v"(kx[1][2]), "v"(kmf[2]), "v"(qf[2]));
        asm volatile("v_mfma_f32_16x16x32_bf16 %1, %5, %8, %1\n\tv_mfma_f32_16x16x32_bf16 %2, %6, %8, %2\n\tv_mfma_f32_16x16x32_bf16 %0, %7, %8, %0\n\t"
                     "v_smfmac_f32_16x16x64_bf16 %3, %9, %10, %4\n\ts_nop 3"
                     : "+v"(sp[0]), "+v"(sp[1]), "+v"(sp[2]), "+v"(av[1]) : "v"(idx), "v"(kx[0][3]), "v"(kx[1][3]), "v"(kmf[3]), "v"(qf[3]), "v"(af[8]), "v"(v1f[0]));
        CS(6);
        unsigned pv[2];
        {
            float m, t, t2;
            asm volatile(
                "v_max3_f32 %0, %3, %4, %5\n\tv_max3_f32 %1, %6, %7, %8\n\tv_max3_f32 %2, %9, %10, %11\n\t"
                "v_max3_f32 %0, %0, %12, %13\n\tv_max3_f32 %1, %1, %2, %14\n\tv_max_f32_e32 %0, %0, %1\n\tv_mov_b32_e32 %1, %0\n\ts_nop 1\n\t"
                "v_permlane16_swap_b32_e32 %0, %1\n\tv_max_f32_e32 %0, %0, %1\n\tv_mov_b32_e32 %1, %0\n\ts_nop 1\n\t"
                "v_permlane32_swap_b32_e32 %0, %1\n\tv_max_f32_e32 %0, %0, %1\n\tv_mul_f32_e32 %0, %15, %0"
                : "=&v"(m), "=&v"(t), "=&v"(t2)
                : "v"(sp[1][0]), "v"(sp[1][1]), "v"(sp[1][2]), "v"(sp[1][3]), "v"(sp[2][0]), "v"(sp[2][1]), "v"(sp[2][2]), "v"(sp[2][3]), "v"(sp[0][0]),
                  "v"(sp[0][1]), "v"(sp[0][2]), "v"(sp[0][3]), "s"(SCL));
            const f32x2 ms = {m, m};
            const f32x2 scl2 = {SCL, SCL};
            f32x2 e[6];
            PKFMA(e[0], (f32x2{sp[0][0], sp[0][1]}), scl2, ms); PKFMA(e[1], (f32x2{sp[0][2], sp[0][3]}), scl2, ms);
            PKFMA(e[2], (f32x2{sp[1][0], sp[1][1]}), scl2, ms); PKFMA(e[3], (f32x2{sp[1][2], sp[1][3]}), scl2, ms);
            PKFMA(e[4], (f32x2{sp[2][0], sp[2][1]}), scl2, ms); PKFMA(e[5], (f32x2{sp[2][2], sp[2][3]}), scl2, ms);
            float x[12];
#pragma unroll
            for (int r = 0; r < 6; ++r) { x[2 * r] = e[r][0]; x[2 * r + 1] = e[r][1]; }
            asm volatile("v_exp_f32_e32 %0, %0\n\tv_exp_f32_e32 %1, %1\n\tv_exp_f32_e32 %2, %2\n\tv_exp_f32_e32 %3, %3\n\tv_exp_f32_e32 %4, %4\n\tv_exp_f32_e32 %5, %5\n\t"
                         "v_exp_f32_e32 %6, %6\n\tv_exp_f32_e32 %7, %7\n\tv_exp_f32_e32 %8, %8\n\tv_exp_f32_e32 %9, %9\n\tv_exp_f32_e32 %10, %10\n\tv_exp_f32_e32 %11, %11"
                         : "+v"(x[4]), "+v"(x[5]), "+v"(x[6]), "+v"(x[7]), "+v"(x[8]), "+v"(x[9]), "+v"(x[10]), "+v"(x[11]), "+v"(x[0]), "+v"(x[1]), "+v"(x[2]), "+v"(x[3]));
            v4u pp; v2u pm;
            VCVT(pp[0], x[4], x[5]); VCVT(pp[1], x[6], x[7]); VCVT(pp[2], x[8], x[9]); VCVT(pp[3], x[10], x[11]);
            VCVT(pm[0], x[0], x[1]); VCVT(pm[1], x[2], x[3]);
            v4u ones; { unsigned o_; asm volatile("v_mov_b32_e32 %0, 0x3f803f80" : "=v"(o_)); ones = v4u{o_, o_, o_, o_}; }
            asm volatile("s_nop 1" : "+v"(pp), "+v"(pm), "+v"(ones));
            f32x4 o0, o1, zz;
            MFMA32_0(o0, vx[0], pp); MFMA32_0(o1, vx[1], pp); MFMA32_0(zz, ones, pp);
            asm volatile("" : "+v"(av[0]), "+v"(av[1]));
            float a0, a1, a2, a3;
            VADD(a0, av[0][0], av[0][1]); VADD(a1, av[0][2], av[0][3]); VADD(a2, av[1][0], av[1][1]); VADD(a3, av[1][2], av[1][3]);
            VCVT(pv[0], a0, a1); VCVT(pv[1], a2, a3);
            unsigned u0, u1, w0, w1;
            VMOV(u0, pv[0]); VMOV(u1, pv[0]); VMOV(w0, pv[1]); VMOV(w1, pv[1]);
            NOP(0);
            SWAP16(u0, u1); SWAP16(w0, w1);
            v2u vm0 = {u0, u1}, vm1 = {w0, w1};
            asm volatile("s_nop 1" : "+v"(vm0), "+v"(vm1), "+v"(o0), "+v"(o1), "+v"(zz));
            MFMA16_ACC(zz, (v2u{ones[0], ones[1]}), pm); MFMA16_ACC(o0, vm0, pm); MFMA16_ACC(o1, vm1, pm);
            asm volatile("s_nop 5" : "+v"(zz));
            float rz; asm volatile("v_rcp_f32_e32 %0, %1" : "=v"(rz) : "v"(zz[0]));
            asm volatile("s_nop 1" : "+v"(o0), "+v"(o1), "+v"(rz));
            const f32x2 rz2 = {rz, rz};
            f32x2 n0, n1, n2, n3;
            PKMUL(n0, (f32x2{o0[0], o0[1]}), rz2); PKMUL(n1, (f32x2{o0[2], o0[3]}), rz2); PKMUL(n2, (f32x2{o1[0], o1[1]}), rz2); PKMUL(n3, (f32x2{o1[2], o1[3]}), rz2);
            unsigned d0, d1, e0_, e1;
            VCVT(d0, n0[0], n0[1]); VCVT(d1, n1[0], n1[1]); VCVT(e0_, n2[0], n2[1]); VCVT(e1, n3[0], n3[1]);
            NOP(0);
            SWAP16(d0, e0_); SWAP16(d1, e1);
            NOP(0);
            v4u val = {nosent(d0), nosent(d1), nosent(e0_), nosent(e1)};
            CS(7);
            __builtin_amdgcn_raw_buffer_store_b128(val, rX, s != NSEG - 1 ? ooff + o_slot(s) * X_O_SLOT : OOB, 0, AUX_SC1);
        }
        __builtin_amdgcn_raw_buffer_store_b128(poison4(), rX, s > 0 ? ooff + o_slot(s - 1) * X_O_SLOT : OOB, 0, AUX_SC1);
        {
            auto a0 = __builtin_amdgcn_permlane32_swap(pk[0], pk[1], false, false);
            auto a1 = __builtin_amdgcn_permlane32_swap(pv[0], pv[1], false, false);
            auto b0 = __builtin_amdgcn_permlane16_swap(a0[0], a1[0], false, false);
            auto b1 = __builtin_amdgcn_permlane16_swap(a0[1], a1[1], false, false);
            v4u val = {b0[0], b0[1], b1[0], b1[1]};
            const int off = (s * H_ + h) * 4096 + (g & 1) * 2048 + (32 * w + 16 * (g >> 1) + c) * 16;
            __builtin_amdgcn_raw_buffer_store_b128(val, rKVM, off, 0, AUX_SC1);
        }
    }
    if (h == 0) CS_OUT(8192);
    asm volatile("s_waitcnt vmcnt(0)\n\ts_barrier" ::: "memory");
    __builtin_amdgcn_raw_buffer_store_b32((unsigned)(NSEG + 1), rX, hoff, 0, AUX_SC1);
    TM(2);
    if (fail && threadIdx.x == 0) __hip_atomic_store(ctl, 1u, RLX_AGENT);
}

constexpr int YL_RED = 0;
constexpr int YL_STAGE = 4096;
DI void y_role(const Params& a, int tp, int rho, LAS unsigned char* lds) {
    const int lane = lane_id(), w = wave_id(), c = lane & 15, g = lane >> 4;
    unsigned char* ws = a.ws;
    gu32* ctl = (gu32*)(ws + WS_CTL);
    bf16x16 W[16][2];
    {
        LAS unsigned char* st = lds + YL_STAGE + w * 32768;
        const rsrc_t rW = mk_rsrc(a.Wo + (size_t)(1024 * w) * D_ + 32 * tp, 1024 * D_ * 2);
        int vo[4];
#pragma unroll
        for (int p = 0; p < 4; ++p) vo[p] = ((16 * p + (lane >> 2)) * D_ + (lane & 3) * 8) * 2;
        auto stage = [&](int win, int slot) {
#pragma unroll
            for (int p = 0; p < 4; ++p) dma16<false>((unsigned)(size_t)st + slot * 4096 + p * 1024, vo[p], rW, win * 64 * D_ * 2);
        };
#pragma unroll
        for (int i = 0; i < 7; ++i) stage(i, i);
        const int gb = lane >> 4, q = (lane & 15) >> 2, p = lane & 3;
#define Y_READ(n) { if ((n) + 7 < 16) stage((n) + 7, ((n) + 7) & 7); wait_vm<4 * ((n) + 7 < 16 ? 7 : 15 - (n))>(); \
            _Pragma("unroll") for (int T = 0; T < 2; ++T) { s16x4 r[4]; \
                _Pragma("unroll") for (int m = 0; m < 4; ++m) { const int d0 = 32 * (m >> 1) + 16 * (gb >> 1) + 4 * (gb & 1) + 2 * (m & 1); const int k = d0 + (q & 1) + 8 * (q >> 1); \
                    r[m] = __builtin_amdgcn_ds_read_tr16_b64_v4i16((LAS s16x4*)(st + ((n) & 7) * 4096 + k * 64 + (16 * T + 4 * p) * 2)); } \
                typedef short s16x16 __attribute__((ext_vector_type(16))); \
                s16x16 v = {r[0][0], r[0][1], r[0][2], r[0][3], r[1][0], r[1][1], r[1][2], r[1][3], r[2][0], r[2][1], r[2][2], r[2][3], r[3][0], r[3][1], r[3][2], r[3][3]}; \
                W[n][T] = __builtin_bit_cast(bf16x16, v); asm volatile("" : "+a"(W[n][T])); } \
            asm volatile("s_waitcnt lgkmcnt(0)" ::: "memory"); }
        Y_READ(0); Y_READ(1); Y_READ(2); Y_READ(3); Y_READ(4); Y_READ(5); Y_READ(6); Y_READ(7);
        Y_READ(8); Y_READ(9); Y_READ(10); Y_READ(11); Y_READ(12); Y_READ(13); Y_READ(14); Y_READ(15);
    }
    const int idx = (lane & 1) ? 0xEEEE : 0x4444;
    const rsrc_t rX = mk_rsrc(a.xr + get_par(a) * 8192, X_BYTES);
    unsigned nfail = 0, lead = (unsigned)a.lead_y;
    bool fail = false;
    CS_DECL;
    TM(1);
    for (int s = 0; s < NSEG - 1; ++s) {
        const int base = X_O + o_slot(s) * X_O_SLOT + 2 * w * 16384 + rho * 1024 + lane * 16;
        unsigned spins = 0;
        CS(0);
        if (!fail && !hint_wait(rX, w, lane, (unsigned)(s + 1))) fail = true;
        CS(1); delay_from(cyc(), lead); CS(2);
        f32x4 D1[2], D2[2];
        float y0, y1;
        while (true) {
            v4u v[8];
            while (true) {
                asm volatile(
                    "buffer_load_dwordx4 %0, %8, %9, 0 offen sc1\n\tbuffer_load_dwordx4 %1, %8, %9, 0 offen offset:2048 sc1\n\t"
                    "buffer_load_dwordx4 %2, %8, %9, %10 offen sc1\n\tbuffer_load_dwordx4 %3, %8, %9, %10 offen offset:2048 sc1\n\t"
                    "buffer_load_dwordx4 %4, %8, %9, %11 offen sc1\n\tbuffer_load_dwordx4 %5, %8, %9, %11 offen offset:2048 sc1\n\t"
                    "buffer_load_dwordx4 %6, %8, %9, %12 offen sc1\n\tbuffer_load_dwordx4 %7, %8, %9, %12 offen offset:2048 sc1"
                    : "=&v"(v[0]), "=&v"(v[1]), "=&v"(v[2]), "=&v"(v[3]), "=&v"(v[4]), "=&v"(v[5]), "=&v"(v[6]), "=&v"(v[7])
                    : "v"(base), "s"(rX), "s"(4096), "s"(16384), "s"(16384 + 4096) : "memory");
                D1[0] = zero4(); D1[1] = zero4(); D2[0] = zero4(); D2[1] = zero4();
                asm volatile("s_waitcnt vmcnt(7)" : "+v"(v[0]), "+v"(D1[0]), "+v"(D1[1]), "+v"(D2[0]), "+v"(D2[1]) :: "memory");
                if (!__any(vmax(0u, v[0]) == ~0u)) break;
                ++nfail;
                if (++spins > SPIN_CAP) { fail = true; break; }
            }
            CS(3);
#define YP4(a_, w0, w1, w2, w3) "v_smfmac_f32_16x16x64_bf16 %0, " a_ ", " w0 ", %4\n\tv_smfmac_f32_16x16x64_bf16 %1, " a_ ", " w1 ", %4\n\t" \
                                "v_smfmac_f32_16x16x64_bf16 %2, " a_ ", " w2 ", %4\n\tv_smfmac_f32_16x16x64_bf16 %3, " a_ ", " w3 ", %4\n\t"
            asm volatile(YP4("%5", "%9", "%10", "%11", "%12") "s_waitcnt vmcnt(6)\n\t" YP4("%6", "%13", "%14", "%15", "%16") "s_waitcnt vmcnt(5)\n\t" YP4("%7", "%17", "%18", "%19", "%20")
                         "s_waitcnt vmcnt(4)\n\t" YP4("%8", "%21", "%22", "%23", "%24")
                         : "+v"(D1[0]), "+v"(D2[0]), "+v"(D1[1]), "+v"(D2[1])
                         : "v"(idx), "v"(v[0]), "v"(v[1]), "v"(v[2]), "v"(v[3]),
                           "a"(W[0][0]), "a"(W[1][0]), "a"(W[0][1]), "a"(W[1][1]), "a"(W[2][0]), "a"(W[3][0]), "a"(W[2][1]), "a"(W[3][1]),
                           "a"(W[4][0]), "a"(W[5][0]), "a"(W[4][1]), "a"(W[5][1]), "a"(W[6][0]), "a"(W[7][0]), "a"(W[6][1]), "a"(W[7][1]) : "memory");
            asm volatile("s_waitcnt vmcnt(3)\n\t" YP4("%5", "%9", "%10", "%11", "%12") "s_waitcnt vmcnt(2)\n\t" YP4("%6", "%13", "%14", "%15", "%16") "s_waitcnt vmcnt(1)\n\t" YP4("%7", "%17", "%18", "%19", "%20")
                         "s_waitcnt vmcnt(0)\n\t" YP4("%8", "%21", "%22", "%23", "%24") "s_nop 7"
                         : "+v"(D1[0]), "+v"(D2[0]), "+v"(D1[1]), "+v"(D2[1])
                         : "v"(idx), "v"(v[4]), "v"(v[5]), "v"(v[6]), "v"(v[7]),
                           "a"(W[8][0]), "a"(W[9][0]), "a"(W[8][1]), "a"(W[9][1]), "a"(W[10][0]), "a"(W[11][0]), "a"(W[10][1]), "a"(W[11][1]),
                           "a"(W[12][0]), "a"(W[13][0]), "a"(W[12][1]), "a"(W[13][1]), "a"(W[14][0]), "a"(W[15][0]), "a"(W[14][1]), "a"(W[15][1]) : "memory");
            y0 = fadd1(fadd1(D1[0][0], D1[0][1]), fadd1(D2[0][2], D2[0][3])); y1 = fadd1(fadd1(D1[1][0], D1[1][1]), fadd1(D2[1][2], D2[1][3]));
            if (fail || !__any(__builtin_isunordered(y0, y1))) break;
            ++nfail;
            if (++spins > SPIN_CAP) { fail = true; break; }
        }
        *(LAS f32x2*)(lds + YL_RED + (w * 64 + lane) * 8) = f32x2{y0, y1};
        lead = pace(lead, (unsigned)a.lead_y, spins > 0);
        CS(4);
        asm volatile("s_waitcnt lgkmcnt(0)\n\ts_barrier" ::: "memory");
        CS(5);
        if (w == 0) {
            const f32x2 t = (*(LAS f32x2*)(lds + YL_RED + lane * 8) + *(LAS f32x2*)(lds + YL_RED + (64 + lane) * 8)) + (*(LAS f32x2*)(lds + YL_RED + (128 + lane) * 8) + *(LAS f32x2*)(lds + YL_RED + (192 + lane) * 8));
            const float x0 = t.x, x1 = t.y;
            const float y0 = u2f(__builtin_amdgcn_update_dpp(0u, f2u(x0), 0xB1, 0xF, 0xF, false)), y1 = u2f(__builtin_amdgcn_update_dpp(0u, f2u(x1), 0xB1, 0xF, 0xF, false));
            const unsigned dw = nosent((c & 1) ? pk2(y1, x1) : pk2(x0, y0));
            const unsigned d1 = __builtin_amdgcn_update_dpp(0u, dw, 0x102, 0xF, 0xF, false), d2 = __builtin_amdgcn_update_dpp(0u, dw, 0x104, 0xF, 0xF, false), d3 = __builtin_amdgcn_update_dpp(0u, dw, 0x106, 0xF, 0xF, false);
            v4u val = {dw, d1, d2, d3};
            const int fo = (tp >> 1) < 8 ? (tp >> 1) * 1024 : 16384;
            const int off = (c & 6) == 0 ? X_MEM + fo + (16 * (2 * (tp & 1) + (c & 1)) + 8 * rho + 2 * g + (c >> 3)) * 16 : 0x7F000000;
#pragma unroll
            for (int q = 0; q < NREP; ++q) __builtin_amdgcn_raw_buffer_store_b128(val, rX, mem_slot(s + 1) * X_MEM_SLOT + q * X_MEM_REP + off, 0, AUX_SC1);
            const v4u pz = poison4();
#pragma unroll
            for (int q = 0; q < NREP; ++q) __builtin_amdgcn_raw_buffer_store_b128(pz, rX, s > 0 ? mem_slot(s) * X_MEM_SLOT + q * X_MEM_REP + off : OOB, 0, AUX_SC1);
        }
        CS(6);
    }
    if (tp == 0 && rho == 0) CS_OUT(8192 + 64);
    TM(2);
    if (fail && threadIdx.x == 0) __hip_atomic_store(ctl, 1u, RLX_AGENT);
}

#define SP_NST 3
#define OUT_NST 4
constexpr int SP_LDS = GP<128, 192, SP_NST>::LDS;
__global__ void __launch_bounds__(256, 1) k_sp(const u16* __restrict__ wsp, const u16* __restrict__ bsp, const u16* __restrict__ z2n, const u16* __restrict__ z1, u16* __restrict__ u) {
    extern __shared__ __attribute__((aligned(16))) unsigned char smem[];
    LAS unsigned char* lds = (LAS unsigned char*)smem;
    const int b = blockIdx.x, l = b & 7, r = b >> 3, mt = 4 * (l & 3) + (r & 3), nt = 6 * (l >> 2) + (r >> 2);
    const int lane = lane_id(), wave = wave_id(), wm = wave >> 1, wn = wave & 1;
    GemmStream<128, 192, SP_NST> gs; gs.init(lds, S_, FH);
    const rsrc_t rA = mk_rsrc(wsp + (size_t)mt * 128 * S_, 128 * S_ * 2), rB = mk_rsrc(z2n + nt * 192, S_ * FH * 2);
    gs.prime(rA, rB);
    f32x16 acc[2][3];
    gs.template tile<0, true>(rA, rB, rA, rB, false, 32, acc);
#pragma unroll
    for (int i = 0; i < 2; ++i) {
        const int row = wm * 64 + i * 32 + (lane & 31);
        const float bb = bf2f(bsp[mt * 128 + row]);
#pragma unroll
        for (int j = 0; j < 3; ++j)
#pragma unroll
            for (int rg = 0; rg < 4; ++rg) {
                const int col = wn * 96 + j * 32 + 8 * rg + 4 * (lane >> 5);
                const v2u o = {pk2(rbf(acc[i][j][4 * rg]) + bb, rbf(acc[i][j][4 * rg + 1]) + bb), pk2(rbf(acc[i][j][4 * rg + 2]) + bb, rbf(acc[i][j][4 * rg + 3]) + bb)};
                *(LAS v2u*)(lds + row * 384 + ((((col >> 3) ^ (row & 7)) << 4) | ((col & 4) << 1))) = o;
            }
    }
    __syncthreads();
#pragma unroll
    for (int it = 0; it < 12; ++it) {
        const int id = it * 256 + threadIdx.x, row = id / 24, ch = id % 24;
        const v4u gv = *(LAS v4u*)(lds + row * 384 + ((ch ^ (row & 7)) << 4));
        const size_t o = (size_t)(mt * 128 + row) * FH + nt * 192 + ch * 8;
        const v4u zv = *(const v4u*)(z1 + o);
        float gf[8], zf[8]; unpack8(gv, gf); unpack8(zv, zf);
        *(v4u*)(u + o) = v4u{pk2(zf[0] * gf[0], zf[1] * gf[1]), pk2(zf[2] * gf[2], zf[3] * gf[3]), pk2(zf[4] * gf[4], zf[5] * gf[5]), pk2(zf[6] * gf[6], zf[7] * gf[7])};
    }
}
constexpr int OUT_LDS = GP<128, 64, OUT_NST>::LDS;
__global__ void __launch_bounds__(256, 1) k_out(const u16* __restrict__ u, const u16* __restrict__ Wout, const u16* __restrict__ x1, const float* __restrict__ gb, u16* __restrict__ out) {
    extern __shared__ __attribute__((aligned(16))) unsigned char smem[];
    LAS unsigned char* lds = (LAS unsigned char*)smem;
    const int b = blockIdx.x, mt = (b & 7) * 2 + ((b >> 3) & 1), nt = b >> 4;
    const int lane = lane_id(), wave = wave_id(), wm = wave >> 1, wn = wave & 1;
    GemmStream<128, 64, OUT_NST> gs; gs.init(lds, FH, D_);
    const rsrc_t rA = mk_rsrc(u + (size_t)mt * 128 * FH, 128 * FH * 2), rB = mk_rsrc(Wout + nt * 64, FH * D_ * 2);
    gs.prime(rA, rB);
    f32x16 acc[2][1];
    gs.template tile2<true, false>(rA, rB, 36, acc);
#pragma unroll
    for (int i = 0; i < 2; ++i) {
        const int row = wm * 64 + i * 32 + (lane & 31);
#pragma unroll
        for (int rg = 0; rg < 4; ++rg) {
            const int col = wn * 32 + 8 * rg + 4 * (lane >> 5);
            const v2u o = {pk2(acc[i][0][4 * rg], acc[i][0][4 * rg + 1]), pk2(acc[i][0][4 * rg + 2], acc[i][0][4 * rg + 3])};
            *(LAS v2u*)(lds + row * 128 + ((((col >> 3) ^ (row & 7)) << 4) | ((col & 4) << 1))) = o;
        }
    }
    __syncthreads();
#pragma unroll
    for (int it = 0; it < 4; ++it) {
        const int id = it * 256 + threadIdx.x, row = id >> 3, ch = id & 7;
        const v4u yv = *(LAS v4u*)(lds + row * 128 + ((ch ^ (row & 7)) << 4));
        const int n = nt * 64 + ch * 8;
        const size_t o = (size_t)(mt * 128 + row) * D_ + n;
        float yf[8], xf[8]; unpack8(yv, yf); unpack8(*(const v4u*)(x1 + o), xf);
        float r[8];
#pragma unroll
        for (int e = 0; e < 8; ++e) r[e] = xf[e] + rbf(gb[n + e] * yf[e]);
        *(v4u*)(out + o) = v4u{pk2(r[0], r[1]), pk2(r[2], r[3]), pk2(r[4], r[5]), pk2(r[6], r[7])};
    }
}

#define HXN 4
#define YXN 2
#define PLACE 1
#define MIX 0
constexpr int MAIN_LDS = 160 * 1024;
static_assert(HL_END <= MAIN_LDS, "lds");
DI void publish_par(const Params& a) {
    if (threadIdx.x < 64) {
        const int lane = lane_id();
        const rsrc_t rP = mk_rsrc(a.xr + X_BYTES + 8192, 65536);
        unsigned lat[2] = {~0u, ~0u};
        for (int it = 0; it < 10; ++it) {
#pragma unroll
            for (int p = 0; p < 2; ++p) {
                const int off = lane < 8 ? p * 8192 + (it & 7) * 1024 + lane * 16 : OOB;
                __builtin_amdgcn_raw_buffer_store_b128(v4u{0u, 0u, 0u, 0u}, rP, off, 0, AUX_SC1);
                asm volatile("s_waitcnt vmcnt(0)" ::: "memory");
                const unsigned long long t0 = cyc();
                const v4u z = __builtin_amdgcn_raw_buffer_load_b128(rP, off, 0, AUX_SC1);
                asm volatile("s_waitcnt vmcnt(0)" :: "v"(z) : "memory");
                const unsigned d = (unsigned)(cyc() - t0);
                lat[p] = d < lat[p] ? d : lat[p];
            }
        }
        if (lane < 8) __hip_atomic_store(ctl_word(a, C_PAR + lane * 32), 0x100u | (lat[0] < lat[1] ? 1u : 0u), RLX_AGENT);
    }
}
__global__ void __launch_bounds__(256, 1) k_main(Params a) {
    extern __shared__ __attribute__((aligned(16))) unsigned char smem[];
    LAS unsigned char* lds = (LAS unsigned char*)smem;
    const int b = blockIdx.x;
    TM(0);
    if (b == 0 && threadIdx.x < 8) __hip_atomic_store(ctl_word(a, C_CFG + threadIdx.x * 32), 0x100u | ((unsigned)__builtin_amdgcn_s_getreg(20 | (3 << 11)) & 7u), RLX_AGENT);
    {
        const int j = b;
    if (j < 108) { task_mod(a, j, lds); if (j < 36) { wg_signal(a, C_MODA); if (threadIdx.x == 0) (void)__hip_atomic_fetch_add(ctl_word(a, C_MODB), 1u, RLX_AGENT); } else wg_signal(a, C_MODB); }
    }
    if (threadIdx.x == 0) { (void)poll_ge(a, ctl_word(a, C_CFG + (b & 7) * 32), 0x100u); *(LAS unsigned*)lds = __hip_atomic_load(ctl_word(a, C_CFG + (b & 7) * 32), RLX_AGENT); }
    __syncthreads();
    const unsigned cfg = (unsigned)__builtin_amdgcn_readfirstlane((int)*(LAS unsigned*)lds);
    __syncthreads();
    const int x0 = cfg & 7;
    const int l = ((x0 + (b & 7)) & 7) ^ (x0 & 4), r = b >> 3;
    if (l < 2) { if (r < 16) head_role(a, l * 16 + r, lds); else if (l == 0 && r == 16) publish_par(a); }
    else if (l < 4) { if (r < 18) y_role(a, r, l - 2, lds); }
    else worker_role(a, r * 4 + (l - 4), lds);
}

#define LEAD_H 800
#define LEAD_Y 700
extern "C" void kernel_launch(void* const* d_in, const int* in_sizes, int n_in, void* d_out, int out_size, void* d_ws, size_t ws_size, hipStream_t stream) {
    const u16* x = (const u16*)d_in[0];
    const u16* Wsp = (const u16*)d_in[9];
    const u16* bsp = (const u16*)d_in[10];
    const u16* Wout = (const u16*)d_in[11];
    unsigned char* ws = (unsigned char*)d_ws;
    float* mod = (float*)(ws + WS_MOD);
    unsigned char* xr = (unsigned char*)(((uintptr_t)ws + WS_END + (1u << 20) + (2u << 20) - 1) & ~(uintptr_t)((2u << 20) - 1));
    (void)hipMemsetAsync(ws + WS_CTL, 0, CTL_BYTES, stream);
    (void)hipMemsetAsync(xr, 0xFF, X_HINT, stream);
    (void)hipMemsetAsync(xr + X_HINT, 0, 16384 + 8192, stream);
    (void)hipFuncSetAttribute((const void*)k_sp, hipFuncAttributeMaxDynamicSharedMemorySize, SP_LDS);
    (void)hipFuncSetAttribute((const void*)k_out, hipFuncAttributeMaxDynamicSharedMemorySize, OUT_LDS);
    (void)hipFuncSetAttribute((const void*)k_main, hipFuncAttributeMaxDynamicSharedMemorySize, MAIN_LDS);
    Params p;
    p.x = x; p.mem0 = (const u16*)d_in[1]; p.Wq = (const u16*)d_in[2]; p.Wk = (const u16*)d_in[3]; p.Wv = (const u16*)d_in[4]; p.Wo = (const u16*)d_in[5];
    p.Win = (const u16*)d_in[8]; p.c = (const u16*)d_in[12]; p.wmod = (const u16*)d_in[13];
    p.ws = ws; p.xr = xr;
    p.lead_h = LEAD_H; p.lead_y = LEAD_Y;
    k_main<<<256, 256, MAIN_LDS, stream>>>(p);
    k_sp<<<192, 256, SP_LDS, stream>>>(Wsp, bsp, (const u16*)(ws + WS_Z2N), (const u16*)(ws + WS_Z1), (u16*)(ws + WS_U));
    k_out<<<144, 256, OUT_LDS, stream>>>((const u16*)(ws + WS_U), Wout, (const u16*)(ws + WS_X1), mod + 5 * D_, (u16*)d_out);
}
```

```cpp
#include <hip/hip_runtime.h>
#include <hip/hip_bf16.h>
#include <stdint.h>

#define GAS __attribute__((address_space(1)))
#define LAS __attribute__((address_space(3)))
#define DI __device__ __forceinline__
typedef unsigned short u16;
typedef __bf16 bf16x2 __attribute__((ext_vector_type(2)));
typedef __bf16 bf16x8 __attribute__((ext_vector_type(8)));
typedef __bf16 bf16x16 __attribute__((ext_vector_type(16)));
typedef short s16x4 __attribute__((ext_vector_type(4)));
typedef float f32x2 __attribute__((ext_vector_type(2)));
typedef float f32x4 __attribute__((ext_vector_type(4)));
typedef float f32x16 __attribute__((ext_vector_type(16)));
typedef unsigned v2u __attribute__((ext_vector_type(2)));
typedef unsigned v4u __attribute__((ext_vector_type(4)));
typedef unsigned v8u __attribute__((ext_vector_type(8)));
typedef GAS unsigned gu32;
typedef __amdgpu_buffer_rsrc_t rsrc_t;
#define RLX_AGENT __ATOMIC_RELAXED, __HIP_MEMORY_SCOPE_AGENT

constexpr int S_ = 2048, D_ = 576, H_ = 32, E_ = 128, HE = 4096, NSEG = 64, FF = 4608, FH = 2304, NRB = 16;
constexpr int OX_LD = HE + 128, VT_LD = S_ + 128;
constexpr int AUX_SC1 = 16;
constexpr int RSRC3 = 0x00020000;
constexpr int OOB = 0x7FFFFF00;

constexpr size_t WS_CTL = 0, CTL_BYTES = 131072;
constexpr size_t WS_MOD = CTL_BYTES;
constexpr size_t WS_H1 = WS_MOD + 16384;
constexpr size_t WS_Q = WS_H1 + (size_t)S_ * D_ * 2;
constexpr size_t WS_K = WS_Q + (size_t)S_ * HE * 2;
constexpr size_t WS_VT = WS_K + (size_t)S_ * HE * 2;
constexpr size_t WS_KVM = WS_VT + (size_t)HE * VT_LD * 2;
constexpr size_t WS_OX = WS_KVM + (size_t)NSEG * H_ * 4096;
constexpr size_t WS_PART = WS_OX + (size_t)S_ * OX_LD * 2;
constexpr size_t WS_X1 = WS_PART + (size_t)4 * S_ * D_ * 4;
constexpr size_t WS_H2 = WS_X1 + (size_t)S_ * D_ * 2;
constexpr size_t WS_Z1 = WS_H2 + (size_t)S_ * D_ * 2;
constexpr size_t WS_Z2 = WS_Z1 + (size_t)S_ * FH * 2;
constexpr size_t WS_Z2N = WS_Z2 + (size_t)S_ * FH * 2;
constexpr size_t WS_U = WS_Z2N + (size_t)S_ * FH * 2;
constexpr size_t WS_END = WS_U + (size_t)S_ * FH * 2;
#define NREP 1
constexpr int X_MEM = 0, X_MEM_REP = 32768, X_MEM_SLOT = NREP * X_MEM_REP;
constexpr int X_O = 3 * X_MEM_SLOT, X_O_SLOT = 131072;
constexpr int X_HINT = X_O + 2 * X_O_SLOT;
constexpr int X_BYTES = X_HINT + 16384;
constexpr int C_TMO = 0, C_MODA = 32, C_MODB = 64, C_H1 = 96, C_OX = 128, C_PART = C_OX + 32 * NRB, C_H2 = C_PART + 32 * NRB, C_Z = C_H2 + 32 * NRB,
              C_KV = 4096, C_Q = C_KV + 16 * NRB * H_, C_END = C_Q + 16 * NRB * H_;
constexpr int C_CHAIN = 2560, C_INIT = 2592, C_VOTE = 2608, C_H1R = 3520, C_U = 2624, C_ZN = C_U + 16 * NRB;
constexpr int C_CFG = 3200, C_PAR = C_CFG + 16;
static_assert(C_Z + 32 * NRB <= 2560 && C_ZN + 16 * NRB <= 3200 && 3520 + 16 * NRB <= C_KV && C_END * 4 <= (int)CTL_BYTES, "control block");

DI unsigned f2u(float x) { return __builtin_bit_cast(unsigned, x); }
DI float u2f(unsigned x) { return __builtin_bit_cast(float, x); }
DI float bf2f(u16 b) { return __builtin_bit_cast(float, (unsigned)b << 16); }
DI float bflo(unsigned w) { return __builtin_bit_cast(float, w << 16); }
DI float bfhi(unsigned w) { return __builtin_bit_cast(float, w & 0xFFFF0000u); }
DI unsigned pk2(float lo, float hi) { f32x2 v = {lo, hi}; bf16x2 b = __builtin_convertvector(v, bf16x2); return __builtin_bit_cast(unsigned, b); }
DI float rbf(float x) { return bflo(pk2(x, 0.f) & 0xFFFFu); }
DI float fadd1(float a, float b) { float r; asm("v_add_f32_e32 %0, %1, %2" : "=v"(r) : "v"(a), "v"(b)); return r; }
DI int lane_id() { return (int)(threadIdx.x & 63); }
DI int wave_id() { return __builtin_amdgcn_readfirstlane((int)(threadIdx.x >> 6)); }

DI f32x4 mfma16(bf16x8 a, bf16x8 b, f32x4 c) { return __builtin_amdgcn_mfma_f32_16x16x32_bf16(a, b, c, 0, 0, 0); }
typedef short s16x4v __attribute__((ext_vector_type(4)));
DI f32x4 mfma16k16(v2u a, v2u b, f32x4 c) { return __builtin_amdgcn_mfma_f32_16x16x16bf16_1k(__builtin_bit_cast(s16x4v, a), __builtin_bit_cast(s16x4v, b), c, 0, 0, 0); }
DI bf16x8 tr2(LAS unsigned char* a0, LAS unsigned char* a1) {
    typedef short s16x8 __attribute__((ext_vector_type(8)));
    const s16x4 lo = __builtin_amdgcn_ds_read_tr16_b64_v4i16((LAS s16x4*)a0), hi = __builtin_amdgcn_ds_read_tr16_b64_v4i16((LAS s16x4*)a1);
    s16x8 v = {lo[0], lo[1], lo[2], lo[3], hi[0], hi[1], hi[2], hi[3]};
    return __builtin_bit_cast(bf16x8, v);
}
DI float xmax16_32(float m) {
    unsigned u = f2u(m);
    auto a = __builtin_amdgcn_permlane16_swap(u, u, false, false);
    m = fmaxf(u2f(a[0]), u2f(a[1]));
    u = f2u(m);
    auto b = __builtin_amdgcn_permlane32_swap(u, u, false, false);
    return fmaxf(u2f(b[0]), u2f(b[1]));
}
DI float xsum16_32(float m) {
    unsigned u = f2u(m);
    auto a = __builtin_amdgcn_permlane16_swap(u, u, false, false);
    m = u2f(a[0]) + u2f(a[1]);
    u = f2u(m);
    auto b = __builtin_amdgcn_permlane32_swap(u, u, false, false);
    return u2f(b[0]) + u2f(b[1]);
}
constexpr float SCALE = 0.08838834764831845f;
constexpr float SCL = 0.08838834764831845f * 1.4426950408889634f;

DI f32x4 smf(bf16x8 a, bf16x16 b, f32x4 c, int idx) { return __builtin_amdgcn_smfmac_f32_16x16x64_bf16(a, b, c, idx, 0, 0); }
DI unsigned nosent(unsigned x) { return x < 0xFFFFFFFEu ? x : 0xFFFFFFFEu; }
DI unsigned umax3(unsigned a, unsigned b, unsigned c) { unsigned r; asm("v_max3_u32 %0, %1, %2, %3" : "=v"(r) : "v"(a), "v"(b), "v"(c)); return r; }
DI unsigned vmax(unsigned acc, v4u v) { return umax3(umax3(acc, v.x, v.y), v.z, v.w); }
DI bool all_valid(v4u v) { return (int)(v.x != ~0u) & (int)(v.y != ~0u) & (int)(v.z != ~0u) & (int)(v.w != ~0u); }
#define CB() asm volatile("" ::: "memory")
#define SMF_A(acc, a, wt, idx) asm volatile("v_smfmac_f32_16x16x64_bf16 %0, %1, %2, %3" : "+v"(acc) : "v"(a), "a"(wt), "v"(idx))
#define SMF_V(acc, a, wt, idx) asm volatile("v_smfmac_f32_16x16x64_bf16 %0, %1, %2, %3" : "+v"(acc) : "v"(a), "v"(wt), "v"(idx))
#define MFMA32_0(d, a, b) asm volatile("v_mfma_f32_16x16x32_bf16 %0, %1, %2, 0" : "=&v"(d) : "v"(a), "v"(b))
#define MFMA32_C(d, a, b, cin) asm volatile("v_mfma_f32_16x16x32_bf16 %0, %1, %2, %3" : "=&v"(d) : "v"(a), "v"(b), "v"(cin))
#define MFMA16_0(d, a, b) asm volatile("v_mfma_f32_16x16x16_bf16 %0, %1, %2, 0" : "=&v"(d) : "v"(a), "v"(b))
#define VMAX3(d, a, b, c) asm volatile("v_max3_f32 %0, %1, %2, %3" : "=v"(d) : "v"(a), "v"(b), "v"(c))
#define VMAX(d, a, b) asm volatile("v_max_f32_e32 %0, %1, %2" : "=v"(d) : "v"(a), "v"(b))
#define VMOV(d, a) asm volatile("v_mov_b32_e32 %0, %1" : "=v"(d) : "v"(a))
#define VADD(d, a, b) asm volatile("v_add_f32_e32 %0, %1, %2" : "=v"(d) : "v"(a), "v"(b))
#define VCVT(d, a, b) asm volatile("v_cvt_pk_bf16_f32 %0, %1, %2" : "=v"(d) : "v"(a), "v"(b))
#define VEXP(x) asm volatile("v_exp_f32_e32 %0, %0" : "+v"(x))
#define SWAP16(a, b) asm volatile("v_permlane16_swap_b32_e32 %0, %1" : "+v"(a), "+v"(b))
#define SWAP32(a, b) asm volatile("v_permlane32_swap_b32_e32 %0, %1" : "+v"(a), "+v"(b))
#define PKFMA(d, a, sc, ms) asm volatile("v_pk_fma_f32 %0, %1, %2, %3 op_sel_hi:[1,0,0] neg_lo:[0,0,1] neg_hi:[0,0,1]" : "=v"(d) : "v"(a), "s"(sc), "v"(ms))
#define PKMUL(d, a, r) asm volatile("v_pk_mul_f32 %0, %1, %2 op_sel_hi:[1,0]" : "=v"(d) : "v"(a), "v"(r))
DI f32x4 zero4() { f32x2 a, b; asm volatile("v_mov_b64 %0, 0" : "=v"(a)); asm volatile("v_mov_b64 %0, 0" : "=v"(b)); return f32x4{a[0], a[1], b[0], b[1]}; }
DI v4u poison4() { v2u a, b; asm volatile("v_mov_b64 %0, -1" : "=v"(a)); asm volatile("v_mov_b64 %0, -1" : "=v"(b)); return v4u{a[0], a[1], b[0], b[1]}; }
#define NOP(n) asm volatile("s_nop " #n)
#define MFMA32_ACC(d, a, b) asm volatile("v_mfma_f32_16x16x32_bf16 %0, %1, %2, %0" : "+v"(d) : "v"(a), "v"(b))
#define MFMA16_ACC(d, a, b) asm volatile("v_mfma_f32_16x16x16_bf16 %0, %1, %2, %0" : "+v"(d) : "v"(a), "v"(b))

struct Params {
    const u16* x; const u16* mem0; const u16* Wq; const u16* Wk; const u16* Wv; const u16* Wo;
    const u16* Win; const u16* c; const u16* wmod; const u16* Wsp; const u16* bsp; const u16* Wout;
    u16* out;
    unsigned char* ws;
    unsigned char* xr;
    int lead_h, lead_y;
};
DI rsrc_t mk_rsrc(const void* p, unsigned bytes) {
    const unsigned long long u = (unsigned long long)(uintptr_t)p;
    const unsigned lo = (unsigned)__builtin_amdgcn_readfirstlane((int)(unsigned)u), hi = (unsigned)__builtin_amdgcn_readfirstlane((int)(unsigned)(u >> 32));
    return __builtin_amdgcn_make_buffer_rsrc((void*)(uintptr_t)(((unsigned long long)hi << 32) | lo), 0, (int)bytes, RSRC3);
}
DI void glds16(const void* g, LAS unsigned char* l) { __builtin_amdgcn_global_load_lds((const GAS unsigned*)g, (LAS unsigned*)l, 16, 0, 0); }
DI unsigned long long cyc() { unsigned long long t; asm volatile("s_memtime %0\n\ts_waitcnt lgkmcnt(0)" : "=s"(t) :: "memory"); return t; }
#define TM(slot)
#define TR(base, i)

constexpr unsigned WAIT_CAP = 1u << 17;
DI gu32* ctl_word(const Params& a, int idx) { return (gu32*)(a.ws + WS_CTL) + idx; }
DI void wg_signal(const Params& a, int idx) {
    asm volatile("s_waitcnt vmcnt(0)" ::: "memory");
    __syncthreads();
    if (threadIdx.x == 0) (void)__hip_atomic_fetch_add(ctl_word(a, idx), 1u, RLX_AGENT);
}
DI bool poll_ge(const Params& a, gu32* word, unsigned want) {
    for (unsigned spins = 0;; ++spins) {
        if (__hip_atomic_load(word, RLX_AGENT) >= want) return true;
        __builtin_amdgcn_s_sleep(2);
        if (spins > WAIT_CAP) { __hip_atomic_store(ctl_word(a, C_TMO), 1u, RLX_AGENT); return false; }
        if ((spins & 63u) == 63u && __hip_atomic_load(ctl_word(a, C_TMO), RLX_AGENT) != 0u) return false;
    }
}
DI void wg_wait_long(const Params& a, gu32* word, unsigned want, unsigned near) {
    if (threadIdx.x == 0) {
        for (unsigned spins = 0; spins < WAIT_CAP; ++spins) {
            if (__hip_atomic_load(word, RLX_AGENT) + near >= want) break;
            __builtin_amdgcn_s_sleep(64);
            if ((spins & 15u) == 15u && __hip_atomic_load(ctl_word(a, C_TMO), RLX_AGENT) != 0u) break;
        }
        (void)poll_ge(a, word, want);
        __builtin_amdgcn_fence(__ATOMIC_ACQUIRE, "wavefront");
    }
    __syncthreads();
}
template <bool ACQ> DI void wg_wait(const Params& a, gu32* word, unsigned want) {
    if (threadIdx.x == 0) {
        (void)poll_ge(a, word, want);
        if (ACQ) { __builtin_amdgcn_fence(__ATOMIC_ACQUIRE, "agent"); asm volatile("s_waitcnt vmcnt(0)" ::: "memory"); }
        else __builtin_amdgcn_fence(__ATOMIC_ACQUIRE, "wavefront");
    }
    __syncthreads();
}
template <int BM, int BN, int NST> struct GP {
    static constexpr int A_BYTES = BM * 128, B_BYTES = BN * 128, STAGE = A_BYTES + B_BYTES, LDS = NST * STAGE;
    static constexpr int TM = BM / 64, TN = BN / 64, PA = BM / 32, PB = BN / 32, LPS = PA + PB;
    static constexpr int p0(int ks) { return ks * LPS / 4; }
};
template <bool SC1> DI void dma16(unsigned m0v, int voff, rsrc_t r, int soff) {
    if constexpr (SC1) asm volatile("s_mov_b32 m0, %0\n\ts_nop 0\n\tbuffer_load_dwordx4 %1, %2, %3 offen sc1 lds" :: "s"(m0v), "v"(voff), "s"(r), "s"(soff) : "memory");
    else asm volatile("s_mov_b32 m0, %0\n\ts_nop 0\n\tbuffer_load_dwordx4 %1, %2, %3 offen lds" :: "s"(m0v), "v"(voff), "s"(r), "s"(soff) : "memory");
}
template <int N> DI void wait_vm() { asm volatile("s_waitcnt vmcnt(%0)" :: "i"(N) : "memory"); }
#define GSB() __builtin_amdgcn_sched_barrier(0)

template <int BM, int BN, int NST, bool ASC1 = false, bool BSC1 = false> struct GemmStream {
    using C = GP<BM, BN, NST>;
    LAS unsigned char* lds; unsigned lbase; int wave, ldb128;
    int voA[C::PA], voB[C::PB];
    int aoff, asw, h, boff[C::TN];
    int slot;
    DI void init(LAS unsigned char* l, int lda, int ldb) {
        lds = l; lbase = (unsigned)(size_t)l; slot = 0; ldb128 = 128 * ldb;
        const int lane = lane_id(); wave = wave_id();
        const int wm = wave >> 1, wn = wave & 1;
#pragma unroll
        for (int p = 0; p < C::PA; ++p) { const int piece = wave + 4 * p, row = piece * 8 + (lane >> 3), c = (lane & 7) ^ ((row >> 1) & 7); voA[p] = (row * lda + c * 8) * 2; }
#pragma unroll
        for (int p = 0; p < C::PB; ++p) { const int piece = wave + 4 * p, sub = piece >> 3, k = (piece & 7) * 8 + (lane >> 3), c = (lane & 7) ^ (((k >> 1) & 1) << 2); voB[p] = (k * ldb + sub * 64 + c * 8) * 2; }
        const int r31 = lane & 31; h = lane >> 5; asw = (r31 >> 1) & 7; aoff = (wm * (BM / 2) + r31) * 128;
        const int blk = (lane >> 4) & 1, q = (lane & 15) >> 2, p = lane & 3;
#pragma unroll
        for (int j = 0; j < C::TN; ++j) { const int n = wn * (BN / 2) + j * 32 + 16 * blk + 4 * p, sub = n >> 6, cn = n & 63; boff[j] = C::A_BYTES + sub * 8192 + (8 * h + q) * 128 + (((cn >> 3) ^ (((q >> 1) & 1) << 2)) << 4) + (cn & 7) * 2; }
    }
    template <int I> DI void piece(rsrc_t rA, rsrc_t rB, int t, int sl) {
        const unsigned sb = lbase + sl * C::STAGE;
        if constexpr (I < C::PA) dma16<ASC1>(sb + (wave + 4 * I) * 1024, voA[I], rA, t * 128);
        else dma16<BSC1>(sb + C::A_BYTES + (wave + 4 * (I - C::PA)) * 1024, voB[I - C::PA], rB, t * ldb128);
    }
    template <int I0, int N> DI void pieces(rsrc_t rA, rsrc_t rB, int t, int sl) {
        if constexpr (N > 0) { piece<I0>(rA, rB, t, sl); pieces<I0 + 1, N - 1>(rA, rB, t, sl); }
    }
    DI void prime(rsrc_t rA, rsrc_t rB) {
#pragma unroll
        for (int t = 0; t < NST - 1; ++t) { int sl = slot + t; if (sl >= NST) sl -= NST; pieces<0, C::LPS>(rA, rB, t, sl); }
    }
    DI void primeB(rsrc_t rB) {
#pragma unroll
        for (int t = 0; t < NST - 1; ++t) { int sl = slot + t; if (sl >= NST) sl -= NST; pieces<C::PA, C::PB>(rB, rB, t, sl); }
    }
    DI void primeA(rsrc_t rA) {
#pragma unroll
        for (int t = 0; t < NST - 1; ++t) { int sl = slot + t; if (sl >= NST) sl -= NST; pieces<0, C::PA>(rA, rA, t, sl); }
    }
    DI void ldfrags(LAS unsigned char* cur, int ks, bf16x8 (&af)[C::TM], bf16x8 (&bf)[C::TN]) {
#pragma unroll
        for (int i = 0; i < C::TM; ++i) af[i] = *(LAS bf16x8*)(cur + aoff + i * 4096 + (((2 * ks + h) ^ asw) << 4));
#pragma unroll
        for (int j = 0; j < C::TN; ++j) bf[j] = tr2(cur + boff[j] + ks * 2048, cur + boff[j] + ks * 2048 + 512);
    }
    static constexpr int NM = C::TM * C::TN;
    template <int K, int P0> DI void job(LAS unsigned char* src, int ksn, bool rd, bf16x8 (&an)[C::TM], bf16x8 (&bn)[C::TN], bool issue, rsrc_t sA, rsrc_t sB, int ts, int sp) {
        if constexpr (K < C::TN) { if (rd) bn[K] = tr2(src + boff[K] + ksn * 2048, src + boff[K] + ksn * 2048 + 512); }
        else if constexpr (K < C::TN + C::TM) { if (rd) an[K - C::TN] = *(LAS bf16x8*)(src + aoff + (K - C::TN) * 4096 + (((2 * ksn + h) ^ asw) << 4)); }
        else { if (issue) piece<P0 + K - C::TN - C::TM>(sA, sB, ts, sp); }
    }
    template <int M, int K, int P0, int NJ> DI void jobs_after(LAS unsigned char* src, int ksn, bool rd, bf16x8 (&an)[C::TM], bf16x8 (&bn)[C::TN], bool issue, rsrc_t sA, rsrc_t sB, int ts, int sp) {
        if constexpr (K < NJ) {
            if constexpr (K * NM / NJ == M) job<K, P0>(src, ksn, rd, an, bn, issue, sA, sB, ts, sp);
            jobs_after<M, K + 1, P0, NJ>(src, ksn, rd, an, bn, issue, sA, sB, ts, sp);
        }
    }
    template <int M, int KS, bool SWAP> DI void kstep(const bf16x8 (&ac)[C::TM], const bf16x8 (&bc)[C::TN], f32x16 (&acc)[C::TM][C::TN], LAS unsigned char* src, int ksn, bool rd,
                                           bf16x8 (&an)[C::TM], bf16x8 (&bn)[C::TN], bool issue, rsrc_t sA, rsrc_t sB, int ts, int sp) {
        if constexpr (M < NM) {
            constexpr int i = M / C::TN, j = M % C::TN;
            acc[i][j] = SWAP ? __builtin_amdgcn_mfma_f32_32x32x16_bf16(bc[j], ac[i], acc[i][j], 0, 0, 0) : __builtin_amdgcn_mfma_f32_32x32x16_bf16(ac[i], bc[j], acc[i][j], 0, 0, 0);
            GSB();
            jobs_after<M, 0, C::p0(KS), C::TM + C::TN + C::p0(KS + 1) - C::p0(KS)>(src, ksn, rd, an, bn, issue, sA, sB, ts, sp);
            GSB();
            kstep<M + 1, KS, SWAP>(ac, bc, acc, src, ksn, rd, an, bn, issue, sA, sB, ts, sp);
        }
    }
    template <bool SWAP, bool SPLITP>
    DI void tile2(rsrc_t rA, rsrc_t rB, int nt, f32x16 (&acc)[C::TM][C::TN]) {
#pragma unroll
        for (int i = 0; i < C::TM; ++i)
#pragma unroll
            for (int j = 0; j < C::TN; ++j)
#pragma unroll
                for (int r = 0; r < 16; ++r) acc[i][j][r] = 0.f;
        bf16x8 a0[C::TM], b0[C::TN], a1[C::TM], b1[C::TN], a2[C::TM], b2[C::TN], a3[C::TM], b3[C::TN];
        if constexpr (SPLITP) wait_vm<(NST - 2) * C::PA>(); else wait_vm<(NST - 2) * C::LPS>();
        asm volatile("s_barrier" ::: "memory");
        ldfrags(lds + slot * C::STAGE, 0, a0, b0);
        ldfrags(lds + slot * C::STAGE, 1, a1, b1);
        for (int t = 0; t < nt; ++t) {
            LAS unsigned char* cur = lds + slot * C::STAGE;
            int sn = slot + 1; if (sn >= NST) sn -= NST;
            int sp = slot + NST - 1; if (sp >= NST) sp -= NST;
            const int tn = t + NST - 1;
            const bool issue = tn < nt, more = t + 1 < nt;
            kstep<0, 0, SWAP>(a0, b0, acc, cur, 2, true, a2, b2, issue, rA, rB, tn, sp);
            kstep<0, 1, SWAP>(a1, b1, acc, cur, 3, true, a3, b3, issue, rA, rB, tn, sp);
            if (more) {
                if (!issue) wait_vm<0>();
                else if (SPLITP && t + 1 <= NST - 2) { if (t == 0) wait_vm<(NST - 3) * C::PA + C::p0(2)>(); else wait_vm<C::p0(2)>(); }
                else wait_vm<(NST - 3) * C::LPS + C::p0(2)>();
            }
            asm volatile("s_waitcnt lgkmcnt(0)\n\ts_barrier" ::: "memory");
            kstep<0, 2, SWAP>(a2, b2, acc, lds + sn * C::STAGE, 0, more, a0, b0, issue, rA, rB, tn, sp);
            kstep<0, 3, SWAP>(a3, b3, acc, lds + sn * C::STAGE, 1, more, a1, b1, issue, rA, rB, tn, sp);
            slot = sn;
        }
    }
    struct NoGate { DI void operator()(int) const {} };
    template <int EXTRA, bool SWAP, int SPLITP = 0, class G = NoGate>
    DI void tile(rsrc_t rA, rsrc_t rB, rsrc_t nA, rsrc_t nB, bool has_next, int nt, f32x16 (&acc)[C::TM][C::TN], G gate = G()) {
#pragma unroll
        for (int i = 0; i < C::TM; ++i)
#pragma unroll
            for (int j = 0; j < C::TN; ++j)
#pragma unroll
                for (int r = 0; r < 16; ++r) acc[i][j][r] = 0.f;
        bf16x8 a0[C::TM], b0[C::TN], a1[C::TM], b1[C::TN];
        constexpr int PL = SPLITP == 2 ? C::PB : C::PA;
        if constexpr (SPLITP != 0) wait_vm<(NST - 2) * PL>(); else wait_vm<(NST - 2) * C::LPS + EXTRA>();
        asm volatile("s_barrier" ::: "memory");
        ldfrags(lds + slot * C::STAGE, 0, a0, b0);
        for (int t = 0; t < nt; ++t) {
            LAS unsigned char* cur = lds + slot * C::STAGE;
            int sn = slot + 1; if (sn >= NST) sn -= NST;
            int sp = slot + NST - 1; if (sp >= NST) sp -= NST;
            const int tn = t + NST - 1;
            const bool in_cur = tn < nt, issue = in_cur || has_next;
            const rsrc_t sA = in_cur ? rA : nA, sB = in_cur ? rB : nB;
            const int ts = in_cur ? tn : tn - nt;
            if (in_cur) gate(tn);
            kstep<0, 0, SWAP>(a0, b0, acc, cur, 1, true, a1, b1, issue, sA, sB, ts, sp);
            kstep<0, 1, SWAP>(a1, b1, acc, cur, 2, true, a0, b0, issue, sA, sB, ts, sp);
            kstep<0, 2, SWAP>(a0, b0, acc, cur, 3, true, a1, b1, issue, sA, sB, ts, sp);
            if (t + 1 < nt) {
                if (!issue) wait_vm<0>();
                else if (t + 1 <= NST - 2) { if constexpr (SPLITP != 0) { if (t == 0) wait_vm<(NST - 3) * PL + C::p0(3)>(); else wait_vm<C::p0(3)>(); } else wait_vm<(NST - 3) * C::LPS + C::p0(3) + EXTRA>(); }
                else wait_vm<(NST - 3) * C::LPS + C::p0(3)>();
            }
            asm volatile("s_waitcnt lgkmcnt(0)\n\ts_barrier" ::: "memory");
            kstep<0, 3, SWAP>(a1, b1, acc, lds + sn * C::STAGE, 0, t + 1 < nt, a0, b0, issue, sA, sB, ts, sp);
            slot = sn;
        }
    }
};

DI unsigned wave_poll_ge(const Params& a, gu32* word, unsigned want) {
    unsigned v;
    for (unsigned spins = 0;; ++spins) {
        v = (unsigned)__builtin_amdgcn_readfirstlane((int)__hip_atomic_load(word, RLX_AGENT));
        if (v >= want) break;
        __builtin_amdgcn_s_sleep(4);
        if (spins > WAIT_CAP) { __hip_atomic_store(ctl_word(a, C_TMO), 1u, RLX_AGENT); break; }
        if ((spins & 63u) == 63u && __builtin_amdgcn_readfirstlane((int)__hip_atomic_load(ctl_word(a, C_TMO), RLX_AGENT)) != 0) break;
    }
    __builtin_amdgcn_fence(__ATOMIC_ACQUIRE, "wavefront");
    return v;
}
DI int get_par(const Params& a) { return (int)(wave_poll_ge(a, ctl_word(a, C_PAR + (blockIdx.x & 7) * 32), 0x100u) & 1u); }
DI float wsum8(float v) { v += __shfl_xor(v, 1); v += __shfl_xor(v, 2); v += __shfl_xor(v, 4); return v; }
DI void unpack8(v4u w, float (&f)[8]) { f[0] = bflo(w.x); f[1] = bfhi(w.x); f[2] = bflo(w.y); f[3] = bfhi(w.y); f[4] = bflo(w.z); f[5] = bfhi(w.z); f[6] = bflo(w.w); f[7] = bfhi(w.w); }

DI void task_mod(const Params& a, int task, LAS unsigned char* lds) {
    LAS float* red = (LAS float*)lds;
    const int tid = threadIdx.x, cg = tid & 3, kr = tid >> 2;
    const int col0 = task * 32 + cg * 8;
    float acc[8];
#pragma unroll
    for (int j = 0; j < 8; ++j) acc[j] = 0.f;
    v4u w[9];
#pragma unroll
    for (int p = 0; p < 9; ++p) w[p] = *(const v4u*)(a.wmod + (size_t)(kr + 64 * p) * 3456 + col0);
#pragma unroll
    for (int p = 0; p < 9; ++p) {
        const float cv = bf2f(a.c[kr + 64 * p]);
        const float sv = rbf(cv / (1.f + __expf(-cv)));
        float f[8]; unpack8(w[p], f);
#pragma unroll
        for (int j = 0; j < 8; ++j) acc[j] += sv * f[j];
    }
#pragma unroll
    for (int j = 0; j < 8; ++j) red[kr * 33 + cg * 8 + j] = acc[j];
    __syncthreads();
    if (tid < 32) {
        float s = 0.f;
        for (int r = 0; r < 64; ++r) s += red[r * 33 + tid];
        __hip_atomic_store((gu32*)(a.ws + WS_MOD) + task * 32 + tid, f2u(rbf(s)), RLX_AGENT);
    }
}
DI void load_modtab(const Params& a, LAS float* tab, int i_shift, int i_scale, int i_gate) {
    const rsrc_t rM = mk_rsrc(a.ws + WS_MOD, 3456 * 4);
    for (int i = threadIdx.x; i < D_; i += 256) {
        tab[i] = rbf(1.f + u2f(__builtin_amdgcn_raw_buffer_load_b32(rM, (i_scale * D_ + i) * 4, 0, AUX_SC1)));
        tab[D_ + i] = u2f(__builtin_amdgcn_raw_buffer_load_b32(rM, (i_shift * D_ + i) * 4, 0, AUX_SC1));
        tab[2 * D_ + i] = i_gate >= 0 ? u2f(__builtin_amdgcn_raw_buffer_load_b32(rM, (i_gate * D_ + i) * 4, 0, AUX_SC1)) : 0.f;
    }
    __syncthreads();
}
DI void task_ln1(const Params& a, int row0, LAS float* tab) {
    const int lane = lane_id(), w = wave_id();
    const int row = row0 + 8 * w + (lane >> 3), sub = lane & 7;
    float v[9][8];
#pragma unroll
    for (int i = 0; i < 9; ++i) unpack8(*(const v4u*)(a.x + (size_t)row * D_ + (sub + 8 * i) * 8), v[i]);
    float s = 0.f;
#pragma unroll
    for (int i = 0; i < 9; ++i)
#pragma unroll
        for (int j = 0; j < 8; ++j) s += v[i][j];
    const float mean = wsum8(s) * (1.f / D_);
    float q = 0.f;
#pragma unroll
    for (int i = 0; i < 9; ++i)
#pragma unroll
        for (int j = 0; j < 8; ++j) { const float d = v[i][j] - mean; q += d * d; }
    const float meanb = rbf(mean), rden = 1.f / rbf(sqrtf(rbf(rbf(wsum8(q) * (1.f / D_)) + 1e-5f)));
#pragma unroll
    for (int i = 0; i < 9; ++i)
#pragma unroll
        for (int j = 0; j < 8; ++j) v[i][j] = rbf(rbf(v[i][j] - meanb) * rden);
    wg_wait<false>(a, ctl_word(a, C_MODA), 36u);
    {
        const rsrc_t rM = mk_rsrc(a.ws + WS_MOD, 3456 * 4);
        unsigned sc[3], sh[3];
#pragma unroll
        for (int k = 0; k < 3; ++k) { const int i = threadIdx.x + 256 * k; sc[k] = __builtin_amdgcn_raw_buffer_load_b32(rM, i < D_ ? (D_ + i) * 4 : OOB, 0, AUX_SC1); sh[k] = __builtin_amdgcn_raw_buffer_load_b32(rM, i < D_ ? i * 4 : OOB, 0, AUX_SC1); }
#pragma unroll
        for (int k = 0; k < 3; ++k) { const int i = threadIdx.x + 256 * k; if (i < D_) { tab[i] = rbf(1.f + u2f(sc[k])); tab[D_ + i] = u2f(sh[k]); } }
        __syncthreads();
    }
    const rsrc_t rO = mk_rsrc(a.ws + WS_H1, S_ * D_ * 2);
#pragma unroll
    for (int i = 0; i < 9; ++i) {
        float o[8];
#pragma unroll
        for (int j = 0; j < 8; ++j) { const int cc = (sub + 8 * i) * 8 + j; o[j] = rbf(v[i][j] * tab[cc]) + tab[D_ + cc]; }
        const v4u ov = {pk2(o[0], o[1]), pk2(o[2], o[3]), pk2(o[4], o[5]), pk2(o[6], o[7])};
        __builtin_amdgcn_raw_buffer_store_b128(ov, rO, (row * D_ + (sub + 8 * i) * 8) * 2, 0, AUX_SC1);
    }
}
DI float wsum64(float v) {
#pragma unroll
    for (int m = 1; m < 64; m <<= 1) v += __shfl_xor(v, m);
    return v;
}
template <int NR>
DI void task_ln2(const Params& a, int row0, LAS float* tab) {
    const int lane = lane_id(), w = wave_id();
    const rsrc_t rX1 = mk_rsrc(a.ws + WS_X1, S_ * D_ * 2), rH2 = mk_rsrc(a.ws + WS_H2, S_ * D_ * 2);
    const bool act = lane < 48;
    v4u p[NR][3][4]; v2u xv[NR][3];
#pragma unroll
    for (int it = 0; it < NR; ++it) {
        const int row = row0 + w * NR + it;
        const rsrc_t rP = mk_rsrc(a.ws + WS_PART + (size_t)(row >> 7) * 4 * 128 * D_ * 4, 4 * 128 * D_ * 4);
#pragma unroll
        for (int i = 0; i < 3; ++i) {
            const int col = (lane + 48 * i) * 4;
            xv[it][i] = act ? *(const v2u*)(a.x + (size_t)row * D_ + col) : v2u{0u, 0u};
#pragma unroll
            for (int kq = 0; kq < 4; ++kq) p[it][i][kq] = __builtin_amdgcn_raw_buffer_load_b128(rP, act ? ((kq * 128 + (row & 127)) * D_ + col) * 4 : OOB, 0, AUX_SC1);
        }
    }
#pragma unroll
    for (int it = 0; it < NR; ++it) {
        const int row = row0 + w * NR + it;
        float v[3][4]; float s = 0.f;
#pragma unroll
        for (int i = 0; i < 3; ++i) {
            const int col = act ? (lane + 48 * i) * 4 : 0;
            const float xs[4] = {bflo(xv[it][i].x), bfhi(xv[it][i].x), bflo(xv[it][i].y), bfhi(xv[it][i].y)};
#pragma unroll
            for (int j = 0; j < 4; ++j) {
                const float t = (u2f(p[it][i][0][j]) + u2f(p[it][i][1][j])) + (u2f(p[it][i][2][j]) + u2f(p[it][i][3][j]));
                v[i][j] = act ? rbf(xs[j] + rbf(tab[2 * D_ + col + j] * rbf(t))) : 0.f;
                s += v[i][j];
            }
        }
        const float mean = wsum64(s) * (1.f / D_);
        float q = 0.f;
#pragma unroll
        for (int i = 0; i < 3; ++i)
#pragma unroll
            for (int j = 0; j < 4; ++j) { const float d = act ? v[i][j] - mean : 0.f; q += d * d; }
        const float meanb = rbf(mean), rden = 1.f / rbf(sqrtf(rbf(rbf(wsum64(q) * (1.f / D_)) + 1e-5f)));
#pragma unroll
        for (int i = 0; i < 3; ++i) {
            const int col = act ? (lane + 48 * i) * 4 : 0;
            float o[4];
#pragma unroll
            for (int j = 0; j < 4; ++j) o[j] = rbf(rbf(rbf(v[i][j] - meanb) * rden) * tab[col + j]) + tab[D_ + col + j];
            const unsigned a0 = pk2(v[i][0], v[i][1]), a1 = pk2(v[i][2], v[i][3]), b0 = pk2(o[0], o[1]), b1 = pk2(o[2], o[3]);
            const v4u xo = {a0, a1, (unsigned)__shfl_down((int)a0, 1), (unsigned)__shfl_down((int)a1, 1)}, ho = {b0, b1, (unsigned)__shfl_down((int)b0, 1), (unsigned)__shfl_down((int)b1, 1)};
            const int off = (act && (lane & 1) == 0) ? (row * D_ + col) * 2 : OOB;
            __builtin_amdgcn_raw_buffer_store_b128(xo, rX1, off, 0, AUX_SC1);
            __builtin_amdgcn_raw_buffer_store_b128(ho, rH2, off, 0, AUX_SC1);
        }
    }
}
template <int NIT>
DI void task_ln3(const Params& a, int row0) {
    const int lane = lane_id(), w = wave_id(), sub = lane & 31;
    const rsrc_t rZ = mk_rsrc(a.ws + WS_Z2, S_ * FH * 2), rN = mk_rsrc(a.ws + WS_Z2N, S_ * FH * 2);
    v4u raw[NIT][9];
#pragma unroll
    for (int it = 0; it < NIT; ++it)
#pragma unroll
        for (int i = 0; i < 9; ++i) raw[it][i] = __builtin_amdgcn_raw_buffer_load_b128(rZ, ((row0 + 2 * NIT * w + 2 * it + (lane >> 5)) * FH + (sub + 32 * i) * 8) * 2, 0, AUX_SC1);
#pragma unroll
    for (int it = 0; it < NIT; ++it) {
        const int row = row0 + 2 * NIT * w + 2 * it + (lane >> 5);
        float v[9][8];
#pragma unroll
        for (int i = 0; i < 9; ++i) unpack8(raw[it][i], v[i]);
        float s = 0.f;
#pragma unroll
        for (int i = 0; i < 9; ++i)
#pragma unroll
            for (int j = 0; j < 8; ++j) s += v[i][j];
#pragma unroll
        for (int m = 1; m < 32; m <<= 1) s += __shfl_xor(s, m);
        const float mean = s * (1.f / FH);
        float q = 0.f;
#pragma unroll
        for (int i = 0; i < 9; ++i)
#pragma unroll
            for (int j = 0; j < 8; ++j) { const float d = v[i][j] - mean; q += d * d; }
#pragma unroll
        for (int m = 1; m < 32; m <<= 1) q += __shfl_xor(q, m);
        const float meanb = rbf(mean), rden = 1.f / rbf(sqrtf(rbf(rbf(q * (1.f / FH)) + 1e-5f)));
#pragma unroll
        for (int i = 0; i < 9; ++i) {
            float o[8];
#pragma unroll
            for (int j = 0; j < 8; ++j) o[j] = rbf(v[i][j] - meanb) * rden;
            const v4u ov = {pk2(o[0], o[1]), pk2(o[2], o[3]), pk2(o[4], o[5]), pk2(o[6], o[7])};
            __builtin_amdgcn_raw_buffer_store_b128(ov, rN, (row * FH + (sub + 32 * i) * 8) * 2, 0, AUX_SC1);
        }
    }
}

constexpr int EPI_STORES = 8;
template <class F> DI void epi_tile128(const f32x16 (&acc)[2][2], LAS unsigned char* img, bool swapped, rsrc_t rO, int dst, int rstride, F&& f) {
    const int lane = lane_id(), wave = wave_id(), wm = wave >> 1, wn = wave & 1;
#pragma unroll
    for (int i = 0; i < 2; ++i)
#pragma unroll
        for (int j = 0; j < 2; ++j)
#pragma unroll
            for (int rg = 0; rg < 4; ++rg) {
                int row, col;
                if (swapped) { row = wm * 64 + i * 32 + (lane & 31); col = wn * 64 + j * 32 + 8 * rg + 4 * (lane >> 5); }
                else {
                    row = wn * 64 + j * 32 + (lane & 31);
                    const int tk = 8 * rg + 4 * (lane >> 5);
                    col = wm * 64 + i * 32 + (tk < 16 ? 2 * tk : 2 * (tk - 16) + 4);
                }
                const v2u o = {pk2(f(acc[i][j][4 * rg]), f(acc[i][j][4 * rg + 1])), pk2(f(acc[i][j][4 * rg + 2]), f(acc[i][j][4 * rg + 3]))};
                *(LAS v2u*)(img + row * 256 + ((((col >> 3) ^ (row & 7)) << 4) | ((col & 4) << 1))) = o;
            }
    asm volatile("s_waitcnt lgkmcnt(0)\n\ts_barrier" ::: "memory");
#pragma unroll
    for (int it = 0; it < EPI_STORES; ++it) {
        const int id = it * 256 + threadIdx.x, row = id >> 4, ch = id & 15;
        const v4u v = *(LAS v4u*)(img + row * 256 + ((ch ^ (row & 7)) << 4));
        __builtin_amdgcn_raw_buffer_store_b128(v, rO, dst + row * rstride + ch * 16, 0, AUX_SC1);
    }
    asm volatile("s_waitcnt lgkmcnt(0)\n\ts_barrier" ::: "memory");
}

DI float gelu_tanh(float x) {
    const float t = x * (1.f + 0.044715f * x * x);
    return x * __builtin_amdgcn_rcpf(1.f + __builtin_amdgcn_exp2f(t * (-2.f * 0.7978845608028654f * 1.4426950408889634f)));
}

DI void qkv_decode(int t, int& rb, int& head, int& mat) {
    if (t < 2 * NRB * H_) { rb = t >> 6; head = (t >> 1) & 31; mat = 1 + (t & 1); }
    else { const int u = t - 2 * NRB * H_; rb = u >> 5; head = u & 31; mat = 0; }
}
DI void phase_qkv(const Params& a, int j, int nw, LAS unsigned char* lds) {
    using GS = GemmStream<128, 128, 3, true>;
    GS gs; gs.init(lds, D_, HE);
    LAS unsigned char* img = lds + GS::C::LDS;
    constexpr int NT = 3 * NRB * H_;
    auto rA = [&](int t) { int rb, h, m; qkv_decode(t, rb, h, m); return mk_rsrc(a.ws + WS_H1 + (size_t)rb * 128 * D_ * 2, 128 * D_ * 2); };
    auto rB = [&](int t) { int rb, h, m; qkv_decode(t, rb, h, m); return mk_rsrc((m == 0 ? a.Wq : (m == 1 ? a.Wk : a.Wv)) + h * 128, D_ * HE * 2); };
    if (j >= NT) return;
    const rsrc_t rA0 = rA(j), rB0 = rB(j);
    gs.primeB(rB0);
    { int rb0, h0, m0; qkv_decode(j, rb0, h0, m0); wg_wait<false>(a, ctl_word(a, C_H1R + rb0 * 16), 4u); }
    if (j == 0) TR(5900, 3);
    gs.primeA(rA0);
    auto gate = [&](int tn) {
        if (tn == 8) { if (threadIdx.x == 0) { (void)poll_ge(a, ctl_word(a, C_H1), 64u); __builtin_amdgcn_fence(__ATOMIC_ACQUIRE, "wavefront"); } asm volatile("s_barrier" ::: "memory"); }
    };
    bool first = true;
    for (int t = j; t < NT; t += nw) {
        int rb, head, mat; qkv_decode(t, rb, head, mat);
        const bool hn = t + nw < NT; const int tn = hn ? t + nw : t;
        f32x16 acc[2][2];
        if (mat == 2) { if (first) gs.template tile<0, false, 1>(rA(t), rB(t), rA(tn), rB(tn), hn, 9, acc, gate); else gs.template tile<EPI_STORES, false>(rA(t), rB(t), rA(tn), rB(tn), hn, 9, acc); }
        else          { if (first) gs.template tile<0, true, 1>(rA(t), rB(t), rA(tn), rB(tn), hn, 9, acc, gate);  else gs.template tile<EPI_STORES, true>(rA(t), rB(t), rA(tn), rB(tn), hn, 9, acc); }
        first = false;
        if (mat == 2) epi_tile128(acc, img, false, mk_rsrc(a.ws + WS_VT, HE * VT_LD * 2), (head * E_ * VT_LD + rb * 128) * 2, VT_LD * 2, [](float x) { return x; });
        else epi_tile128(acc, img, true, mk_rsrc(a.ws + (mat == 0 ? WS_Q : WS_K), S_ * HE * 2), (head * S_ + rb * 128) * E_ * 2, E_ * 2, [](float x) { return x; });
        wg_signal(a, (mat == 0 ? C_Q : C_KV) + (rb * H_ + head) * 16);
        if (t == 0) TR(5900, 4);
    }
}

DI void xattn_unit(const Params& a, int seg, int head, LAS unsigned char* wl  ) {
    const int lane = lane_id(), c = lane & 15, g = lane >> 4;
    const rsrc_t rQ = mk_rsrc(a.ws + WS_Q, S_ * HE * 2), rK = mk_rsrc(a.ws + WS_K, S_ * HE * 2), rV = mk_rsrc(a.ws + WS_VT, HE * VT_LD * 2);
    const rsrc_t rM = mk_rsrc(a.ws + WS_KVM, NSEG * H_ * 4096), rO = mk_rsrc(a.ws + WS_OX, S_ * OX_LD * 2);
    const int tok0 = head * S_ + seg * 32;
    const int km = (seg * H_ + head) * 4096;
    {
        const v4u x0 = __builtin_amdgcn_raw_buffer_load_b128(rM, km + lane * 16, 0, AUX_SC1), x1 = __builtin_amdgcn_raw_buffer_load_b128(rM, km + 1024 + lane * 16, 0, AUX_SC1);
        *(LAS v4u*)(wl + lane * 16) = x0; *(LAS v4u*)(wl + 1024 + lane * 16) = x1;
    }
    v4u qf[2][4], kf[2][4];
#pragma unroll
    for (int t = 0; t < 2; ++t)
#pragma unroll
        for (int ks = 0; ks < 4; ++ks) {
            qf[t][ks] = __builtin_amdgcn_raw_buffer_load_b128(rQ, ((tok0 + 16 * t + c) * E_ + 32 * ks + 8 * g) * 2, 0, AUX_SC1);
            kf[t][ks] = __builtin_amdgcn_raw_buffer_load_b128(rK, ((tok0 + 16 * t + c) * E_ + 32 * ks + 8 * g) * 2, 0, AUX_SC1);
        }
    v4u vf[8]; v2u vmf[8];
#pragma unroll
    for (int et = 0; et < 8; ++et) {
        const int e = 16 * et + c;
        vf[et] = __builtin_amdgcn_raw_buffer_load_b128(rV, ((head * E_ + e) * VT_LD + seg * 32 + 8 * g) * 2, 0, AUX_SC1);
        vmf[et] = __builtin_amdgcn_raw_buffer_load_b64(rM, km + 2048 + e * 16 + (g & 1) * 8, 0, AUX_SC1);
    }
    __builtin_amdgcn_s_waitcnt(0xC07F);
    __builtin_amdgcn_wave_barrier();
    const int q = c >> 2, p = c & 3;
    f32x4 st[2][3];
#pragma unroll
    for (int t = 0; t < 2; ++t)
#pragma unroll
        for (int kt = 0; kt < 3; ++kt) st[t][kt] = f32x4{0, 0, 0, 0};
#pragma unroll
    for (int ks = 0; ks < 4; ++ks) {
        const int e0 = 32 * ks + 8 * g + q;
        const bf16x8 km8 = tr2(wl + e0 * 16 + (p & 1) * 8, wl + (e0 + 4) * 16 + (p & 1) * 8);
#pragma unroll
        for (int t = 0; t < 2; ++t) {
            const bf16x8 qq = __builtin_bit_cast(bf16x8, qf[t][ks]);
            st[t][0] = mfma16(km8, qq, st[t][0]);
            st[t][1] = mfma16(__builtin_bit_cast(bf16x8, kf[0][ks]), qq, st[t][1]);
            st[t][2] = mfma16(__builtin_bit_cast(bf16x8, kf[1][ks]), qq, st[t][2]);
        }
    }
    bf16x8 pf[2]; v2u pm[2]; float rz[2];
#pragma unroll
    for (int t = 0; t < 2; ++t) {
        const int qi = 16 * t + c;
        float s[12];
#pragma unroll
        for (int r = 0; r < 4; ++r) {
            s[r] = g < 2 ? rbf(rbf(st[t][0][r]) * SCALE) : -INFINITY;
            s[4 + r] = (4 * g + r) <= qi ? rbf(rbf(st[t][1][r]) * SCALE) : -INFINITY;
            s[8 + r] = (16 + 4 * g + r) <= qi ? rbf(rbf(st[t][2][r]) * SCALE) : -INFINITY;
        }
        float m = s[0];
#pragma unroll
        for (int r = 1; r < 12; ++r) m = fmaxf(m, s[r]);
        m = xmax16_32(m);
        float z = 0.f;
#pragma unroll
        for (int r = 0; r < 12; ++r) { s[r] = __builtin_amdgcn_exp2f((s[r] - m) * 1.4426950408889634f); z += s[r]; }
        z = xsum16_32(z);
        const float rzz = 1.f / z;
#pragma unroll
        for (int r = 0; r < 12; ++r) s[r] *= rzz;
        rz[t] = 1.f;
        pm[t] = v2u{pk2(s[0], s[1]), pk2(s[2], s[3])};
        const v4u pp = {pk2(s[4], s[5]), pk2(s[6], s[7]), pk2(s[8], s[9]), pk2(s[10], s[11])};
        pf[t] = __builtin_bit_cast(bf16x8, pp);
    }
#pragma unroll
    for (int ep = 0; ep < 4; ++ep) {
        f32x4 o[2][2];
#pragma unroll
        for (int et = 0; et < 2; ++et)
#pragma unroll
            for (int t = 0; t < 2; ++t) {
                o[et][t] = mfma16(__builtin_bit_cast(bf16x8, vf[2 * ep + et]), pf[t], f32x4{0, 0, 0, 0});
                o[et][t] = mfma16k16(vmf[2 * ep + et], pm[t], o[et][t]);
            }
#pragma unroll
        for (int t = 0; t < 2; ++t) {
            const unsigned d0 = pk2(o[0][t][0] * rz[t], o[0][t][1] * rz[t]), d1 = pk2(o[0][t][2] * rz[t], o[0][t][3] * rz[t]);
            const unsigned e0 = pk2(o[1][t][0] * rz[t], o[1][t][1] * rz[t]), e1 = pk2(o[1][t][2] * rz[t], o[1][t][3] * rz[t]);
            auto s0 = __builtin_amdgcn_permlane16_swap(d0, e0, false, false);
            auto s1 = __builtin_amdgcn_permlane16_swap(d1, e1, false, false);
            const v4u val = {s0[0], s1[0], s0[1], s1[1]};
            const int eo = 32 * ep + ((g & 1) << 4) + ((g >> 1) << 3);
            __builtin_amdgcn_raw_buffer_store_b128(val, rO, ((seg * 32 + 16 * t + c) * OX_LD + head * E_ + eo) * 2, 0, AUX_SC1);
        }
    }
}
DI void role_att(const Params& a, int head, LAS unsigned char* lds) {
    const int w = wave_id(), lane = lane_id();
    const int par = get_par(a);
    for (int rb = 0; rb < NRB; ++rb) {
        const int seg = 4 * rb + w;
        (void)wave_poll_ge(a, ctl_word(a, C_KV + (rb * H_ + head) * 16), 2u); (void)wave_poll_ge(a, ctl_word(a, C_Q + (rb * H_ + head) * 16), 1u);
        (void)wave_poll_ge(a, (gu32*)(a.xr + par * 8192 + X_HINT + 8192 + head * 256), (unsigned)(seg + 2));
        xattn_unit(a, seg, head, lds + w * 2048);
        wg_signal(a, C_OX + rb * 32);
    }
}
DI void role_wo(const Params& a, int task, LAS unsigned char* lds) {
    using GS = GemmStream<128, 64, 4, true>;
    GS gs; gs.init(lds, OX_LD, D_);
    const int kq = task & 3, nt = task >> 2;
    const int lane = lane_id(), wave = wave_id(), wm = wave >> 1, wn = wave & 1;
    const rsrc_t rB = mk_rsrc(a.Wo + (size_t)kq * 1024 * D_ + nt * 64, 1024 * D_ * 2);
    for (int rb = 0; rb < NRB; ++rb) {
        gs.primeB(rB);
        wg_wait<false>(a, ctl_word(a, C_OX + rb * 32), 32u);
        if (task == 0) TR(5800, rb * 4);
        const rsrc_t rA = mk_rsrc(a.ws + WS_OX + ((size_t)rb * 128 * OX_LD + kq * 1024) * 2, 128 * OX_LD * 2);
        gs.primeA(rA);
        f32x16 acc[2][1];
        gs.template tile2<true, true>(rA, rB, 16, acc);
        if (task == 0) TR(5800, rb * 4 + 1);
        const rsrc_t rP = mk_rsrc(a.ws + WS_PART + (size_t)(rb * 4 + kq) * 128 * D_ * 4, 128 * D_ * 4);
        LAS unsigned char* img = lds + GS::C::LDS;
#pragma unroll
        for (int i = 0; i < 2; ++i)
#pragma unroll
            for (int rg = 0; rg < 4; ++rg) {
                const int m = wm * 64 + i * 32 + (lane & 31), ch = wn * 8 + 2 * rg + (lane >> 5);
                *(LAS f32x4*)(img + m * 256 + ((ch ^ (m & 15)) << 4)) = f32x4{acc[i][0][4 * rg], acc[i][0][4 * rg + 1], acc[i][0][4 * rg + 2], acc[i][0][4 * rg + 3]};
            }
        asm volatile("s_waitcnt lgkmcnt(0)\n\ts_barrier" ::: "memory");
#pragma unroll
        for (int it = 0; it < 8; ++it) {
            const int id = it * 256 + threadIdx.x, m = id >> 4, ch = id & 15;
            const v4u v = *(LAS v4u*)(img + m * 256 + ((ch ^ (m & 15)) << 4));
            __builtin_amdgcn_raw_buffer_store_b128(v, rP, (m * D_ + nt * 64) * 4 + ch * 16, 0, AUX_SC1);
        }
        wg_signal(a, C_PART + rb * 32);
        if (task == 0) TR(5800, rb * 4 + 2);
    }
}
DI void role_ln2(const Params& a, int q, LAS unsigned char* lds) {
    LAS float* tab = (LAS float*)lds;
    wg_wait<false>(a, ctl_word(a, C_MODB), 108u);
    load_modtab(a, tab, 3, 4, 2);
    for (int rb = 0; rb < NRB; ++rb) {
        wg_wait<false>(a, ctl_word(a, C_PART + rb * 32), 36u);
        task_ln2<2>(a, rb * 128 + q * 8, tab);
        wg_signal(a, C_H2 + rb * 32);
    }
}
DI void role_win(const Params& a, int nt, LAS unsigned char* lds) {
    using GS = GemmStream<128, 128, 3, true>;
    GS gs; gs.init(lds, D_, FF);
    LAS unsigned char* img = lds + GS::C::LDS;
    const rsrc_t rB = mk_rsrc(a.Win + nt * 128, D_ * FF * 2);
    const rsrc_t rO = mk_rsrc(a.ws + (nt < 18 ? WS_Z1 : WS_Z2), S_ * FH * 2);
    for (int rb = 0; rb < NRB; ++rb) {
        gs.primeB(rB);
        wg_wait<false>(a, ctl_word(a, C_H2 + rb * 32), 16u);
        const rsrc_t rA = mk_rsrc(a.ws + WS_H2 + (size_t)rb * 128 * D_ * 2, 128 * D_ * 2);
        gs.primeA(rA);
        f32x16 acc[2][2];
        gs.template tile<0, true, 1>(rA, rB, rA, rB, false, 9, acc);
        epi_tile128(acc, img, true, rO, (rb * 128 * FH + (nt % 18) * 128) * 2, FH * 2, [](float x) { return gelu_tanh(rbf(x)); });
        wg_signal(a, C_Z + rb * 32);
    }
}
DI void role_ln3(const Params& a, int q) {
    for (int rb = 0; rb < NRB; ++rb) {
        wg_wait<false>(a, ctl_word(a, C_Z + rb * 32), 36u);
        task_ln3<2>(a, rb * 128 + q * 16);
        wg_signal(a, C_ZN + rb * 16);
    }
}
DI void worker_role(const Params& a, int j, LAS unsigned char* lds) {
    if (j < 64) {
        task_ln1(a, j * 32, (LAS float*)lds);
        asm volatile("s_waitcnt vmcnt(0)" ::: "memory");
        __syncthreads();
        if (threadIdx.x == 0) { (void)__hip_atomic_fetch_add(ctl_word(a, C_H1R + (j >> 2) * 16), 1u, RLX_AGENT); (void)__hip_atomic_fetch_add(ctl_word(a, C_H1), 1u, RLX_AGENT); }
        if (j == 0) TR(5900, 2);
    }
    constexpr int NQKV = 128;
    phase_qkv(a, j, NQKV, lds);
    TM(1);
#define RMASK 31
    if (j < 32) { if (RMASK & 1) role_att(a, j, lds); }
    else if (j < 68) { if (RMASK & 2) role_wo(a, j - 32, lds); }
    else if (j < 84) { if (RMASK & 4) role_ln2(a, j - 68, lds); }
    else if (j < 120) { if (RMASK & 8) role_win(a, j - 84, lds); }
    else if (j < 128) { if (RMASK & 16) role_ln3(a, j - 120); }
    TM(2);
}

constexpr unsigned SPIN_CAP = 1u << 14;
#define P_INC 50
#define P_DEC 2
DI unsigned pace(unsigned lead, unsigned base, bool early) { return early ? (lead + P_INC < base + 1200u ? lead + P_INC : lead) : (lead > P_DEC && lead + 300u > base ? lead - P_DEC : lead); }
DI bool hint_wait(rsrc_t rX, int w, int lane, unsigned want) {
    const int off = lane < 8 ? X_HINT + (8 * w + lane) * 256 : OOB;
    for (unsigned spins = 0; spins < (1u << 16); ++spins) {
        CB();
        const unsigned v = __builtin_amdgcn_raw_buffer_load_b32(rX, off, 0, AUX_SC1);
        if (!__any(lane < 8 && v < want)) return true;
    }
    return false;
}
DI void delay_from(unsigned long long t0, unsigned d) { while ((unsigned)(cyc() - t0) + 40u < d) {} }
#define CS(i)
#define CS_DECL
#define CS_OUT(base)
#define STR2_(x) #x
#define STR_(x) STR2_(x)
#define LDGAP ""
DI int mem_slot(int s) { return s % 3; }
DI int o_slot(int s) { return s & 1; }
constexpr int HL_Q = 0;
constexpr int HL_K = HL_Q + 2048;
constexpr int HL_KX = HL_K + 2048;
constexpr int HL_WV = HL_KX + 2 * 8192;
constexpr int HL_STAGE = HL_WV + 4 * 9 * 2048;
constexpr int HL_END = HL_STAGE + 4 * 16384;

DI int wsw(int k) { return ((k & 1) | (((k >> 2) & 1) << 1) | (((k >> 3) & 1) << 2)) << 1; }
constexpr int w_slot(int n) {
    const int k = n < 18 ? n % 9 : (n - 18) % 5;
    return k < 4 ? HL_STAGE + k * 16384 : (k == 4 ? HL_KX : HL_WV + (k - 5) * 16384);
}
constexpr int w_issue(int n) { return n <= 22 ? n - 8 : n - 4; }
constexpr int w_younger(int n) { int y = 0; for (int m = n + 1; m < 27; ++m) if (m < 8 || w_issue(m) <= n) ++y; return y; }
DI void stage_w(LAS unsigned char* dst, rsrc_t r, const int (&vo)[4], int soff, int wave) {
#pragma unroll
    for (int p = 0; p < 4; ++p) dma16<false>((unsigned)(size_t)dst + (wave + 4 * p) * 1024, vo[p], r, soff);
}
DI bf16x16 read_wfrag(LAS unsigned char* st, int col, int lane) {
    const int gb = lane >> 4, q = (lane & 15) >> 2, p = lane & 3;
    s16x4 r[4];
#pragma unroll
    for (int m = 0; m < 4; ++m) {
        const int d0 = 32 * (m >> 1) + 16 * (gb >> 1) + 4 * (gb & 1) + 2 * (m & 1);
        const int k = d0 + (q & 1) + 8 * (q >> 1);
        const int cb = (col + 4 * p) * 2;
        const int ch = (cb >> 4) ^ wsw(k);
        r[m] = __builtin_amdgcn_ds_read_tr16_b64_v4i16((LAS s16x4*)(st + k * 256 + ch * 16 + (cb & 15)));
    }
    typedef short s16x16 __attribute__((ext_vector_type(16)));
    s16x16 v = {r[0][0], r[0][1], r[0][2], r[0][3], r[1][0], r[1][1], r[1][2], r[1][3], r[2][0], r[2][1], r[2][2], r[2][3], r[3][0], r[3][1], r[3][2], r[3][3]};
    return __builtin_bit_cast(bf16x16, v);
}

DI void head_role(const Params& a, int h, LAS unsigned char* lds) {
    const int lane = lane_id(), w = wave_id(), c = lane & 15, g = lane >> 4;
    unsigned char* ws = a.ws;
    gu32* ctl = (gu32*)(ws + WS_CTL);
    const rsrc_t rKVM = mk_rsrc(ws + WS_KVM, NSEG * H_ * 4096);
    bf16x16 WA[4][8];
    bf16x16 WB[4];
    bf16x16 WV[9];
    LAS unsigned char* wv1 = lds + HL_WV + w * 9 * 2048 + lane * 16;
    {
        const rsrc_t src[3] = {mk_rsrc(a.Wq + h * 128, D_ * HE * 2), mk_rsrc(a.Wk + h * 128, D_ * HE * 2), mk_rsrc(a.Wv + h * 128, D_ * HE * 2)};
        int vo[4];
#pragma unroll
        for (int p = 0; p < 4; ++p) { const int k = (w + 4 * p) * 4 + (lane >> 4); vo[p] = (k * HE + ((lane & 15) ^ wsw(k)) * 8) * 2; }
#define W_STAGE(n) stage_w(lds + w_slot(n), src[(n) / 9], vo, ((n) % 9) * 64 * HE * 2, w)
#define W_READ(n) { wait_vm<4 * w_younger(n)>(); __builtin_amdgcn_s_barrier(); \
            const bf16x16 f0 = read_wfrag(lds + w_slot(n), 32 * w, lane), f1 = read_wfrag(lds + w_slot(n), 32 * w + 16, lane); \
            constexpr int m_ = (n) / 9, f_ = (n) % 9; \
            if constexpr (m_ < 2 && f_ < 8) { WA[2 * m_][f_] = f0; WA[2 * m_ + 1][f_] = f1; asm volatile("" : "+a"(WA[2 * m_][f_]), "+a"(WA[2 * m_ + 1][f_])); } \
            else if constexpr (m_ < 2) { WB[2 * m_] = f0; WB[2 * m_ + 1] = f1; } \
            else { WV[f_] = f0; const v8u t = __builtin_bit_cast(v8u, f1); \
                   *(LAS v4u*)(wv1 + f_ * 2048) = v4u{t[0], t[1], t[2], t[3]}; *(LAS v4u*)(wv1 + f_ * 2048 + 1024) = v4u{t[4], t[5], t[6], t[7]}; } \
            asm volatile("s_waitcnt lgkmcnt(0)" ::: "memory"); __builtin_amdgcn_s_barrier(); }
        W_STAGE(0); W_STAGE(1); W_STAGE(2); W_STAGE(3); W_STAGE(4); W_STAGE(5); W_STAGE(6); W_STAGE(7);
        W_STAGE(8); W_READ(0); W_STAGE(9); W_READ(1); W_STAGE(10); W_READ(2); W_STAGE(11); W_READ(3); W_STAGE(12); W_READ(4); W_STAGE(13); W_READ(5);
        W_STAGE(14); W_READ(6); W_STAGE(15); W_READ(7); W_STAGE(16); W_READ(8); W_STAGE(17); W_READ(9); W_STAGE(18); W_READ(10); W_STAGE(19); W_READ(11);
        W_STAGE(20); W_READ(12); W_STAGE(21); W_READ(13); W_STAGE(22); W_READ(14); W_READ(15); W_READ(16); W_READ(17); W_READ(18);
        W_STAGE(23); W_READ(19); W_STAGE(24); W_READ(20); W_STAGE(25); W_READ(21); W_STAGE(26); W_READ(22); W_READ(23); W_READ(24); W_READ(25); W_READ(26);
    }
    const int idx = (lane & 1) ? 0xEEEE : 0x4444;
    const rsrc_t rX = mk_rsrc(a.xr + get_par(a) * 8192, X_BYTES);
    const rsrc_t rK = mk_rsrc(ws + WS_K + (size_t)h * S_ * E_ * 2, S_ * E_ * 2), rV = mk_rsrc(ws + WS_VT + (size_t)h * E_ * VT_LD * 2, E_ * VT_LD * 2);
    const int q4 = c >> 2, p4 = c & 3;
    LAS unsigned char* qkw = lds + HL_Q + (32 * w + c) * 16 + 4 * g;
    LAS unsigned char* qr = lds + HL_Q + (8 * g + q4) * 16 + (p4 & 1) * 8;
    LAS unsigned char* kr = lds + HL_K + (8 * g + q4) * 16 + (p4 >> 1) * 8;
    const int kxr = HL_KX + c * 256;
    int kxsrc[2];
#pragma unroll
    for (int p = 0; p < 2; ++p) { const int key = 8 * w + 4 * p + g; kxsrc[p] = key * E_ + ((c ^ (key & 15)) * 8); }
    int ooff;
    {
        const int el = 32 * w + ((g & 1) << 4) + ((g >> 1) << 3);
        const int win = el >> 6, gg = (el >> 4) & 3, hf = (el >> 3) & 1;
        const int r = 4 * (c & 3) + 2 * win + hf;
        const int p = h * 2 + ((c >> 2) & 1);
        ooff = c < 8 ? X_O + (p >> 3) * 16384 + (p & 7) * 1024 + (16 * gg + r) * 16 : 0x7F000000;
    }
    const float ninf = (g & 1) ? -INFINITY : 0.f;
    const int hoff = (w < 2 && lane == 0) ? X_HINT + w * 8192 + h * 256 : OOB;
    unsigned nfail = 0, lead = (unsigned)a.lead_h;
    bool fail = false;
    TM(1);
    CS_DECL;
    for (int s = 0; s < NSEG; ++s) {
        if ((s & 3) == 0) (void)wave_poll_ge(a, ctl_word(a, C_KV + ((s >> 2) * H_ + h) * 16), 2u);
        v4u vx[2], kxr_[2];
        LAS unsigned char* kxb = lds + HL_KX + (s & 1) * 8192;
#pragma unroll
        for (int p = 0; p < 2; ++p) kxr_[p] = __builtin_amdgcn_raw_buffer_load_b128(rK, (s * 32 * E_ + kxsrc[p]) * 2, 0, AUX_SC1);
#pragma unroll
        for (int t = 0; t < 2; ++t) vx[t] = __builtin_amdgcn_raw_buffer_load_b128(rV, ((32 * w + 16 * t + c) * VT_LD + s * 32 + 8 * g) * 2, 0, AUX_SC1);
#define KX_TO_LDS() { *(LAS v4u*)(kxb + (2 * w) * 1024 + lane * 16) = kxr_[0]; *(LAS v4u*)(kxb + (2 * w + 1) * 1024 + lane * 16) = kxr_[1]; }
        bf16x16 v1f[4];
#define LDV1(slot, f) { const v4u lo_ = *(LAS v4u*)(wv1 + (f) * 2048), hi_ = *(LAS v4u*)(wv1 + (f) * 2048 + 1024); \
                        v1f[slot] = __builtin_bit_cast(bf16x16, (v8u{lo_[0], lo_[1], lo_[2], lo_[3], hi_[0], hi_[1], hi_[2], hi_[3]})); }
#define SB() __builtin_amdgcn_sched_barrier(0)
#define QK4(f) { SMF_A(acc[0], af[f], WA[0][f], idx); SMF_A(acc[1], af[f], WA[1][f], idx); SMF_A(acc[2], af[f], WA[2][f], idx); SMF_A(acc[3], af[f], WA[3][f], idx); }
        LDV1(0, 0); LDV1(1, 1); LDV1(2, 2); LDV1(3, 3);
        v4u af[9];
        f32x4 acc[4], av[2];
        unsigned pq[2], pk[2];
        {
            unsigned spins = 0;
            CS(0);
            if (s > 0) { if (!fail && !hint_wait(rX, w, lane, (unsigned)s)) fail = true; __builtin_amdgcn_s_barrier(); }
            CS(1);
            KX_TO_LDS();
            if (s > 0) delay_from(cyc(), lead);
            CS(2);
            const int base = X_MEM + mem_slot(s) * X_MEM_SLOT + (w % NREP) * X_MEM_REP + lane * 16;
#define QK4S(a_, w0, w1, w2, w3) "v_smfmac_f32_16x16x64_bf16 %0, " a_ ", " w0 ", %4\n\tv_smfmac_f32_16x16x64_bf16 %1, " a_ ", " w1 ", %4\n\t" \
                                 "v_smfmac_f32_16x16x64_bf16 %2, " a_ ", " w2 ", %4\n\tv_smfmac_f32_16x16x64_bf16 %3, " a_ ", " w3 ", %4\n\t"
            float qa0, qa1;
            while (true) {
                if (s == 0) {
#pragma unroll
                    for (int f = 0; f < 9; ++f) af[f] = *(const v4u*)(a.mem0 + (c >> 1) * D_ + 64 * f + 16 * g + 8 * (c & 1));
                    asm volatile("s_waitcnt vmcnt(0)" : "+v"(af[0]), "+v"(af[1]), "+v"(af[2]), "+v"(af[3]), "+v"(af[4]), "+v"(af[5]), "+v"(af[6]), "+v"(af[7]), "+v"(af[8]) :: "memory");
#pragma unroll
                    for (int T = 0; T < 4; ++T) acc[T] = zero4();
                    av[0] = zero4(); av[1] = zero4();
                } else {
                    while (true) {
                        asm volatile(
                            "buffer_load_dwordx4 %0, %9, %10, 0 offen sc1\n\tbuffer_load_dwordx4 %1, %9, %10, 0 offen offset:1024 sc1\n\t"
                            "buffer_load_dwordx4 %2, %9, %10, 0 offen offset:2048 sc1\n\tbuffer_load_dwordx4 %3, %9, %10, 0 offen offset:3072 sc1\n\t"
                            "buffer_load_dwordx4 %4, %9, %10, %11 offen sc1\n\tbuffer_load_dwordx4 %5, %9, %10, %11 offen offset:1024 sc1\n\t"
                            "buffer_load_dwordx4 %6, %9, %10, %11 offen offset:2048 sc1\n\tbuffer_load_dwordx4 %7, %9, %10, %11 offen offset:3072 sc1\n\t"
                            "buffer_load_dwordx4 %8, %9, %10, %12 offen sc1"
                            : "=&v"(af[0]), "=&v"(af[1]), "=&v"(af[2]), "=&v"(af[3]), "=&v"(af[4]), "=&v"(af[5]), "=&v"(af[6]), "=&v"(af[7]), "=&v"(af[8])
                            : "v"(base), "s"(rX), "s"(4096), "s"(16384) : "memory");
#pragma unroll
                        for (int T = 0; T < 4; ++T) acc[T] = zero4();
                        av[0] = zero4(); av[1] = zero4();
                        asm volatile("s_waitcnt vmcnt(8)" : "+v"(af[0]), "+v"(acc[0]), "+v"(acc[1]), "+v"(acc[2]), "+v"(acc[3]), "+v"(av[0]), "+v"(av[1]) :: "memory");
                        if (!__any(vmax(0u, af[0]) == ~0u)) break;
                        ++nfail;
                        if (++spins > SPIN_CAP) { fail = true; break; }
                    }
                }
                CS(3);
                asm volatile(QK4S("%5", "%10", "%11", "%12", "%13") "s_waitcnt vmcnt(7)\n\t" QK4S("%6", "%14", "%15", "%16", "%17") "s_waitcnt vmcnt(6)\n\t" QK4S("%7", "%18", "%19", "%20", "%21")
                             "s_waitcnt vmcnt(5)\n\t" QK4S("%8", "%22", "%23", "%24", "%25") "s_waitcnt vmcnt(4)\n\t" QK4S("%9", "%26", "%27", "%28", "%29")
                             : "+v"(acc[0]), "+v"(acc[1]), "+v"(acc[2]), "+v"(acc[3])
                             : "v"(idx), "v"(af[0]), "v"(af[1]), "v"(af[2]), "v"(af[3]), "v"(af[4]),
                               "a"(WA[0][0]), "a"(WA[1][0]), "a"(WA[2][0]), "a"(WA[3][0]), "a"(WA[0][1]), "a"(WA[1][1]), "a"(WA[2][1]), "a"(WA[3][1]),
                               "a"(WA[0][2]), "a"(WA[1][2]), "a"(WA[2][2]), "a"(WA[3][2]), "a"(WA[0][3]), "a"(WA[1][3]), "a"(WA[2][3]), "a"(WA[3][3]),
                               "a"(WA[0][4]), "a"(WA[1][4]), "a"(WA[2][4]), "a"(WA[3][4]) : "memory");
                asm volatile("s_waitcnt vmcnt(3)\n\t" QK4S("%5", "%9", "%10", "%11", "%12") "s_waitcnt vmcnt(2)\n\t" QK4S("%6", "%13", "%14", "%15", "%16") "s_waitcnt vmcnt(1)\n\t" QK4S("%7", "%17", "%18", "%19", "%20")
                             "s_waitcnt vmcnt(0)\n\t" QK4S("%8", "%21", "%22", "%23", "%24")
                             : "+v"(acc[0]), "+v"(acc[1]), "+v"(acc[2]), "+v"(acc[3])
                             : "v"(idx), "v"(af[5]), "v"(af[6]), "v"(af[7]), "v"(af[8]),
                               "a"(WA[0][5]), "a"(WA[1][5]), "a"(WA[2][5]), "a"(WA[3][5]), "a"(WA[0][6]), "a"(WA[1][6]), "a"(WA[2][6]), "a"(WA[3][6]),
                               "a"(WA[0][7]), "a"(WA[1][7]), "a"(WA[2][7]), "a"(WA[3][7]), "v"(WB[0]), "v"(WB[1]), "v"(WB[2]), "v"(WB[3]) : "memory");
                asm volatile("v_smfmac_f32_16x16x64_bf16 %0, %4, %5, %7\n\tv_smfmac_f32_16x16x64_bf16 %1, %4, %6, %7\n\t"
                             "v_add_f32_e32 %2, %8, %9\n\tv_add_f32_e32 %3, %10, %11"
                             : "+v"(av[0]), "+v"(av[1]), "=&v"(qa0), "=&v"(qa1)
                             : "v"(af[0]), "v"(WV[0]), "v"(v1f[0]), "v"(idx), "v"(acc[0][0]), "v"(acc[0][1]), "v"(acc[0][2]), "v"(acc[0][3]));
                if (s == 0 || fail || !__any(__builtin_isunordered(qa0, qa1))) break;
                ++nfail;
                if (++spins > SPIN_CAP) { fail = true; break; }
            }
            if (s > 0) lead = pace(lead, (unsigned)a.lead_h, spins > 0);
            CS(4);
            float x0, x1, x2, x3, x4, x5;
            asm volatile("v_add_f32_e32 %4, %12, %13\n\tv_add_f32_e32 %5, %14, %15\n\tv_cvt_pk_bf16_f32 %0, %10, %11\n\t"
                         "v_add_f32_e32 %6, %16, %17\n\tv_add_f32_e32 %7, %18, %19\n\tv_cvt_pk_bf16_f32 %1, %4, %5\n\t"
                         "v_add_f32_e32 %8, %20, %21\n\tv_add_f32_e32 %9, %22, %23\n\tv_cvt_pk_bf16_f32 %2, %6, %7\n\tv_cvt_pk_bf16_f32 %3, %8, %9"
                         : "=&v"(pq[0]), "=&v"(pq[1]), "=&v"(pk[0]), "=&v"(pk[1]), "=&v"(x0), "=&v"(x1), "=&v"(x2), "=&v"(x3), "=&v"(x4), "=&v"(x5)
                         : "v"(qa0), "v"(qa1), "v"(acc[1][0]), "v"(acc[1][1]), "v"(acc[1][2]), "v"(acc[1][3]), "v"(acc[2][0]), "v"(acc[2][1]), "v"(acc[2][2]), "v"(acc[2][3]),
                           "v"(acc[3][0]), "v"(acc[3][1]), "v"(acc[3][2]), "v"(acc[3][3]));
        }
        *(LAS unsigned*)(qkw) = pq[0]; *(LAS unsigned*)(qkw + 256) = pq[1];
        *(LAS unsigned*)(qkw + 2048) = pk[0]; *(LAS unsigned*)(qkw + 2048 + 256) = pk[1];
        SB();
        asm volatile("v_smfmac_f32_16x16x64_bf16 %0, %3, %6, %2\n\tv_smfmac_f32_16x16x64_bf16 %1, %3, %9, %2\n\tv_smfmac_f32_16x16x64_bf16 %0, %4, %7, %2\n\t"
                     "v_smfmac_f32_16x16x64_bf16 %1, %4, %10, %2\n\tv_smfmac_f32_16x16x64_bf16 %0, %5, %8, %2\n\tv_smfmac_f32_16x16x64_bf16 %1, %5, %11, %2"
                     : "+v"(av[0]), "+v"(av[1]) : "v"(idx), "v"(af[1]), "v"(af[2]), "v"(af[3]), "v"(WV[1]), "v"(WV[2]), "v"(WV[3]), "v"(v1f[1]), "v"(v1f[2]), "v"(v1f[3]));
        SB();
        LDV1(0, 4); LDV1(1, 5); LDV1(2, 6);
        SB();
        asm volatile("s_waitcnt lgkmcnt(6)\n\ts_barrier" ::: "memory");
        __builtin_amdgcn_raw_buffer_store_b32((unsigned)(s + 1), rX, hoff, 0, AUX_SC1);
        CS(5);
        bf16x8 qf[4], kmf[4], kx[2][4];
#define LDS_SC(ks) { qf[ks] = tr2(qr + (ks) * 512, qr + (ks) * 512 + 64); \
                     kx[0][ks] = *(LAS bf16x8*)(lds + (s & 1) * 8192 + kxr + (((4 * (ks) + g) ^ c) << 4)); \
                     kx[1][ks] = *(LAS bf16x8*)(lds + (s & 1) * 8192 + kxr + 4096 + (((4 * (ks) + g) ^ c) << 4)); \
                     kmf[ks] = tr2(kr + (ks) * 512, kr + (ks) * 512 + 64); }
        LDS_SC(0); LDS_SC(1); LDV1(3, 7);
        SB();
        asm volatile("v_smfmac_f32_16x16x64_bf16 %0, %3, %6, %2\n\tv_smfmac_f32_16x16x64_bf16 %1, %3, %9, %2\n\tv_smfmac_f32_16x16x64_bf16 %0, %4, %7, %2\n\t"
                     "v_smfmac_f32_16x16x64_bf16 %1, %4, %10, %2\n\tv_smfmac_f32_16x16x64_bf16 %0, %5, %8, %2\n\tv_smfmac_f32_16x16x64_bf16 %1, %5, %11, %2"
                     : "+v"(av[0]), "+v"(av[1]) : "v"(idx), "v"(af[4]), "v"(af[5]), "v"(af[6]), "v"(WV[4]), "v"(WV[5]), "v"(WV[6]), "v"(v1f[0]), "v"(v1f[1]), "v"(v1f[2]));
        f32x4 sp[3];
        { f32x2 c0, c1; const f32x2 ni = {ninf, ninf}; asm volatile("v_mov_b64 %0, %2\n\tv_mov_b64 %1, %2" : "=&v"(c0), "=&v"(c1) : "v"(ni)); sp[0] = f32x4{c0[0], c0[1], c1[0], c1[1]}; }
        SB();
        LDV1(0, 8); LDS_SC(2);
        SB();
        asm volatile("v_mfma_f32_16x16x32_bf16 %1, %3, %6, 0\n\tv_mfma_f32_16x16x32_bf16 %2, %4, %6, 0\n\tv_mfma_f32_16x16x32_bf16 %0, %5, %6, %0"
                     : "+v"(sp[0]), "=&v"(sp[1]), "=&v"(sp[2]) : "v"(kx[0][0]), "v"(kx[1][0]), "v"(kmf[0]), "v"(qf[0]));
        SB();
        LDS_SC(3);
        SB();
        asm volatile("v_mfma_f32_16x16x32_bf16 %1, %6, %9, %1\n\tv_mfma_f32_16x16x32_bf16 %2, %7, %9, %2\n\tv_mfma_f32_16x16x32_bf16 %0, %8, %9, %0\n\t"
                     "v_smfmac_f32_16x16x64_bf16 %3, %10, %12, %5\n\tv_smfmac_f32_16x16x64_bf16 %4, %10, %14, %5\n\tv_smfmac_f32_16x16x64_bf16 %3, %11, %13, %5"
                     : "+v"(sp[0]), "+v"(sp[1]), "+v"(sp[2]), "+v"(av[0]), "+v"(av[1])
                     : "v"(idx), "v"(kx[0][1]), "v"(kx[1][1]), "v"(kmf[1]), "v"(qf[1]), "v"(af[7]), "v"(af[8]), "v"(WV[7]), "v"(WV[8]), "v"(v1f[3]));
        asm volatile("v_mfma_f32_16x16x32_bf16 %1, %3, %6, %1\n\tv_mfma_f32_16x16x32_bf16 %2, %4, %6, %2\n\tv_mfma_f32_16x16x32_bf16 %0, %5, %6, %0"
                     : "+v"(sp[0]), "+v"(sp[1]), "+v"(sp[2]) : "v"(kx[0][2]), "v"(kx[1][2]), "v"(kmf[2]), "v"(qf[2]));
        asm volatile("v_mfma_f32_16x16x32_bf16 %1, %5, %8, %1\n\tv_mfma_f32_16x16x32_bf16 %2, %6, %8, %2\n\tv_mfma_f32_16x16x32_bf16 %0, %7, %8, %0\n\t"
                     "v_smfmac_f32_16x16x64_bf16 %3, %9, %10, %4\n\ts_nop 3"
                     : "+v"(sp[0]), "+v"(sp[1]), "+v"(sp[2]), "+v"(av[1]) : "v"(idx), "v"(kx[0][3]), "v"(kx[1][3]), "v"(kmf[3]), "v"(qf[3]), "v"(af[8]), "v"(v1f[0]));
        CS(6);
        unsigned pv[2];
        {
            float m, t, t2;
            asm volatile(
                "v_max3_f32 %0, %3, %4, %5\n\tv_max3_f32 %1, %6, %7, %8\n\tv_max3_f32 %2, %9, %10, %11\n\t"
                "v_max3_f32 %0, %0, %12, %13\n\tv_max3_f32 %1, %1, %2, %14\n\tv_max_f32_e32 %0, %0, %1\n\tv_mov_b32_e32 %1, %0\n\ts_nop 1\n\t"
                "v_permlane16_swap_b32_e32 %0, %1\n\tv_max_f32_e32 %0, %0, %1\n\tv_mov_b32_e32 %1, %0\n\ts_nop 1\n\t"
                "v_permlane32_swap_b32_e32 %0, %1\n\tv_max_f32_e32 %0, %0, %1\n\tv_mul_f32_e32 %0, %15, %0"
                : "=&v"(m), "=&v"(t), "=&v"(t2)
                : "v"(sp[1][0]), "v"(sp[1][1]), "v"(sp[1][2]), "v"(sp[1][3]), "v"(sp[2][0]), "v"(sp[2][1]), "v"(sp[2][2]), "v"(sp[2][3]), "v"(sp[0][0]),
                  "v"(sp[0][1]), "v"(sp[0][2]), "v"(sp[0][3]), "s"(SCL));
            const f32x2 ms = {m, m};
            const f32x2 scl2 = {SCL, SCL};
            f32x2 e[6];
            PKFMA(e[0], (f32x2{sp[0][0], sp[0][1]}), scl2, ms); PKFMA(e[1], (f32x2{sp[0][2], sp[0][3]}), scl2, ms);
            PKFMA(e[2], (f32x2{sp[1][0], sp[1][1]}), scl2, ms); PKFMA(e[3], (f32x2{sp[1][2], sp[1][3]}), scl2, ms);
            PKFMA(e[4], (f32x2{sp[2][0], sp[2][1]}), scl2, ms); PKFMA(e[5], (f32x2{sp[2][2], sp[2][3]}), scl2, ms);
            float x[12];
#pragma unroll
            for (int r = 0; r < 6; ++r) { x[2 * r] = e[r][0]; x[2 * r + 1] = e[r][1]; }
            asm volatile("v_exp_f32_e32 %0, %0\n\tv_exp_f32_e32 %1, %1\n\tv_exp_f32_e32 %2, %2\n\tv_exp_f32_e32 %3, %3\n\tv_exp_f32_e32 %4, %4\n\tv_exp_f32_e32 %5, %5\n\t"
                         "v_exp_f32_e32 %6, %6\n\tv_exp_f32_e32 %7, %7\n\tv_exp_f32_e32 %8, %8\n\tv_exp_f32_e32 %9, %9\n\tv_exp_f32_e32 %10, %10\n\tv_exp_f32_e32 %11, %11"
                         : "+v"(x[4]), "+v"(x[5]), "+v"(x[6]), "+v"(x[7]), "+v"(x[8]), "+v"(x[9]), "+v"(x[10]), "+v"(x[11]), "+v"(x[0]), "+v"(x[1]), "+v"(x[2]), "+v"(x[3]));
            v4u pp; v2u pm;
            VCVT(pp[0], x[4], x[5]); VCVT(pp[1], x[6], x[7]); VCVT(pp[2], x[8], x[9]); VCVT(pp[3], x[10], x[11]);
            VCVT(pm[0], x[0], x[1]); VCVT(pm[1], x[2], x[3]);
            v4u ones; { unsigned o_; asm volatile("v_mov_b32_e32 %0, 0x3f803f80" : "=v"(o_)); ones = v4u{o_, o_, o_, o_}; }
            asm volatile("s_nop 1" : "+v"(pp), "+v"(pm), "+v"(ones));
            f32x4 o0, o1, zz;
            MFMA32_0(o0, vx[0], pp); MFMA32_0(o1, vx[1], pp); MFMA32_0(zz, ones, pp);
            asm volatile("" : "+v"(av[0]), "+v"(av[1]));
            float a0, a1, a2, a3;
            VADD(a0, av[0][0], av[0][1]); VADD(a1, av[0][2], av[0][3]); VADD(a2, av[1][0], av[1][1]); VADD(a3, av[1][2], av[1][3]);
            VCVT(pv[0], a0, a1); VCVT(pv[1], a2, a3);
            unsigned u0, u1, w0, w1;
            VMOV(u0, pv[0]); VMOV(u1, pv[0]); VMOV(w0, pv[1]); VMOV(w1, pv[1]);
            NOP(0);
            SWAP16(u0, u1); SWAP16(w0, w1);
            v2u vm0 = {u0, u1}, vm1 = {w0, w1};
            asm volatile("s_nop 1" : "+v"(vm0), "+v"(vm1), "+v"(o0), "+v"(o1), "+v"(zz));
            MFMA16_ACC(zz, (v2u{ones[0], ones[1]}), pm); MFMA16_ACC(o0, vm0, pm); MFMA16_ACC(o1, vm1, pm);
            asm volatile("s_nop 5" : "+v"(zz));
            float rz; asm volatile("v_rcp_f32_e32 %0, %1" : "=v"(rz) : "v"(zz[0]));
            asm volatile("s_nop 1" : "+v"(o0), "+v"(o1), "+v"(rz));
            const f32x2 rz2 = {rz, rz};
            f32x2 n0, n1, n2, n3;
            PKMUL(n0, (f32x2{o0[0], o0[1]}), rz2); PKMUL(n1, (f32x2{o0[2], o0[3]}), rz2); PKMUL(n2, (f32x2{o1[0], o1[1]}), rz2); PKMUL(n3, (f32x2{o1[2], o1[3]}), rz2);
            unsigned d0, d1, e0_, e1;
            VCVT(d0, n0[0], n0[1]); VCVT(d1, n1[0], n1[1]); VCVT(e0_, n2[0], n2[1]); VCVT(e1, n3[0], n3[1]);
            NOP(0);
            SWAP16(d0, e0_); SWAP16(d1, e1);
            NOP(0);
            v4u val = {nosent(d0), nosent(d1), nosent(e0_), nosent(e1)};
            CS(7);
            __builtin_amdgcn_raw_buffer_store_b128(val, rX, s != NSEG - 1 ? ooff + o_slot(s) * X_O_SLOT : OOB, 0, AUX_SC1);
        }
        __builtin_amdgcn_raw_buffer_store_b128(poison4(), rX, s > 0 ? ooff + o_slot(s - 1) * X_O_SLOT : OOB, 0, AUX_SC1);
        {
            auto a0 = __builtin_amdgcn_permlane32_swap(pk[0], pk[1], false, false);
            auto a1 = __builtin_amdgcn_permlane32_swap(pv[0], pv[1], false, false);
            auto b0 = __builtin_amdgcn_permlane16_swap(a0[0], a1[0], false, false);
            auto b1 = __builtin_amdgcn_permlane16_swap(a0[1], a1[1], false, false);
            v4u val = {b0[0], b0[1], b1[0], b1[1]};
            const int off = (s * H_ + h) * 4096 + (g & 1) * 2048 + (32 * w + 16 * (g >> 1) + c) * 16;
            __builtin_amdgcn_raw_buffer_store_b128(val, rKVM, off, 0, AUX_SC1);
        }
    }
    if (h == 0) CS_OUT(8192);
    asm volatile("s_waitcnt vmcnt(0)\n\ts_barrier" ::: "memory");
    __builtin_amdgcn_raw_buffer_store_b32((unsigned)(NSEG + 1), rX, hoff, 0, AUX_SC1);
    if (threadIdx.x == 0) (void)__hip_atomic_fetch_add(ctl_word(a, C_CHAIN), 1u, RLX_AGENT);
    TM(2);
    if (fail && threadIdx.x == 0) __hip_atomic_store(ctl, 1u, RLX_AGENT);
}

constexpr int YL_RED = 0;
constexpr int YL_STAGE = 4096;
DI void y_role(const Params& a, int tp, int rho, LAS unsigned char* lds) {
    const int lane = lane_id(), w = wave_id(), c = lane & 15, g = lane >> 4;
    unsigned char* ws = a.ws;
    gu32* ctl = (gu32*)(ws + WS_CTL);
    bf16x16 W[16][2];
    {
        LAS unsigned char* st = lds + YL_STAGE + w * 32768;
        const rsrc_t rW = mk_rsrc(a.Wo + (size_t)(1024 * w) * D_ + 32 * tp, 1024 * D_ * 2);
        int vo[4];
#pragma unroll
        for (int p = 0; p < 4; ++p) vo[p] = ((16 * p + (lane >> 2)) * D_ + (lane & 3) * 8) * 2;
        auto stage = [&](int win, int slot) {
#pragma unroll
            for (int p = 0; p < 4; ++p) dma16<false>((unsigned)(size_t)st + slot * 4096 + p * 1024, vo[p], rW, win * 64 * D_ * 2);
        };
#pragma unroll
        for (int i = 0; i < 7; ++i) stage(i, i);
        const int gb = lane >> 4, q = (lane & 15) >> 2, p = lane & 3;
#define Y_READ(n) { if ((n) + 7 < 16) stage((n) + 7, ((n) + 7) & 7); wait_vm<4 * ((n) + 7 < 16 ? 7 : 15 - (n))>(); \
            _Pragma("unroll") for (int T = 0; T < 2; ++T) { s16x4 r[4]; \
                _Pragma("unroll") for (int m = 0; m < 4; ++m) { const int d0 = 32 * (m >> 1) + 16 * (gb >> 1) + 4 * (gb & 1) + 2 * (m & 1); const int k = d0 + (q & 1) + 8 * (q >> 1); \
                    r[m] = __builtin_amdgcn_ds_read_tr16_b64_v4i16((LAS s16x4*)(st + ((n) & 7) * 4096 + k * 64 + (16 * T + 4 * p) * 2)); } \
                typedef short s16x16 __attribute__((ext_vector_type(16))); \
                s16x16 v = {r[0][0], r[0][1], r[0][2], r[0][3], r[1][0], r[1][1], r[1][2], r[1][3], r[2][0], r[2][1], r[2][2], r[2][3], r[3][0], r[3][1], r[3][2], r[3][3]}; \
                W[n][T] = __builtin_bit_cast(bf16x16, v); asm volatile("" : "+a"(W[n][T])); } \
            asm volatile("s_waitcnt lgkmcnt(0)" ::: "memory"); }
        Y_READ(0); Y_READ(1); Y_READ(2); Y_READ(3); Y_READ(4); Y_READ(5); Y_READ(6); Y_READ(7);
        Y_READ(8); Y_READ(9); Y_READ(10); Y_READ(11); Y_READ(12); Y_READ(13); Y_READ(14); Y_READ(15);
    }
    const int idx = (lane & 1) ? 0xEEEE : 0x4444;
    const rsrc_t rX = mk_rsrc(a.xr + get_par(a) * 8192, X_BYTES);
    unsigned nfail = 0, lead = (unsigned)a.lead_y;
    bool fail = false;
    CS_DECL;
    TM(1);
    for (int s = 0; s < NSEG - 1; ++s) {
        const int base = X_O + o_slot(s) * X_O_SLOT + 2 * w * 16384 + rho * 1024 + lane * 16;
        unsigned spins = 0;
        CS(0);
        if (!fail && !hint_wait(rX, w, lane, (unsigned)(s + 1))) fail = true;
        CS(1); delay_from(cyc(), lead); CS(2);
        f32x4 D1[2], D2[2];
        float y0, y1;
        while (true) {
            v4u v[8];
            while (true) {
                asm volatile(
                    "buffer_load_dwordx4 %0, %8, %9, 0 offen sc1\n\tbuffer_load_dwordx4 %1, %8, %9, 0 offen offset:2048 sc1\n\t"
                    "buffer_load_dwordx4 %2, %8, %9, %10 offen sc1\n\tbuffer_load_dwordx4 %3, %8, %9, %10 offen offset:2048 sc1\n\t"
                    "buffer_load_dwordx4 %4, %8, %9, %11 offen sc1\n\tbuffer_load_dwordx4 %5, %8, %9, %11 offen offset:2048 sc1\n\t"
                    "buffer_load_dwordx4 %6, %8, %9, %12 offen sc1\n\tbuffer_load_dwordx4 %7, %8, %9, %12 offen offset:2048 sc1"
                    : "=&v"(v[0]), "=&v"(v[1]), "=&v"(v[2]), "=&v"(v[3]), "=&v"(v[4]), "=&v"(v[5]), "=&v"(v[6]), "=&v"(v[7])
                    : "v"(base), "s"(rX), "s"(4096), "s"(16384), "s"(16384 + 4096) : "memory");
                D1[0] = zero4(); D1[1] = zero4(); D2[0] = zero4(); D2[1] = zero4();
                asm volatile("s_waitcnt vmcnt(7)" : "+v"(v[0]), "+v"(D1[0]), "+v"(D1[1]), "+v"(D2[0]), "+v"(D2[1]) :: "memory");
                if (!__any(vmax(0u, v[0]) == ~0u)) break;
                ++nfail;
                if (++spins > SPIN_CAP) { fail = true; break; }
            }
            CS(3);
#define YP4(a_, w0, w1, w2, w3) "v_smfmac_f32_16x16x64_bf16 %0, " a_ ", " w0 ", %4\n\tv_smfmac_f32_16x16x64_bf16 %1, " a_ ", " w1 ", %4\n\t" \
                                "v_smfmac_f32_16x16x64_bf16 %2, " a_ ", " w2 ", %4\n\tv_smfmac_f32_16x16x64_bf16 %3, " a_ ", " w3 ", %4\n\t"
            asm volatile(YP4("%5", "%9", "%10", "%11", "%12") "s_waitcnt vmcnt(6)\n\t" YP4("%6", "%13", "%14", "%15", "%16") "s_waitcnt vmcnt(5)\n\t" YP4("%7", "%17", "%18", "%19", "%20")
                         "s_waitcnt vmcnt(4)\n\t" YP4("%8", "%21", "%22", "%23", "%24")
                         : "+v"(D1[0]), "+v"(D2[0]), "+v"(D1[1]), "+v"(D2[1])
                         : "v"(idx), "v"(v[0]), "v"(v[1]), "v"(v[2]), "v"(v[3]),
                           "a"(W[0][0]), "a"(W[1][0]), "a"(W[0][1]), "a"(W[1][1]), "a"(W[2][0]), "a"(W[3][0]), "a"(W[2][1]), "a"(W[3][1]),
                           "a"(W[4][0]), "a"(W[5][0]), "a"(W[4][1]), "a"(W[5][1]), "a"(W[6][0]), "a"(W[7][0]), "a"(W[6][1]), "a"(W[7][1]) : "memory");
            asm volatile("s_waitcnt vmcnt(3)\n\t" YP4("%5", "%9", "%10", "%11", "%12") "s_waitcnt vmcnt(2)\n\t" YP4("%6", "%13", "%14", "%15", "%16") "s_waitcnt vmcnt(1)\n\t" YP4("%7", "%17", "%18", "%19", "%20")
                         "s_waitcnt vmcnt(0)\n\t" YP4("%8", "%21", "%22", "%23", "%24") "s_nop 7"
                         : "+v"(D1[0]), "+v"(D2[0]), "+v"(D1[1]), "+v"(D2[1])
                         : "v"(idx), "v"(v[4]), "v"(v[5]), "v"(v[6]), "v"(v[7]),
                           "a"(W[8][0]), "a"(W[9][0]), "a"(W[8][1]), "a"(W[9][1]), "a"(W[10][0]), "a"(W[11][0]), "a"(W[10][1]), "a"(W[11][1]),
                           "a"(W[12][0]), "a"(W[13][0]), "a"(W[12][1]), "a"(W[13][1]), "a"(W[14][0]), "a"(W[15][0]), "a"(W[14][1]), "a"(W[15][1]) : "memory");
            y0 = fadd1(fadd1(D1[0][0], D1[0][1]), fadd1(D2[0][2], D2[0][3])); y1 = fadd1(fadd1(D1[1][0], D1[1][1]), fadd1(D2[1][2], D2[1][3]));
            if (fail || !__any(__builtin_isunordered(y0, y1))) break;
            ++nfail;
            if (++spins > SPIN_CAP) { fail = true; break; }
        }
        *(LAS f32x2*)(lds + YL_RED + (w * 64 + lane) * 8) = f32x2{y0, y1};
        lead = pace(lead, (unsigned)a.lead_y, spins > 0);
        CS(4);
        asm volatile("s_waitcnt lgkmcnt(0)\n\ts_barrier" ::: "memory");
        CS(5);
        if (w == 0) {
            const f32x2 t = (*(LAS f32x2*)(lds + YL_RED + lane * 8) + *(LAS f32x2*)(lds + YL_RED + (64 + lane) * 8)) + (*(LAS f32x2*)(lds + YL_RED + (128 + lane) * 8) + *(LAS f32x2*)(lds + YL_RED + (192 + lane) * 8));
            const float x0 = t.x, x1 = t.y;
            const float y0 = u2f(__builtin_amdgcn_update_dpp(0u, f2u(x0), 0xB1, 0xF, 0xF, false)), y1 = u2f(__builtin_amdgcn_update_dpp(0u, f2u(x1), 0xB1, 0xF, 0xF, false));
            const unsigned dw = nosent((c & 1) ? pk2(y1, x1) : pk2(x0, y0));
            const unsigned d1 = __builtin_amdgcn_update_dpp(0u, dw, 0x102, 0xF, 0xF, false), d2 = __builtin_amdgcn_update_dpp(0u, dw, 0x104, 0xF, 0xF, false), d3 = __builtin_amdgcn_update_dpp(0u, dw, 0x106, 0xF, 0xF, false);
            v4u val = {dw, d1, d2, d3};
            const int fo = (tp >> 1) < 8 ? (tp >> 1) * 1024 : 16384;
            const int off = (c & 6) == 0 ? X_MEM + fo + (16 * (2 * (tp & 1) + (c & 1)) + 8 * rho + 2 * g + (c >> 3)) * 16 : 0x7F000000;
#pragma unroll
            for (int q = 0; q < NREP; ++q) __builtin_amdgcn_raw_buffer_store_b128(val, rX, mem_slot(s + 1) * X_MEM_SLOT + q * X_MEM_REP + off, 0, AUX_SC1);
            const v4u pz = poison4();
#pragma unroll
            for (int q = 0; q < NREP; ++q) __builtin_amdgcn_raw_buffer_store_b128(pz, rX, s > 0 ? mem_slot(s) * X_MEM_SLOT + q * X_MEM_REP + off : OOB, 0, AUX_SC1);
        }
        CS(6);
    }
    if (tp == 0 && rho == 0) CS_OUT(8192 + 64);
    TM(2);
    if (fail && threadIdx.x == 0) __hip_atomic_store(ctl, 1u, RLX_AGENT);
}

DI void tail_spatial(const Params& a, int mt, int nt, LAS unsigned char* lds) {
    const int lane = lane_id(), wave = wave_id(), wm = wave >> 1, wn = wave & 1;
    GemmStream<128, 256, 3, false, true> gs; gs.init(lds, S_, FH);
    const rsrc_t rA = mk_rsrc(a.Wsp + (size_t)mt * 128 * S_, 128 * S_ * 2), rB = mk_rsrc(a.ws + WS_Z2N + nt * 256 * 2, S_ * FH * 2);
    LAS unsigned* flag = (LAS unsigned*)(lds + GP<128, 256, 3>::LDS);
    if (wave == 0) {
        const unsigned v = __hip_atomic_load(ctl_word(a, C_ZN + (lane & 15) * 16), RLX_AGENT);
        const unsigned long long done = __ballot(v >= 8u) & 0xFFFFull;
        if (lane == 0) *flag = (unsigned)__builtin_ctzll(~done);
    }
    __syncthreads();
    int ready = __builtin_amdgcn_readfirstlane((int)*flag);
    auto gate = [&](int tn) {
        if ((tn >> 1) >= ready) {
            if (threadIdx.x == 0) { (void)poll_ge(a, ctl_word(a, C_ZN + (tn >> 1) * 16), 8u); __builtin_amdgcn_fence(__ATOMIC_ACQUIRE, "wavefront"); }
            asm volatile("s_barrier" ::: "memory");
            ready = (tn >> 1) + 1;
        }
    };
    gs.primeA(rA);
    gate(0);
    gs.primeB(rB);
    f32x16 acc[2][4];
    gs.template tile<0, true, 2>(rA, rB, rA, rB, false, 32, acc, gate);
#pragma unroll
    for (int i = 0; i < 2; ++i) {
        const int row = wm * 64 + i * 32 + (lane & 31);
        const float bb = bf2f(a.bsp[mt * 128 + row]);
#pragma unroll
        for (int j = 0; j < 4; ++j)
#pragma unroll
            for (int rg = 0; rg < 4; ++rg) {
                const int col = wn * 128 + j * 32 + 8 * rg + 4 * (lane >> 5);
                const v2u o = {pk2(rbf(acc[i][j][4 * rg]) + bb, rbf(acc[i][j][4 * rg + 1]) + bb), pk2(rbf(acc[i][j][4 * rg + 2]) + bb, rbf(acc[i][j][4 * rg + 3]) + bb)};
                *(LAS v2u*)(lds + row * 512 + ((((col >> 3) ^ (row & 7)) << 4) | ((col & 4) << 1))) = o;
            }
    }
    __syncthreads();
    const rsrc_t rZ = mk_rsrc(a.ws + WS_Z1, S_ * FH * 2), rU = mk_rsrc(a.ws + WS_U, S_ * FH * 2);
#pragma unroll
    for (int hf = 0; hf < 2; ++hf) {
        v4u zv[8];
#pragma unroll
        for (int it = 0; it < 8; ++it) { const int id = (hf * 8 + it) * 256 + threadIdx.x, row = id >> 5, ch = id & 31; zv[it] = __builtin_amdgcn_raw_buffer_load_b128(rZ, ((mt * 128 + row) * FH + nt * 256 + ch * 8) * 2, 0, AUX_SC1); }
#pragma unroll
        for (int it = 0; it < 8; ++it) {
            const int id = (hf * 8 + it) * 256 + threadIdx.x, row = id >> 5, ch = id & 31;
            const v4u gv = *(LAS v4u*)(lds + row * 512 + ((ch ^ (row & 7)) << 4));
            float gf[8], zf[8]; unpack8(gv, gf); unpack8(zv[it], zf);
            const v4u o = {pk2(zf[0] * gf[0], zf[1] * gf[1]), pk2(zf[2] * gf[2], zf[3] * gf[3]), pk2(zf[4] * gf[4], zf[5] * gf[5]), pk2(zf[6] * gf[6], zf[7] * gf[7])};
            __builtin_amdgcn_raw_buffer_store_b128(o, rU, ((mt * 128 + row) * FH + nt * 256 + ch * 8) * 2, 0, AUX_SC1);
        }
    }
    wg_signal(a, C_U + mt * 16);
}
DI void tail_out(const Params& a, int mt, int nt, LAS unsigned char* lds) {
    const int lane = lane_id(), wave = wave_id(), wm = wave >> 1, wn = wave & 1;
    GemmStream<128, 64, 4, true> gs; gs.init(lds, FH, D_);
    const rsrc_t rA = mk_rsrc(a.ws + WS_U + (size_t)mt * 128 * FH * 2, 128 * FH * 2), rB = mk_rsrc(a.Wout + nt * 64, FH * D_ * 2);
    gs.primeB(rB);
    wg_wait_long(a, ctl_word(a, C_ZN + (NRB - 1) * 16), 8u, 0u);
    wg_wait<false>(a, ctl_word(a, C_U + mt * 16), 9u);
    gs.primeA(rA);
    f32x16 acc[2][1];
    gs.template tile2<true, true>(rA, rB, 36, acc);
#pragma unroll
    for (int i = 0; i < 2; ++i) {
        const int row = wm * 64 + i * 32 + (lane & 31);
#pragma unroll
        for (int rg = 0; rg < 4; ++rg) {
            const int col = wn * 32 + 8 * rg + 4 * (lane >> 5);
            const v2u o = {pk2(acc[i][0][4 * rg], acc[i][0][4 * rg + 1]), pk2(acc[i][0][4 * rg + 2], acc[i][0][4 * rg + 3])};
            *(LAS v2u*)(lds + row * 128 + ((((col >> 3) ^ (row & 7)) << 4) | ((col & 4) << 1))) = o;
        }
    }
    __syncthreads();
    const rsrc_t rX1 = mk_rsrc(a.ws + WS_X1, S_ * D_ * 2), rM = mk_rsrc(a.ws + WS_MOD, 3456 * 4);
#pragma unroll
    for (int it = 0; it < 4; ++it) {
        const int id = it * 256 + threadIdx.x, row = id >> 3, ch = id & 7;
        const v4u yv = *(LAS v4u*)(lds + row * 128 + ((ch ^ (row & 7)) << 4));
        const int n = nt * 64 + ch * 8;
        const v4u xv = __builtin_amdgcn_raw_buffer_load_b128(rX1, ((mt * 128 + row) * D_ + n) * 2, 0, AUX_SC1);
        const v4u g0 = __builtin_amdgcn_raw_buffer_load_b128(rM, (5 * D_ + n) * 4, 0, AUX_SC1), g1 = __builtin_amdgcn_raw_buffer_load_b128(rM, (5 * D_ + n + 4) * 4, 0, AUX_SC1);
        const float gb[8] = {u2f(g0.x), u2f(g0.y), u2f(g0.z), u2f(g0.w), u2f(g1.x), u2f(g1.y), u2f(g1.z), u2f(g1.w)};
        float yf[8], xf[8]; unpack8(yv, yf); unpack8(xv, xf);
        float r[8];
#pragma unroll
        for (int e = 0; e < 8; ++e) r[e] = xf[e] + rbf(gb[e] * yf[e]);
        *(v4u*)(a.out + (size_t)(mt * 128 + row) * D_ + n) = v4u{pk2(r[0], r[1]), pk2(r[2], r[3]), pk2(r[4], r[5]), pk2(r[6], r[7])};
    }
}
DI void tail_phase(const Params& a, int l, int r, LAS unsigned char* lds) {
    __syncthreads();
    if (l < 4) {
        wg_wait_long(a, ctl_word(a, C_CHAIN), 32u, 0u);
        tail_spatial(a, 4 * l + (r & 3), r >> 2, lds);
        TR(5200, blockIdx.x);
        if (r < 8) { __syncthreads(); tail_out(a, 12 + l, 1 + r, lds); }
    } else {
        const int j = r * 4 + (l - 4);
        if (j < 16) { tail_spatial(a, j, 8, lds); TR(5200, blockIdx.x); }
        else { const int x = j & 3, k = (j - 16) >> 2; if (k < 27) tail_out(a, x + 4 * (k / 9), k % 9, lds); else tail_out(a, 12 + x, 0, lds); }
    }
}

constexpr int MAIN_LDS = 160 * 1024;
static_assert(HL_END <= MAIN_LDS, "lds");
DI void init_exchange(const Params& a, int part) {
    const rsrc_t rI = mk_rsrc(a.xr, X_HINT + 24576);
    constexpr int N16 = (X_HINT + 24576) / 16 / 8;
    static_assert((X_HINT + 24576) % 128 == 0, "eight equal parts");
    for (int i = threadIdx.x; i < N16; i += 256) {
        const int off = (part * N16 + i) * 16;
        const unsigned v = off < X_HINT ? ~0u : 0u;
        __builtin_amdgcn_raw_buffer_store_b128(v4u{v, v, v, v}, rI, off, 0, AUX_SC1);
    }
    asm volatile("s_waitcnt vmcnt(0)" ::: "memory");
    if (threadIdx.x < 64) {
        const int lane = lane_id();
        const rsrc_t rP = mk_rsrc(a.xr + X_BYTES + 8192, 65536);
        unsigned lat[2] = {~0u, ~0u};
        for (int it = 0; it < 8; ++it) {
#pragma unroll
            for (int p = 0; p < 2; ++p) {
                const int off = lane < 8 ? p * 8192 + part * 1024 + (it & 1) * 128 + lane * 16 : OOB;
                __builtin_amdgcn_raw_buffer_store_b128(v4u{0u, 0u, 0u, 0u}, rP, off, 0, AUX_SC1);
                asm volatile("s_waitcnt vmcnt(0)" ::: "memory");
                const unsigned long long t0 = cyc();
                const v4u z = __builtin_amdgcn_raw_buffer_load_b128(rP, off, 0, AUX_SC1);
                asm volatile("s_waitcnt vmcnt(0)" :: "v"(z) : "memory");
                const unsigned d = (unsigned)(cyc() - t0);
                lat[p] = d < lat[p] ? d : lat[p];
            }
        }
        if (lane == 0) (void)__hip_atomic_fetch_add(ctl_word(a, C_VOTE), lat[0] < lat[1] ? 1u : 0x10000u, RLX_AGENT);
        asm volatile("s_waitcnt vmcnt(0)" ::: "memory");
    }
    wg_signal(a, C_INIT);
}
DI void publish_par(const Params& a) {
    if (threadIdx.x < 64) {
        (void)wave_poll_ge(a, ctl_word(a, C_INIT), 8u);
        const unsigned v = (unsigned)__builtin_amdgcn_readfirstlane((int)__hip_atomic_load(ctl_word(a, C_VOTE), RLX_AGENT));
        if (lane_id() < 8) __hip_atomic_store(ctl_word(a, C_PAR + lane_id() * 32), 0x100u | ((v & 0xFFFFu) > (v >> 16) ? 1u : 0u), RLX_AGENT);
    }
}
__global__ void __launch_bounds__(256, 1) k_main(Params a) {
    extern __shared__ __attribute__((aligned(16))) unsigned char smem[];
    LAS unsigned char* lds = (LAS unsigned char*)smem;
    const int b = blockIdx.x;
    TM(0);
    if (b == 0 && threadIdx.x < 8) __hip_atomic_store(ctl_word(a, C_CFG + threadIdx.x * 32), 0x100u | ((unsigned)__builtin_amdgcn_s_getreg(20 | (3 << 11)) & 7u), RLX_AGENT);
    {
        const int j = b;
    if (j < 108) { task_mod(a, j, lds); if (j < 36) { wg_signal(a, C_MODA); if (threadIdx.x == 0) (void)__hip_atomic_fetch_add(ctl_word(a, C_MODB), 1u, RLX_AGENT); } else wg_signal(a, C_MODB); }
    }
    if (threadIdx.x == 0) { (void)poll_ge(a, ctl_word(a, C_CFG + (b & 7) * 32), 0x100u); *(LAS unsigned*)lds = __hip_atomic_load(ctl_word(a, C_CFG + (b & 7) * 32), RLX_AGENT); }
    __syncthreads();
    const unsigned cfg = (unsigned)__builtin_amdgcn_readfirstlane((int)*(LAS unsigned*)lds);
    __syncthreads();
    const int x0 = cfg & 7;
    const int l = ((x0 + (b & 7)) & 7) ^ (x0 & 4), r = b >> 3;
    if (l < 2) { if (r < 16) head_role(a, l * 16 + r, lds); else if (l == 0 && r < 24) { init_exchange(a, r - 16); if (r == 16) publish_par(a); } }
    else if (l < 4) { if (r < 18) y_role(a, r, l - 2, lds); }
    else worker_role(a, r * 4 + (l - 4), lds);
    tail_phase(a, l, r, lds);
    TR(5500, blockIdx.x);
}

#define LEAD_H 600
#define LEAD_Y 550
extern "C" void kernel_launch(void* const* d_in, const int* in_sizes, int n_in, void* d_out, int out_size, void* d_ws, size_t ws_size, hipStream_t stream) {
    unsigned char* ws = (unsigned char*)d_ws;
    unsigned char* xr = (unsigned char*)(((uintptr_t)ws + WS_END + (1u << 20) + (2u << 20) - 1) & ~(uintptr_t)((2u << 20) - 1));
    (void)hipMemsetAsync(ws + WS_CTL, 0, CTL_BYTES, stream);
    (void)hipFuncSetAttribute((const void*)k_main, hipFuncAttributeMaxDynamicSharedMemorySize, MAIN_LDS);
    Params p;
    p.x = (const u16*)d_in[0]; p.mem0 = (const u16*)d_in[1]; p.Wq = (const u16*)d_in[2]; p.Wk = (const u16*)d_in[3]; p.Wv = (const u16*)d_in[4]; p.Wo = (const u16*)d_in[5];
    p.Win = (const u16*)d_in[8]; p.c = (const u16*)d_in[12]; p.wmod = (const u16*)d_in[13];
    p.Wsp = (const u16*)d_in[9]; p.bsp = (const u16*)d_in[10]; p.Wout = (const u16*)d_in[11];
    p.out = (u16*)d_out;
    p.ws = ws; p.xr = xr;
    p.lead_h = LEAD_H; p.lead_y = LEAD_Y;
    k_main<<<256, 256, MAIN_LDS, stream>>>(p);
}
```
